```python
import torch

def _softmax(x, axis=-1):
    return torch.softmax(x, dim=axis)

def _gelu(x):
    return torch.nn.functional.gelu(x, approximate='tanh')

def _silu(x):
    return torch.nn.functional.silu(x)

def _sigmoid(x):
    return torch.sigmoid(x)

def _layernorm(x, axis=-1, eps=1e-05):
    m = x.mean(dim=axis, keepdim=True)
    v = x.var(dim=axis, keepdim=True, unbiased=False)
    return (x - m) / torch.sqrt(v + eps)

def _scatter_add(tgt, idx, src, axis=0):
    return tgt.clone().index_add_(axis, idx, src)

def _segment_sum(src, seg, n):
    out = torch.zeros((n,) + src.shape[1:], dtype=src.dtype)
    return out.index_add_(0, seg, src)

def _pad(x, pw):
    flat = tuple((v for pair in reversed(pw) for v in pair))
    return torch.nn.functional.pad(x, flat)
kernel_name = 'interleave_layers__memory_aug_recurrent_mem_tr__dense_activation_gelu__prompt'

def setup_inputs():

    _w = lambda a: (lambda t: t.to(torch.bfloat16) if t.is_floating_point() else t)(torch.as_tensor(a))

    def _A_setup():
        B, S, Dm = (1, 2048, 512)
        H, Dh = (32, 128)
        M, seg = (8, 32)
        seg = min(seg, S)
        S = S // seg * seg
        return {'x': _w((lambda *s: torch.randn(*s, dtype=torch.bfloat16) * 0.05)(B, S, Dm)), 'mem0': _w((lambda *s: torch.randn(*s, dtype=torch.bfloat16) * 0.2)(B, M, Dm)), 'Wq': _w((lambda *s: torch.randn(*s, dtype=torch.bfloat16) * 1.6)(Dm, H, Dh) * Dm ** (-0.5)), 'Wk': _w((lambda *s: torch.randn(*s, dtype=torch.bfloat16) * 1.6)(Dm, H, Dh) * Dm ** (-0.5)), 'Wv': _w((lambda *s: torch.randn(*s, dtype=torch.bfloat16) * 0.2)(Dm, H, Dh) * Dm ** (-0.5)), 'Wo': _w((lambda *s: torch.randn(*s, dtype=torch.bfloat16) * 0.2)(H, Dh, Dm) * (H * Dh) ** (-0.5)), 'seg_len': seg}
    _A_in = _A_setup()

    def _B_setup():
        B, S, D, F = (1, 2048, 512, 1376)
        return {'x': _w((lambda *s: torch.randn(*s, dtype=torch.bfloat16) * 0.05)(B, S, D)), 'w1': _w((lambda *s: torch.randn(*s, dtype=torch.bfloat16) * 0.4)(D, F) * D ** (-0.5)), 'b1': _w((lambda *s: torch.randn(*s, dtype=torch.bfloat16) * 12.8)(F) * 0.01), 'w2': _w((lambda *s: torch.randn(*s, dtype=torch.bfloat16) * 0.1)(F, D) * F ** (-0.5)), 'b2': _w((lambda *s: torch.randn(*s, dtype=torch.bfloat16) * 0.8)(D) * 0.01)}
    _B_in = _B_setup()
    return {'A_x': _A_in['x'], 'A_mem0': _A_in['mem0'], 'A_Wq': _A_in['Wq'], 'A_Wk': _A_in['Wk'], 'A_Wv': _A_in['Wv'], 'A_Wo': _A_in['Wo'], 'A_seg_len': _A_in['seg_len'], 'B_x': _B_in['x'], 'B_w1': _B_in['w1'], 'B_b1': _B_in['b1'], 'B_w2': _B_in['w2'], 'B_b2': _B_in['b2']}

def reference(A_x, A_mem0, A_Wq, A_Wk, A_Wv, A_Wo, A_seg_len, B_x, B_w1, B_b1, B_w2, B_b2):

    def _A_ref(x, mem0, Wq, Wk, Wv, Wo, seg_len):
        B, S, Dm = x.shape
        H, Dh = (Wq.shape[1], Wq.shape[2])
        M = mem0.shape[1]
        scale = Dh ** (-0.5)
        n_seg = S // seg_len
        L = M + seg_len
        idx = torch.arange(L)
        mask = (idx[:, None] < M) | (idx[:, None] >= idx[None, :])
        mem = mem0
        outs = []
        for s in range(n_seg):
            xs = x[:, s * seg_len:(s + 1) * seg_len, :].clone()
            h = torch.concatenate([mem, xs], axis=1)
            q = torch.einsum('btd,dhe->bhte', h, Wq)
            k = torch.einsum('btd,dhe->bhte', h, Wk)
            v = torch.einsum('btd,dhe->bhte', h, Wv)
            scores = torch.einsum('bhqe,bhke->bhqk', q, k) * scale
            scores = torch.where(mask[None, None], scores, -1000000000.0)
            p = _softmax(scores, axis=-1)
            o = torch.einsum('bhqk,bhke->bhqe', p, v)
            o = torch.einsum('bhte,hed->btd', o, Wo)
            mem = o[:, :M, :].clone()
            outs.append(o[:, M:, :])
        return torch.concatenate(outs, axis=1)

    def _B_ref(x, w1, b1, w2, b2):
        h = torch.einsum('bsd,df->bsf', x, w1) + b1
        h = _gelu(h)
        return torch.einsum('bsf,fd->bsd', h, w2) + b2
    x = A_x
    x = x + _A_ref(_layernorm(x, axis=-1), A_mem0, A_Wq, A_Wk, A_Wv, A_Wo, A_seg_len)
    x = x + _B_ref(_layernorm(x, axis=-1), B_w1, B_b1, B_w2, B_b2)
    x = x + _A_ref(_layernorm(x, axis=-1), A_mem0, A_Wq, A_Wk, A_Wv, A_Wo, A_seg_len)
    x = x + _B_ref(_layernorm(x, axis=-1), B_w1, B_b1, B_w2, B_b2)
    return x
```

```cpp
#include <hip/hip_runtime.h>
#include <hip/hip_bf16.h>
#include <stdint.h>

typedef unsigned short u16;
typedef short bf16x8 __attribute__((ext_vector_type(8)));
typedef float f32x4 __attribute__((ext_vector_type(4)));

constexpr int S_ = 2048, D_ = 512, H_ = 32, E_ = 128, M_ = 8, SEG = 32, NSEG = 64, F_ = 1376, HE = 4096;

__device__ __forceinline__ float bf2f(u16 b) { return __uint_as_float(((unsigned)b) << 16); }
__device__ __forceinline__ u16 f2bf(float f) { __hip_bfloat16 h = __float2bfloat16(f); return *reinterpret_cast<u16*>(&h); }

__global__ void ln_kernel(const float* __restrict__ x, u16* __restrict__ out, int rows) {
    int row = blockIdx.x * 4 + (threadIdx.x >> 6), lane = threadIdx.x & 63;
    if (row >= rows) return;
    float v[8]; float s = 0;
    for (int j = 0; j < 8; ++j) { v[j] = x[(size_t)row * D_ + lane * 8 + j]; s += v[j]; }
    for (int m = 32; m >= 1; m >>= 1) s += __shfl_xor(s, m);
    float mean = s * (1.0f / D_); float q = 0;
    for (int j = 0; j < 8; ++j) { float d = v[j] - mean; q += d * d; }
    for (int m = 32; m >= 1; m >>= 1) q += __shfl_xor(q, m);
    float rs = 1.0f / sqrtf(q * (1.0f / D_) + 1e-5f);
    for (int j = 0; j < 8; ++j) out[(size_t)row * D_ + lane * 8 + j] = f2bf((v[j] - mean) * rs);
}
__global__ void cvt_in(const u16* __restrict__ x, float* __restrict__ o, int n) {
    int i = blockIdx.x * 256 + threadIdx.x; if (i < n) o[i] = bf2f(x[i]);
}
__global__ void cvt_out(const float* __restrict__ x, u16* __restrict__ o, int n) {
    int i = blockIdx.x * 256 + threadIdx.x; if (i < n) o[i] = f2bf(x[i]);
}

template <int MODE>
__global__ void __launch_bounds__(256) gemm_kernel(const u16* __restrict__ A, int lda, const u16* __restrict__ B, int ldb,
                                                   u16* __restrict__ Cb, float* __restrict__ X, int ldc, const u16* __restrict__ bias,
                                                   int M, int N, int K) {
    __shared__ u16 As[64][40];
    __shared__ u16 Bs[64][40];
    int tid = threadIdx.x, lane = tid & 63, w = tid >> 6;
    int m0 = blockIdx.y * 64, n0 = blockIdx.x * 64;
    int wm = (w >> 1) * 32, wn = (w & 1) * 32;
    f32x4 acc[2][2] = {};
    for (int k0 = 0; k0 < K; k0 += 32) {
        for (int i = tid; i < 64 * 32; i += 256) {
            int r = i >> 5, c = i & 31;
            As[r][c] = (m0 + r < M && k0 + c < K) ? A[(size_t)(m0 + r) * lda + k0 + c] : (u16)0;
        }
        for (int i = tid; i < 64 * 32; i += 256) {
            int k = i >> 6, n = i & 63;
            Bs[n][k] = (n0 + n < N && k0 + k < K) ? B[(size_t)(k0 + k) * ldb + n0 + n] : (u16)0;
        }
        __syncthreads();
        bf16x8 a[2], b[2];
        for (int i = 0; i < 2; ++i) {
            for (int j = 0; j < 8; ++j) {
                a[i][j] = (short)As[wm + i * 16 + (lane & 15)][(lane >> 4) * 8 + j];
                b[i][j] = (short)Bs[wn + i * 16 + (lane & 15)][(lane >> 4) * 8 + j];
            }
        }
        for (int i = 0; i < 2; ++i) for (int j = 0; j < 2; ++j)
            acc[i][j] = __builtin_amdgcn_mfma_f32_16x16x32_bf16(a[i], b[j], acc[i][j], 0, 0, 0);
        __syncthreads();
    }
    for (int i = 0; i < 2; ++i) for (int j = 0; j < 2; ++j) for (int r = 0; r < 4; ++r) {
        int m = m0 + wm + i * 16 + (lane >> 4) * 4 + r, n = n0 + wn + j * 16 + (lane & 15);
        if (m >= M || n >= N) continue;
        float v = acc[i][j][r];
        if (MODE == 0) Cb[(size_t)m * ldc + n] = f2bf(v);
        else if (MODE == 1) {
            v += bf2f(bias[n]);
            float u = 0.7978845608028654f * (v + 0.044715f * v * v * v);
            float t = tanhf(u);
            Cb[(size_t)m * ldc + n] = f2bf(0.5f * v * (1.0f + t));
        } else {
            if (bias) v += bf2f(bias[n]);
            X[(size_t)m * ldc + n] += v;
        }
    }
}

__global__ void __launch_bounds__(384) chain_a(const u16* __restrict__ mem, const u16* __restrict__ Wq, const u16* __restrict__ Wk,
                                               const u16* __restrict__ Wv, const u16* __restrict__ QKV, u16* __restrict__ KM,
                                               u16* __restrict__ VM, u16* __restrict__ OM, int s) {
    __shared__ float ms[8][512];
    __shared__ float qkv[3][8][128];
    __shared__ float sc[8][40];
    int h = blockIdx.x, tid = threadIdx.x;
    for (int i = tid; i < 8 * 512; i += 384) ms[i >> 9][i & 511] = bf2f(mem[i]);
    __syncthreads();
    {
        int which = tid >> 7, e = tid & 127;
        const u16* W = which == 0 ? Wq : (which == 1 ? Wk : Wv);
        float acc[8] = {};
        for (int d = 0; d < 512; ++d) {
            float wv = bf2f(W[(size_t)d * HE + h * 128 + e]);
            for (int r = 0; r < 8; ++r) acc[r] += ms[r][d] * wv;
        }
        for (int r = 0; r < 8; ++r) {
            u16 b = f2bf(acc[r]); qkv[which][r][e] = bf2f(b);
            if (which == 1) KM[((size_t)s * 8 + r) * HE + h * 128 + e] = b;
            if (which == 2) VM[((size_t)s * 8 + r) * HE + h * 128 + e] = b;
        }
    }
    __syncthreads();
    if (tid < 320) {
        int r = tid / 40, c = tid % 40; float a = 0;
        for (int e = 0; e < 128; ++e) {
            float kv = c < 8 ? qkv[1][c][e] : bf2f(QKV[(size_t)(s * 32 + c - 8) * 3 * HE + HE + h * 128 + e]);
            a += qkv[0][r][e] * kv;
        }
        sc[r][c] = a * 0.08838834764831845f;
    }
    __syncthreads();
    if (tid < 8) {
        float mx = -1e30f; for (int c = 0; c < 40; ++c) mx = fmaxf(mx, sc[tid][c]);
        float sum = 0; for (int c = 0; c < 40; ++c) { float p = expf(sc[tid][c] - mx); sc[tid][c] = p; sum += p; }
        for (int c = 0; c < 40; ++c) sc[tid][c] /= sum;
    }
    __syncthreads();
    for (int i = tid; i < 8 * 128; i += 384) {
        int r = i >> 7, e = i & 127; float a = 0;
        for (int c = 0; c < 40; ++c) {
            float vv = c < 8 ? qkv[2][c][e] : bf2f(QKV[(size_t)(s * 32 + c - 8) * 3 * HE + 2 * HE + h * 128 + e]);
            a += sc[r][c] * vv;
        }
        OM[(size_t)r * HE + h * 128 + e] = f2bf(a);
    }
}
__global__ void __launch_bounds__(256) chain_b(const u16* __restrict__ OM, const u16* __restrict__ Wo, u16* __restrict__ mem) {
    __shared__ float red[32][8][8];
    int c = threadIdx.x & 7, kg = threadIdx.x >> 3, col = blockIdx.x * 8 + c;
    float acc[8] = {};
    for (int k = kg * 128; k < kg * 128 + 128; ++k) {
        float wv = bf2f(Wo[(size_t)k * D_ + col]);
        for (int r = 0; r < 8; ++r) acc[r] += bf2f(OM[(size_t)r * HE + k]) * wv;
    }
    for (int r = 0; r < 8; ++r) red[kg][r][c] = acc[r];
    __syncthreads();
    if (threadIdx.x < 64) {
        int r = threadIdx.x >> 3; float a = 0;
        for (int g = 0; g < 32; ++g) a += red[g][r][c];
        mem[r * D_ + col] = f2bf(a);
    }
}
__global__ void __launch_bounds__(128) attn_x(const u16* __restrict__ QKV, const u16* __restrict__ KM, const u16* __restrict__ VM, u16* __restrict__ O) {
    __shared__ float q[32][129], k[40][129], v[40][128], sc[32][41];
    int s = blockIdx.x, h = blockIdx.y, tid = threadIdx.x;
    for (int i = tid; i < 32 * 128; i += 128) {
        int r = i >> 7, e = i & 127; size_t base = (size_t)(s * 32 + r) * 3 * HE + h * 128 + e;
        q[r][e] = bf2f(QKV[base]); k[8 + r][e] = bf2f(QKV[base + HE]); v[8 + r][e] = bf2f(QKV[base + 2 * HE]);
    }
    for (int i = tid; i < 8 * 128; i += 128) {
        int r = i >> 7, e = i & 127; size_t base = ((size_t)s * 8 + r) * HE + h * 128 + e;
        k[r][e] = bf2f(KM[base]); v[r][e] = bf2f(VM[base]);
    }
    __syncthreads();
    for (int i = tid; i < 32 * 40; i += 128) {
        int r = i / 40, c = i % 40; float a = 0;
        for (int e = 0; e < 128; ++e) a += q[r][e] * k[c][e];
        sc[r][c] = (c <= r + 8) ? a * 0.08838834764831845f : -1e9f;
    }
    __syncthreads();
    if (tid < 32) {
        float mx = -1e30f; for (int c = 0; c < 40; ++c) mx = fmaxf(mx, sc[tid][c]);
        float sum = 0; for (int c = 0; c < 40; ++c) { float p = expf(sc[tid][c] - mx); sc[tid][c] = p; sum += p; }
        for (int c = 0; c < 40; ++c) sc[tid][c] /= sum;
    }
    __syncthreads();
    for (int i = tid; i < 32 * 128; i += 128) {
        int r = i >> 7, e = i & 127; float a = 0;
        for (int c = 0; c < 40; ++c) a += sc[r][c] * v[c][e];
        O[(size_t)(s * 32 + r) * HE + h * 128 + e] = f2bf(a);
    }
}

extern "C" void kernel_launch(void* const* d_in, const int* in_sizes, int n_in, void* d_out, int out_size, void* d_ws, size_t ws_size,
                              hipStream_t stream) {
    const u16* x = (const u16*)d_in[0]; const u16* mem0 = (const u16*)d_in[1];
    const u16* Wq = (const u16*)d_in[2]; const u16* Wk = (const u16*)d_in[3]; const u16* Wv = (const u16*)d_in[4]; const u16* Wo = (const u16*)d_in[5];
    const u16* w1 = (const u16*)d_in[8]; const u16* b1 = (const u16*)d_in[9]; const u16* w2 = (const u16*)d_in[10]; const u16* b2 = (const u16*)d_in[11];
    char* ws = (char*)d_ws; size_t off = 0;
    auto alloc = [&](size_t bytes) { void* p = ws + off; off += (bytes + 255) & ~(size_t)255; return p; };
    float* X = (float*)alloc((size_t)S_ * D_ * 4);
    u16* XN = (u16*)alloc((size_t)S_ * D_ * 2);
    u16* QKV = (u16*)alloc((size_t)S_ * 3 * HE * 2);
    u16* O = (u16*)alloc((size_t)S_ * HE * 2);
    u16* KM = (u16*)alloc((size_t)NSEG * 8 * HE * 2);
    u16* VM = (u16*)alloc((size_t)NSEG * 8 * HE * 2);
    u16* OM = (u16*)alloc((size_t)8 * HE * 2);
    u16* MEM = (u16*)alloc((size_t)8 * D_ * 2);
    u16* Hb = (u16*)alloc((size_t)S_ * F_ * 2);
    cvt_in<<<S_ * D_ / 256, 256, 0, stream>>>(x, X, S_ * D_);
    for (int layer = 0; layer < 2; ++layer) {
        ln_kernel<<<S_ / 4, 256, 0, stream>>>(X, XN, S_);
        gemm_kernel<0><<<dim3(HE / 64, S_ / 64), 256, 0, stream>>>(XN, D_, Wq, HE, QKV, nullptr, 3 * HE, nullptr, S_, HE, D_);
        gemm_kernel<0><<<dim3(HE / 64, S_ / 64), 256, 0, stream>>>(XN, D_, Wk, HE, QKV + HE, nullptr, 3 * HE, nullptr, S_, HE, D_);
        gemm_kernel<0><<<dim3(HE / 64, S_ / 64), 256, 0, stream>>>(XN, D_, Wv, HE, QKV + 2 * HE, nullptr, 3 * HE, nullptr, S_, HE, D_);
        for (int s = 0; s < NSEG; ++s) {
            chain_a<<<32, 384, 0, stream>>>(s == 0 ? mem0 : MEM, Wq, Wk, Wv, QKV, KM, VM, OM, s);
            if (s + 1 < NSEG) chain_b<<<64, 256, 0, stream>>>(OM, Wo, MEM);
        }
        attn_x<<<dim3(NSEG, H_), 128, 0, stream>>>(QKV, KM, VM, O);
        gemm_kernel<2><<<dim3(D_ / 64, S_ / 64), 256, 0, stream>>>(O, HE, Wo, D_, nullptr, X, D_, nullptr, S_, D_, HE);
        ln_kernel<<<S_ / 4, 256, 0, stream>>>(X, XN, S_);
        gemm_kernel<1><<<dim3((F_ + 63) / 64, S_ / 64), 256, 0, stream>>>(XN, D_, w1, F_, Hb, nullptr, F_, b1, S_, F_, D_);
        gemm_kernel<2><<<dim3(D_ / 64, S_ / 64), 256, 0, stream>>>(Hb, F_, w2, D_, nullptr, X, D_, b2, S_, D_, F_);
    }
    cvt_out<<<S_ * D_ / 256, 256, 0, stream>>>(X, (u16*)d_out, S_ * D_);
}
```

```cpp
#include <hip/hip_runtime.h>
#include <hip/hip_bf16.h>
#include <stdint.h>

namespace mk {
typedef unsigned short u16;
typedef __bf16 bf16x8 __attribute__((ext_vector_type(8)));
typedef __bf16 bf16x16 __attribute__((ext_vector_type(16)));
typedef __bf16 bf16x2 __attribute__((ext_vector_type(2)));
typedef float f32x2 __attribute__((ext_vector_type(2)));
typedef float f32x4 __attribute__((ext_vector_type(4)));
typedef unsigned v2u __attribute__((ext_vector_type(2)));
typedef unsigned v4u __attribute__((ext_vector_type(4)));
typedef unsigned v8u __attribute__((ext_vector_type(8)));
typedef __amdgpu_buffer_rsrc_t rsrc_t;
#define DI __device__ __forceinline__
#define LDSP __attribute__((address_space(3)))

constexpr int NSEG = 64, NF = 11, FDIM = 1376;
constexpr unsigned O_XNA = 0u;
constexpr unsigned O_XNM = 4194304u;
constexpr unsigned O_X1 = 8388608u;
constexpr unsigned O_X2 = 16777216u;
constexpr unsigned O_MP = 25165824u;
constexpr unsigned O_TS = 29360128u;
constexpr unsigned O_FLG = 30408704u;
constexpr unsigned O_MEMB = 33554432u;
constexpr unsigned O_OX = 41943040u;
constexpr unsigned O_WOP = 75497472u;
constexpr unsigned O_MLPP = 109051904u;
constexpr unsigned O_DBG = 176160768u;
constexpr unsigned WS_NEED = 184549376u;
constexpr unsigned O_FO = 30408704u;
constexpr unsigned O_FWO = 30441472u;
constexpr unsigned O_FMP = 30445568u;
constexpr unsigned O_FXM = 30457856u;
constexpr unsigned O_FXA = 30474240u;
constexpr unsigned O_FE = 30459776u;
constexpr unsigned O_HC = 30670848u;
constexpr unsigned O_HR = 30674944u;
constexpr unsigned O_HE = 30679040u;
constexpr unsigned O_HD = 30687232u;
constexpr unsigned O_PM = 30736384u;
constexpr int MR_DELAY = 20;
constexpr int FLAG_SLEEP = 2;
constexpr int PFG = 64, PFS = 0, PFM = 6;
#define PFNT ""
constexpr int FS0 = 1;
constexpr int TSU = 5, TSH = 2;
constexpr int MED = 25;
#define DMB 1
constexpr int MRX = 1;
constexpr int M1D = 10;
constexpr int MUNI = 1;
constexpr int CPF = 1;
constexpr int TLP = 2, LNS = 40, LNB = 8;
constexpr int MPN = 0;
constexpr int DIS = 0;
constexpr int WOM = 0;
constexpr int REP = 8;
constexpr unsigned MEMB_STRIDE = 131072, PSEL0 = 0, PSEL1 = 1;
constexpr int NPM = 4;
constexpr unsigned SENT = 0xFFFFFFFFu;
constexpr int SC1 = 16;
constexpr unsigned OOB = 0x7ff00000u;
constexpr unsigned SPIN_LIMIT = 1u << 17;

DI unsigned pk(float a, float b) { return __builtin_bit_cast(unsigned, __builtin_convertvector((f32x2){a, b}, bf16x2)); }
DI f32x4 mfma32(v4u a, v4u b, f32x4 c) { return __builtin_amdgcn_mfma_f32_16x16x32_bf16(__builtin_bit_cast(bf16x8, a), __builtin_bit_cast(bf16x8, b), c, 0, 0, 0); }
DI f32x4 smf(v4u a, v8u b, f32x4 c, int idx) { return __builtin_amdgcn_smfmac_f32_16x16x64_bf16(__builtin_bit_cast(bf16x8, a), __builtin_bit_cast(bf16x16, b), c, idx, 0, 0); }
DI v8u cat(v4u a, v4u b) { return __builtin_shufflevector(a, b, 0, 1, 2, 3, 4, 5, 6, 7); }
DI v4u pack8(f32x4 a, f32x4 b) { return (v4u){pk(a[0], a[1]), pk(a[2], a[3]), pk(b[0], b[1]), pk(b[2], b[3])}; }
DI v2u pack4(f32x4 a) { return (v2u){pk(a[0], a[1]), pk(a[2], a[3])}; }
DI void wg_barrier() { asm volatile("s_waitcnt lgkmcnt(0)\n\ts_barrier" ::: "memory"); }
DI void drain_vm() { asm volatile("s_waitcnt vmcnt(0)" ::: "memory"); }
DI v4u ld16(rsrc_t rs, unsigned off, unsigned so = 0) { return __builtin_amdgcn_raw_buffer_load_b128(rs, off, so, 0); }
DI v4u ld16c(rsrc_t rs, unsigned off, unsigned so = 0) { return __builtin_amdgcn_raw_buffer_load_b128(rs, off, so, SC1); }
DI unsigned ld4c(rsrc_t rs, unsigned off, unsigned so = 0) { return __builtin_amdgcn_raw_buffer_load_b32(rs, off, so, SC1); }
DI void st16c(rsrc_t rs, unsigned off, v4u v, unsigned so = 0) { __builtin_amdgcn_raw_buffer_store_b128(v, rs, off, so, SC1); }
DI void st4c(rsrc_t rs, unsigned off, unsigned v, unsigned so = 0) { __builtin_amdgcn_raw_buffer_store_b32(v, rs, off, so, SC1); }
DI unsigned umin(unsigned a, unsigned b) { return a < b ? a : b; }
DI unsigned umax(unsigned a, unsigned b) { return a > b ? a : b; }
DI unsigned max4(v4u v) { return umax(umax(v[0], v[1]), umax(v[2], v[3])); }
DI v4u lds_rd16(LDSP unsigned char* p) { return *(LDSP v4u*)p; }
DI void lds_wr16(LDSP unsigned char* p, v4u v) { *(LDSP v4u*)p = v; }

DI float red_g_sum(float x) {
    auto a = __builtin_amdgcn_permlane16_swap(__float_as_uint(x), __float_as_uint(x), false, false);
    x = __uint_as_float(a[0]) + __uint_as_float(a[1]);
    auto b = __builtin_amdgcn_permlane32_swap(__float_as_uint(x), __float_as_uint(x), false, false);
    return __uint_as_float(b[0]) + __uint_as_float(b[1]);
}
template <int CTRL> DI float dpp(float x) { return __uint_as_float(__builtin_amdgcn_update_dpp(0, __float_as_uint(x), CTRL, 0xf, 0xf, true)); }
DI float wave_sum(float x) {
    x += dpp<0xB1>(x);
    x += dpp<0x4E>(x);
    x += dpp<0x141>(x);
    x += dpp<0x140>(x);
    return red_g_sum(x);
}

DI void ln8(v4u (&v)[8]) {
    float f[64], s = 0, q = 0;
#pragma unroll
    for (int k = 0; k < 32; ++k) { f[2 * k] = __uint_as_float(v[k >> 2][k & 3] << 16); f[2 * k + 1] = __uint_as_float(v[k >> 2][k & 3] & 0xffff0000u); s += f[2 * k] + f[2 * k + 1]; }
    s += dpp<0xB1>(s); s += dpp<0x4E>(s); s += dpp<0x141>(s);
    s *= 1.0f / 512;
#pragma unroll
    for (int k = 0; k < 64; ++k) { f[k] -= s; q += f[k] * f[k]; }
    q += dpp<0xB1>(q); q += dpp<0x4E>(q); q += dpp<0x141>(q);
    q = 1.0f / sqrtf(q * (1.0f / 512) + 1e-5f);
#pragma unroll
    for (int k = 0; k < 32; ++k) v[k >> 2][k & 3] = pk(f[2 * k] * q, f[2 * k + 1] * q);
}
template <int N> DI void nap() { asm volatile("s_sleep %0" ::"n"(N) : "memory"); }
DI unsigned ldu(rsrc_t rs, unsigned off) { return __builtin_amdgcn_readfirstlane(ld4c(rs, 0, off)); }
DI unsigned gate(rsrc_t rs, LDSP unsigned* lds, int id, unsigned gc, int helper, int w, int lane) {
    unsigned e = ldu(rs, O_HE + helper * 4);
    drain_vm();
    wg_barrier();
    if (w == 0)
        for (unsigned n = 0; n < SPIN_LIMIT; ++n) {
            st4c(rs, lane ? OOB : 0, e, O_HD + helper * 128);
            const unsigned e2 = ld4c(rs, 0, O_HE + helper * 4), g = ld4c(rs, 0, O_HR + id * 4);
            e = __builtin_amdgcn_readfirstlane(e2);
            *lds = g;
            if ((__builtin_amdgcn_readfirstlane(g) ^ gc) < 2) break;
            nap<4>();
        }
    wg_barrier();
    return (__builtin_amdgcn_readfirstlane(*lds) & 1) << 13;
}
DI void arm(rsrc_t rs, const u16* __restrict__ mem0, int q, int lane) {
    const v4u sv = {SENT, SENT, SENT, SENT};
    v4u o;
    const int ch = (0x78 >> ((lane ^ lane >> 4) & 3) * 2) & 3;
    for (int i = 0; i < 4; ++i) o[i] = *(const unsigned*)(mem0 + (lane >> 4) * 1024 + (lane & 4) * 128 + 64 * (q & 7) + 16 * ch + 4 * i + (lane >> 2 & 2));
    for (int k = 0; k < 2; ++k) st16c(rs, lane * 16, (v4u){0, 0, 0, 0}, O_FLG + (q + k * 64) * 1024);
    for (int u = q; u < 2304 + (1024 >> TSH); u += 64) st16c(rs, lane * 16, sv, u < 2048 ? O_MP + u * 1024 : (u < 2304 ? O_MEMB + (u - 1920) * 1024 : O_TS + (u - 2304 + (u - 2304 & ~15)) * 1024));
    for (int i = 0; i < 4; ++i) o[i] = umin(o[i], SENT - 1);
    for (int k = 0; k < 2; ++k) st16c(rs, lane * 16, o, O_MEMB + (q + k * 64) * 1024);
}
DI void ln_role(rsrc_t rs, LDSP unsigned* lds, const u16* __restrict__ x, int cu, int w, int lane, int id, unsigned g0) {
    const int q = cu * 4 + w;
    const unsigned mb = O_MEMB + q * 4096 + lane * 16, gc = (g0 + 2) & ~1u;
    unsigned lp = 0;
#pragma unroll 1
    for (int s0 = 0; s0 < 64; s0 += 8) {
        const unsigned ro = ((s0 + (lane >> 3)) * 32 + q) * 1024 + (lane & 7) * 16;
        v4u v[8];
#pragma unroll
        for (int k = 0; k < 8; ++k) v[k] = *(const v4u*)((const unsigned char*)x + ro + k * 128);
        if (s0 == 0) st4c(rs, lane ? OOB : 0, gc, O_HC + id * 4);
#pragma unroll 1
        for (int j = s0 ? s0 + 1 : 3; j <= s0 + 8 && j < 64; ++j)
        {
            for (int k = 0; k < 4; ++k) st16c(rs, mb + k * 1024, (v4u){SENT, SENT, SENT, SENT}, j * MEMB_STRIDE);
            if (LNS && s0 >= LNB) nap<LNS>();
        }
        ln8(v);
        if (s0 == 0) lp = gate(rs, lds, id, __builtin_amdgcn_readfirstlane(gc), 32 + cu, w, lane);
#pragma unroll
        for (int k = 0; k < 8; ++k) st16c(rs, (s0 + (lane >> 3)) * 65536 + (lane & 7) * 16 + k * 128, v[k], O_XNA + (cu & 3) * 16384 + (w * 2 + (cu >> 2)) * 1024 + (lp ^ (s0 & 32) << 8));
        drain_vm();
        wg_barrier();
        if (w == 0) st4c(rs, (lane >> 5) * 32768 + (lane & 31) * 128, (s0 + 8) << 24, O_FXA + lp + (cu >> 2) * 16384 + (cu & 2) * 2048 + (cu & 1) * 16);
    }
}
DI void master_role(rsrc_t rs, LDSP unsigned* lds, int w, int lane) {
    const unsigned da = O_HD + (lane & 15) * 128;
    if (w == 0) {
        const unsigned e0 = ld4c(rs, da) + 1;
        st4c(rs, lane < 16 ? lane * 4 : OOB, e0, O_HE);
        lds[lane] = e0;
    }
    unsigned best[2] = {~0u, ~0u};
#pragma unroll 1
    for (int n = 0; n < NPM; ++n)
#pragma unroll
        for (int k = 0; k < 2; ++k) {
            unsigned long long t0, t1;
            asm volatile("s_waitcnt vmcnt(0)\n\ts_memtime %0\n\ts_waitcnt lgkmcnt(0)\n\tbuffer_store_dword %2, off, %3, %4 sc1\n\ts_waitcnt vmcnt(0)\n\ts_memtime %1\n\ts_waitcnt lgkmcnt(0)"
                         : "=&s"(t0), "=&s"(t1) : "v"(0), "s"(rs), "s"(O_PM + k * 8192 + (w & 1) * 4096 + (w >> 1) * 16384 + n * 256) : "memory");
            best[k] = umin(best[k], (unsigned)(t1 - t0));
        }
    lds[64 + w] = best[0];
    lds[68 + w] = best[1];
    wg_barrier();
    const unsigned e = lds[lane & 15];
    const unsigned sd = umin(umin(lds[64], lds[65]), umin(lds[66], lds[67])) < umin(umin(lds[68], lds[69]), umin(lds[70], lds[71])) ? PSEL1 : PSEL0;
    v4u last = {0, 0, 0, 0};
    bool on = false;
    for (unsigned n = 0; n < (DIS ? 200 : SPIN_LIMIT); ++n) {
        v4u c = ld16c(rs, w * 1024 + lane * 16, O_HC);
        const unsigned d = ld4c(rs, on ? O_FE + (lane & 7) * 4 : da);
        for (int i = 0; i < 4; ++i) c[i] = (c[i] & ~1u) | sd;
        if (!on) {
            if (__builtin_amdgcn_ballot_w64(d != e)) { nap<1>(); continue; }
            st16c(rs, w * 1024 + lane * 16, c, O_HR);
            on = true;
        } else {
            if (c[0] != last[0] || c[1] != last[1] || c[2] != last[2] || c[3] != last[3]) st16c(rs, w * 1024 + lane * 16, c, O_HR);
            if (!__builtin_amdgcn_ballot_w64(d < (3u << 24))) break;
            nap<16>();
        }
        last = c;
    }
}

DI void head_role(unsigned char* ws, const u16* Wq, const u16* Wk, const u16* Wv, const u16* Wo, int id, unsigned gc) {
    asm volatile(
        "s_mov_b64 s[40:41], %[pw]\n\t"
        "s_mov_b32 s42, 184549376\n\t"
        "s_mov_b32 s43, 0x20000\n\t"
        "s_lshr_b32 s44, %[id], 7\n\t"
        "s_and_b32 s45, %[id], 31\n\t"
        "s_bfe_u32 s46, %[id], 0x10005\n\t"
        "s_bfe_u32 s47, %[id], 0x10006\n\t"
        "s_lshl_b32 s78, %[id], 2\n\t"
        "s_add_u32 s78, s78, 30674944\n\t"
        "s_mov_b32 s79, %[gc]\n\t"
        "s_mov_b64 s[80:81], %[pq]\n\t"
        "s_mov_b32 s82, 4194304\n\t"
        "s_mov_b32 s83, 0x20000\n\t"
        "s_mov_b64 s[84:85], %[pk]\n\t"
        "s_mov_b32 s86, 4194304\n\t"
        "s_mov_b32 s87, 0x20000\n\t"
        "s_mov_b64 s[88:89], %[pv]\n\t"
        "s_mov_b32 s90, 4194304\n\t"
        "s_mov_b32 s91, 0x20000\n\t"
        "s_mov_b64 s[92:93], %[po]\n\t"
        "s_mov_b32 s94, 4194304\n\t"
        "s_mov_b32 s95, 0x20000\n\t"
        "s_mov_b32 s53, 131072\n\t"
        "s_mov_b32 s55, 0x3e0293ee\n\t"
        "s_sub_u32 s56, s46, 1\n\t"
        "s_mov_b32 s57, 0xceabfb8f\n\t"
        "s_mov_b32 s18, 269\n\t"
        "s_mov_b32 s17, 15600\n\t"
        "s_getpc_b64 s[60:61]\n\t"
        "Lgoa_%=:\n\t"
        "s_add_u32 s60, s60, Lgo_%=-Lgoa_%=\n\t"
        "s_addc_u32 s61, s61, 0\n\t"
        "s_mov_b32 s19, 220\n\t"
        "s_mov_b32 s20, 0\n\t"
        "s_mov_b32 s32, 0\n\t"
        "s_mov_b32 s33, 0\n\t"
        "s_mul_i32 s29, s46, 8\n\t"
        "s_add_u32 s19, s19, s29\n\t"
        "s_mov_b32 s29, 0\n\t"
        "s_mul_i32 s38, s45, 16388\n\t"
        "s_and_b32 s38, s38, 0xf007c\n\t"
        "s_add_u32 s38, s38, 29360128\n\t"
        "s_mov_b32 s96, 0x3f803f80\n\t"
        "s_mov_b32 s98, 0x3f003f00\n\t"
        "s_mov_b32 s97, 0x3f803f80\n\t"
        "s_mov_b32 s99, 0x3f003f00\n\t"
        "s_mov_b32 s21, 0x53800000\n\t"
        "s_lshl_b32 s48, s46, 6\n\t"
        "s_add_u32 s48, s48, s47\n\t"
        "s_min_u32 s13, s44, 1\n\t"
        "s_lshl_b32 s13, s13, 12\n\t"
        "s_lshr_b32 s71, s44, 1\n\t"
        "s_lshl_b32 s71, s71, 12\n\t"
        "s_lshl_b32 s35, s44, 10\n\t"
        "s_add_u32 s13, s44, 1\n\t"
        "s_and_b32 s13, s13, 2\n\t"
        "s_lshl_b32 s13, s13, 11\n\t"
        "s_lshl_b32 s71, s44, 13\n\t"
        "s_sub_u32 s71, s71, 12288\n\t"
        "s_and_b32 s49, s44, 1\n\t"
        "s_mul_i32 s71, s71, s49\n\t"
        "s_lshl_b32 s12, s46, 13\n\t"
        "s_lshl4_add_u32 s31, s35, s12\n\t"
        "v_mbcnt_lo_u32_b32 v10, -1, 0\n\t"
        "v_mbcnt_hi_u32_b32 v10, -1, v10\n\t"
        "v_lshlrev_b32 v0, 4, v10\n\t"
        "s_lshl_b32 s74, s47, 16\n\t"
        "s_add_u32 s74, s74, s31\n\t"
        "s_lshr_b32 s49, s74, 8\n\t"
        "s_and_b32 s49, s49, 0x2000\n\t"
        "s_xor_b32 s74, s74, s49\n\t"
        "s_lshl_b32 s49, s44, 15\n\t"
        "s_add_u32 s49, s49, 32768\n\t"
        "s_lshl_b32 s59, s45, 17\n\t"
        "s_lshl_b32 s51, s44, 8\n\t"
        "s_add_u32 s59, s59, s51\n\t"
        "s_lshl_b32 s51, s44, 15\n\t"
        "s_add_u32 s59, s59, s51\n\t"
        "v_and_b32 v22, 16, v10\n\t"
        "v_lshrrev_b32 v13, 5, v10\n\t"
        "v_lshl_add_u32 v22, v13, 2, v22\n\t"
        "v_and_b32 v13, 0xf0, v0\n\t"
        "v_lshl_add_u32 v22, v22, 10, v13\n\t"
        "v_lshrrev_b32 v13, 4, v10\n\t"
        "v_mul_u32_u24 v21, 8192, v13\n\t"
        "s_and_b32 s51, s44, 1\n\t"
        "s_lshl_b32 s51, s51, 3\n\t"
        "v_lshl_or_b32 v13, v13, 1, s51\n\t"
        "v_xor_b32 v13, v13, v10\n\t"
        "v_and_b32 v13, 15, v13\n\t"
        "v_lshl_add_u32 v21, v13, 4, v21\n\t"
        "v_and_b32 v26, 48, v10\n\t"
        "v_lshlrev_b32 v26, 7, v26\n\t"
        "v_and_b32 v13, 12, v10\n\t"
        "v_lshl_add_u32 v26, v13, 6, v26\n\t"
        "s_lshl_b32 s51, s44, 3\n\t"
        "v_xor_b32 v13, s51, v10\n\t"
        "v_and_b32 v13, 31, v13\n\t"
        "v_lshl_add_u32 v26, v13, 3, v26\n\t"
        "v_add_u32 v26, 32768, v26\n\t"
        "v_xor_b32 v27, 32, v26\n\t"
        "s_lshl_b32 s58, s45, 8\n\t"
        "s_lshl_b32 s51, s44, 16\n\t"
        "s_add_u32 s58, s58, s51\n\t"
        "s_lshl_b32 s70, s44, 11\n\t"
        "s_add_u32 s70, s70, 32768\n\t"
        "s_cmp_eq_u32 s46, 0\n\t"
        "s_cbranch_scc1 Lnd_%=\n\t"
        "s_sleep 50\n\t"
        "Lnd_%=:\n\t"
        "s_add_u32 m0, s70, 0\n\t"
        "s_add_u32 s52, s58, 0\n\t"
        "buffer_load_dwordx4 v21, s[88:91], s52 offen lds\n\t"
        "v_add_u32 v28, 0x10000, v26\n\t"
        "v_add_u32 v29, 0x10000, v27\n\t"
        "s_mov_b32 s72, 0x05040100\n\t"
        "s_add_u32 s52, s58, 31744\n\t"
        "buffer_load_dwordx4 v21, s[88:91], s52 offen offset:1024 lds\n\t"
        "s_mov_b32 s73, 0x07060302\n\t"
        "v_and_b32 v11, 15, v10\n\t"
        "v_lshrrev_b32 v12, 4, v10\n\t"
        "s_add_u32 m0, s70, 8192\n\t"
        "s_add_u32 s52, s58, 262144\n\t"
        "buffer_load_dwordx4 v21, s[88:91], s52 offen lds\n\t"
        "v_add_u32 v1, s49, v0\n\t"
        "v_bfe_i32 v15, v10, 0, 1\n\t"
        "v_bfe_i32 v16, v10, 1, 1\n\t"
        "s_add_u32 s52, s58, 293888\n\t"
        "buffer_load_dwordx4 v21, s[88:91], s52 offen offset:1024 lds\n\t"
        "v_bfe_i32 v17, v10, 2, 1\n\t"
        "v_bfe_i32 v18, v10, 3, 1\n\t"
        "v_bfe_i32 v19, v10, 4, 1\n\t"
        "s_xor_b32 s52, s59, 0\n\t"
        "buffer_load_dwordx4 v[32:35], v22, s[92:95], s52 offen offset:0\n\t"
        "v_bfe_i32 v20, v10, 5, 1\n\t"
        "v_mov_b32 v3, 0x1c\n\t"
        "v_and_b32 v13, 0x20, v15\n\t"
        "buffer_load_dwordx4 v[36:39], v22, s[92:95], s52 offen offset:1024\n\t"
        "v_xor_b32 v3, v3, v13\n\t"
        "v_and_b32 v13, 0x40, v16\n\t"
        "v_xor_b32 v3, v3, v13\n\t"
        "buffer_load_dwordx4 v[40:43], v22, s[92:95], s52 offen offset:2048\n\t"
        "v_and_b32 v13, 0x80, v17\n\t"
        "v_xor_b32 v3, v3, v13\n\t"
        "v_and_b32 v13, 0x100, v18\n\t"
        "buffer_load_dwordx4 v[44:47], v22, s[92:95], s52 offen offset:3072\n\t"
        "v_xor_b32 v3, v3, v13\n\t"
        "v_and_b32 v13, 0x200, v19\n\t"
        "v_xor_b32 v3, v3, v13\n\t"
        "s_add_u32 m0, s70, 32768\n\t"
        "s_add_u32 s52, s58, 524288\n\t"
        "buffer_load_dwordx4 v21, s[88:91], s52 offen lds\n\t"
        "v_and_b32 v13, 0x400, v20\n\t"
        "v_xor_b32 v3, v3, v13\n\t"
        "v_mov_b32 v2, 0x0\n\t"
        "s_add_u32 s52, s58, 556032\n\t"
        "buffer_load_dwordx4 v21, s[88:91], s52 offen offset:1024 lds\n\t"
        "v_and_b32 v13, 0x10, v15\n\t"
        "v_xor_b32 v2, v2, v13\n\t"
        "v_and_b32 v13, 0x20, v16\n\t"
        "s_add_u32 m0, s70, 40960\n\t"
        "s_add_u32 s52, s58, 786432\n\t"
        "buffer_load_dwordx4 v21, s[88:91], s52 offen lds\n\t"
        "v_xor_b32 v2, v2, v13\n\t"
        "v_and_b32 v13, 0x40, v17\n\t"
        "v_xor_b32 v2, v2, v13\n\t"
        "s_add_u32 s52, s58, 818176\n\t"
        "buffer_load_dwordx4 v21, s[88:91], s52 offen offset:1024 lds\n\t"
        "v_and_b32 v13, 0x80, v18\n\t"
        "v_xor_b32 v2, v2, v13\n\t"
        "v_and_b32 v13, 0x100, v19\n\t"
        "s_add_u32 m0, s70, 65536\n\t"
        "s_add_u32 s52, s58, 1048576\n\t"
        "buffer_load_dwordx4 v21, s[88:91], s52 offen lds\n\t"
        "v_xor_b32 v2, v2, v13\n\t"
        "v_and_b32 v13, 0x200, v20\n\t"
        "v_xor_b32 v2, v2, v13\n\t"
        "s_add_u32 s52, s58, 1080320\n\t"
        "buffer_load_dwordx4 v21, s[88:91], s52 offen offset:1024 lds\n\t"
        "s_lshl_b32 s50, s44, 11\n\t"
        "v_add_u32 v2, s50, v2\n\t"
        "v_mov_b32 v4, 0x0\n\t"
        "s_add_u32 m0, s70, 73728\n\t"
        "s_add_u32 s52, s58, 1310720\n\t"
        "buffer_load_dwordx4 v21, s[88:91], s52 offen lds\n\t"
        "v_and_b32 v13, 0x40, v15\n\t"
        "v_xor_b32 v4, v4, v13\n\t"
        "v_and_b32 v13, 0x80, v16\n\t"
        "s_add_u32 s52, s58, 1342464\n\t"
        "buffer_load_dwordx4 v21, s[88:91], s52 offen offset:1024 lds\n\t"
        "v_xor_b32 v4, v4, v13\n\t"
        "v_and_b32 v13, 0x110, v17\n\t"
        "v_xor_b32 v4, v4, v13\n\t"
        "s_xor_b32 s52, s59, 8192\n\t"
        "buffer_load_dwordx4 v[48:51], v22, s[92:95], s52 offen offset:0\n\t"
        "v_and_b32 v13, 0x220, v18\n\t"
        "v_xor_b32 v4, v4, v13\n\t"
        "v_and_b32 v13, 0x30, v19\n\t"
        "buffer_load_dwordx4 v[52:55], v22, s[92:95], s52 offen offset:1024\n\t"
        "v_xor_b32 v4, v4, v13\n\t"
        "v_and_b32 v13, 0x10, v20\n\t"
        "v_xor_b32 v4, v4, v13\n\t"
        "buffer_load_dwordx4 v[56:59], v22, s[92:95], s52 offen offset:2048\n\t"
        "v_mov_b32 v5, 0x0\n\t"
        "v_and_b32 v13, 0x8, v15\n\t"
        "v_xor_b32 v5, v5, v13\n\t"
        "buffer_load_dwordx4 v[60:63], v22, s[92:95], s52 offen offset:3072\n\t"
        "v_and_b32 v13, 0x10, v16\n\t"
        "v_xor_b32 v5, v5, v13\n\t"
        "v_and_b32 v13, 0x20, v17\n\t"
        "s_add_u32 m0, s70, 98304\n\t"
        "s_add_u32 s52, s58, 1572864\n\t"
        "buffer_load_dwordx4 v21, s[88:91], s52 offen lds\n\t"
        "v_xor_b32 v5, v5, v13\n\t"
        "v_and_b32 v13, 0x40, v18\n\t"
        "v_xor_b32 v5, v5, v13\n\t"
        "s_add_u32 s52, s58, 1604608\n\t"
        "buffer_load_dwordx4 v21, s[88:91], s52 offen offset:1024 lds\n\t"
        "v_and_b32 v13, 0x80, v19\n\t"
        "v_xor_b32 v5, v5, v13\n\t"
        "v_and_b32 v13, 0x140, v20\n\t"
        "s_add_u32 m0, s70, 106496\n\t"
        "s_add_u32 s52, s58, 1835008\n\t"
        "buffer_load_dwordx4 v21, s[88:91], s52 offen lds\n\t"
        "v_xor_b32 v5, v5, v13\n\t"
        "s_lshl_b32 s50, s44, 10\n\t"
        "s_add_u32 s50, s50, 24576\n\t"
        "s_add_u32 s52, s58, 1866752\n\t"
        "buffer_load_dwordx4 v21, s[88:91], s52 offen offset:1024 lds\n\t"
        "v_add_u32 v5, s50, v5\n\t"
        "v_mov_b32 v6, 0x0\n\t"
        "v_and_b32 v13, 0x80, v15\n\t"
        "s_add_u32 m0, s70, 16384\n\t"
        "s_add_u32 s52, s58, 2097152\n\t"
        "buffer_load_dwordx4 v21, s[88:91], s52 offen lds\n\t"
        "v_xor_b32 v6, v6, v13\n\t"
        "v_and_b32 v13, 0x140, v16\n\t"
        "v_xor_b32 v6, v6, v13\n\t"
        "s_add_u32 s52, s58, 2128896\n\t"
        "buffer_load_dwordx4 v21, s[88:91], s52 offen offset:1024 lds\n\t"
        "v_and_b32 v13, 0x8, v17\n\t"
        "v_xor_b32 v6, v6, v13\n\t"
        "v_and_b32 v13, 0x10, v18\n\t"
        "s_add_u32 m0, s70, 24576\n\t"
        "s_add_u32 s52, s58, 2359296\n\t"
        "buffer_load_dwordx4 v21, s[88:91], s52 offen lds\n\t"
        "v_xor_b32 v6, v6, v13\n\t"
        "v_and_b32 v13, 0x20, v19\n\t"
        "v_xor_b32 v6, v6, v13\n\t"
        "s_add_u32 s52, s58, 2391040\n\t"
        "buffer_load_dwordx4 v21, s[88:91], s52 offen offset:1024 lds\n\t"
        "v_and_b32 v13, 0x40, v20\n\t"
        "v_xor_b32 v6, v6, v13\n\t"
        "v_mov_b32 v7, 0x0\n\t"
        "s_xor_b32 s52, s59, 32768\n\t"
        "buffer_load_dwordx4 v[64:67], v22, s[92:95], s52 offen offset:0\n\t"
        "v_and_b32 v13, 0x10, v15\n\t"
        "v_xor_b32 v7, v7, v13\n\t"
        "v_and_b32 v13, 0x20, v16\n\t"
        "buffer_load_dwordx4 v[68:71], v22, s[92:95], s52 offen offset:1024\n\t"
        "v_xor_b32 v7, v7, v13\n\t"
        "v_and_b32 v13, 0x40, v17\n\t"
        "v_xor_b32 v7, v7, v13\n\t"
        "buffer_load_dwordx4 v[72:75], v22, s[92:95], s52 offen offset:2048\n\t"
        "v_and_b32 v13, 0x80, v18\n\t"
        "v_xor_b32 v7, v7, v13\n\t"
        "v_and_b32 v13, 0x8000, v19\n\t"
        "buffer_load_dwordx4 v[76:79], v22, s[92:95], s52 offen offset:3072\n\t"
        "v_xor_b32 v7, v7, v13\n\t"
        "v_and_b32 v13, 0x10000, v20\n\t"
        "v_xor_b32 v7, v7, v13\n\t"
        "s_add_u32 m0, s70, 49152\n\t"
        "s_add_u32 s52, s58, 2621440\n\t"
        "buffer_load_dwordx4 v21, s[88:91], s52 offen lds\n\t"
        "v_mov_b32 v8, 0x4444\n\t"
        "v_and_b32 v13, 0xaaaa, v16\n\t"
        "v_xor_b32 v8, v8, v13\n\t"
        "s_add_u32 s52, s58, 2653184\n\t"
        "buffer_load_dwordx4 v21, s[88:91], s52 offen offset:1024 lds\n\t"
        "s_lshl_b32 s50, s44, 10\n\t"
        "v_add_u32 v9, s50, v0\n\t"
        "s_cmp_eq_u32 s44, 1\n\t"
        "s_cselect_b32 s50, 15, 0\n\t"
        "s_add_u32 m0, s70, 57344\n\t"
        "s_add_u32 s52, s58, 2883584\n\t"
        "buffer_load_dwordx4 v21, s[88:91], s52 offen lds\n\t"
        "v_or_b32 v11, s50, v11\n\t"
        "v_lshl_add_u32 v13, v12, 2, 0\n\t"
        "v_cmp_le_u32 s[62:63], v13, v11\n\t"
        "s_add_u32 s52, s58, 2915328\n\t"
        "buffer_load_dwordx4 v21, s[88:91], s52 offen offset:1024 lds\n\t"
        "v_lshl_add_u32 v13, v12, 2, 1\n\t"
        "v_cmp_le_u32 s[64:65], v13, v11\n\t"
        "v_lshl_add_u32 v13, v12, 2, 2\n\t"
        "s_add_u32 m0, s70, 81920\n\t"
        "s_add_u32 s52, s58, 3145728\n\t"
        "buffer_load_dwordx4 v21, s[88:91], s52 offen lds\n\t"
        "v_cmp_le_u32 s[66:67], v13, v11\n\t"
        "v_lshl_add_u32 v13, v12, 2, 3\n\t"
        "v_cmp_le_u32 s[68:69], v13, v11\n\t"
        "s_add_u32 s52, s58, 3177472\n\t"
        "buffer_load_dwordx4 v21, s[88:91], s52 offen offset:1024 lds\n\t"
        "s_nop 4\n\t"
        "s_lshl_b32 s77, s47, 3\n\t"
        "s_lshr_b32 s50, s44, 1\n\t"
        "s_add_u32 m0, s70, 90112\n\t"
        "s_add_u32 s52, s58, 3407872\n\t"
        "buffer_load_dwordx4 v21, s[88:91], s52 offen lds\n\t"
        "s_add_u32 s77, s77, s50\n\t"
        "s_lshl_b32 s77, s77, 5\n\t"
        "s_add_u32 s77, s77, s45\n\t"
        "s_add_u32 s52, s58, 3439616\n\t"
        "buffer_load_dwordx4 v21, s[88:91], s52 offen offset:1024 lds\n\t"
        "s_lshl_b32 s77, s77, 7\n\t"
        "s_add_u32 s77, s77, 30474240\n\t"
        "s_add_u32 s77, s77, s12\n\t"
        "s_xor_b32 s52, s59, 40960\n\t"
        "buffer_load_dwordx4 v[80:83], v22, s[92:95], s52 offen offset:0\n\t"
        "s_and_b32 s50, s44, 1\n\t"
        "s_lshl4_add_u32 s77, s50, s77\n\t"
        "s_and_b32 s76, s45, 16\n\t"
        "buffer_load_dwordx4 v[84:87], v22, s[92:95], s52 offen offset:1024\n\t"
        "s_add_u32 s76, s76, s45\n\t"
        "s_lshl_b32 s76, s76, 7\n\t"
        "s_add_u32 s76, s76, s47\n\t"
        "buffer_load_dwordx4 v[88:91], v22, s[92:95], s52 offen offset:2048\n\t"
        "s_lshl2_add_u32 s76, s76, s12\n\t"
        "s_add_u32 s76, s76, 30408704\n\t"
        "s_cmp_eq_u32 s44, 0\n\t"
        "s_cselect_b32 s76, s76, 0x7ff00000\n\t"
        "s_cselect_b64 s[14:15], 15, 0\n\t"
        "buffer_load_dwordx4 v[92:95], v22, s[92:95], s52 offen offset:3072\n\t"
        "s_add_u32 s39, s47, 1\n\t"
        "s_lshl_b32 s39, s39, 24\n\t"
        "s_add_u32 m0, s70, 114688\n\t"
        "s_add_u32 s52, s58, 3670016\n\t"
        "buffer_load_dwordx4 v21, s[88:91], s52 offen lds\n\t"
        "s_add_u32 s52, s58, 3701760\n\t"
        "buffer_load_dwordx4 v21, s[88:91], s52 offen offset:1024 lds\n\t"
        "s_add_u32 m0, s70, 122880\n\t"
        "s_add_u32 s52, s58, 3932160\n\t"
        "buffer_load_dwordx4 v21, s[88:91], s52 offen lds\n\t"
        "s_add_u32 s52, s58, 3963904\n\t"
        "buffer_load_dwordx4 v21, s[88:91], s52 offen offset:1024 lds\n\t"
        "s_waitcnt vmcnt(36)\n\t"
        "s_barrier\n\t"
        "ds_read_b64_tr_b16 v[128:129], v26 offset:0\n\t"
        "ds_read_b64_tr_b16 v[130:131], v26 offset:1024\n\t"
        "ds_read_b64_tr_b16 v[192:193], v27 offset:0\n\t"
        "ds_read_b64_tr_b16 v[194:195], v27 offset:1024\n\t"
        "ds_read_b64_tr_b16 v[132:133], v26 offset:8192\n\t"
        "ds_read_b64_tr_b16 v[134:135], v26 offset:9216\n\t"
        "ds_read_b64_tr_b16 v[196:197], v27 offset:8192\n\t"
        "ds_read_b64_tr_b16 v[198:199], v27 offset:9216\n\t"
        "ds_read_b64_tr_b16 v[136:137], v26 offset:32768\n\t"
        "ds_read_b64_tr_b16 v[138:139], v26 offset:33792\n\t"
        "ds_read_b64_tr_b16 v[200:201], v27 offset:32768\n\t"
        "ds_read_b64_tr_b16 v[202:203], v27 offset:33792\n\t"
        "ds_read_b64_tr_b16 v[140:141], v26 offset:40960\n\t"
        "ds_read_b64_tr_b16 v[142:143], v26 offset:41984\n\t"
        "ds_read_b64_tr_b16 v[204:205], v27 offset:40960\n\t"
        "ds_read_b64_tr_b16 v[206:207], v27 offset:41984\n\t"
        "v_mov_b32 v96, v32\n\t"
        "v_mov_b32 v97, v34\n\t"
        "v_mov_b32 v100, v36\n\t"
        "v_mov_b32 v101, v38\n\t"
        "v_mov_b32 v104, v40\n\t"
        "v_mov_b32 v105, v42\n\t"
        "v_mov_b32 v108, v44\n\t"
        "v_mov_b32 v109, v46\n\t"
        "v_mov_b32 v98, v33\n\t"
        "v_mov_b32 v99, v35\n\t"
        "v_mov_b32 v102, v37\n\t"
        "v_mov_b32 v103, v39\n\t"
        "v_mov_b32 v106, v41\n\t"
        "v_mov_b32 v107, v43\n\t"
        "v_mov_b32 v110, v45\n\t"
        "v_mov_b32 v111, v47\n\t"
        "v_perm_b32 v112, v100, v96, s72\n\t"
        "v_perm_b32 v113, v101, v97, s72\n\t"
        "v_perm_b32 v114, v108, v104, s72\n\t"
        "v_perm_b32 v115, v109, v105, s72\n\t"
        "v_accvgpr_write_b32 a0, v112\n\t"
        "v_accvgpr_write_b32 a1, v113\n\t"
        "v_accvgpr_write_b32 a2, v114\n\t"
        "v_accvgpr_write_b32 a3, v115\n\t"
        "v_perm_b32 v116, v100, v96, s73\n\t"
        "v_perm_b32 v117, v101, v97, s73\n\t"
        "v_perm_b32 v118, v108, v104, s73\n\t"
        "v_perm_b32 v119, v109, v105, s73\n\t"
        "v_accvgpr_write_b32 a8, v116\n\t"
        "v_accvgpr_write_b32 a9, v117\n\t"
        "v_accvgpr_write_b32 a10, v118\n\t"
        "v_accvgpr_write_b32 a11, v119\n\t"
        "v_perm_b32 v120, v102, v98, s72\n\t"
        "v_perm_b32 v121, v103, v99, s72\n\t"
        "v_perm_b32 v122, v110, v106, s72\n\t"
        "v_perm_b32 v123, v111, v107, s72\n\t"
        "v_accvgpr_write_b32 a64, v120\n\t"
        "v_accvgpr_write_b32 a65, v121\n\t"
        "v_accvgpr_write_b32 a66, v122\n\t"
        "v_accvgpr_write_b32 a67, v123\n\t"
        "v_perm_b32 v124, v102, v98, s73\n\t"
        "v_perm_b32 v125, v103, v99, s73\n\t"
        "v_perm_b32 v126, v110, v106, s73\n\t"
        "v_perm_b32 v127, v111, v107, s73\n\t"
        "v_accvgpr_write_b32 a72, v124\n\t"
        "v_accvgpr_write_b32 a73, v125\n\t"
        "v_accvgpr_write_b32 a74, v126\n\t"
        "v_accvgpr_write_b32 a75, v127\n\t"
        "s_xor_b32 s52, s59, 65536\n\t"
        "buffer_load_dwordx4 v[32:35], v22, s[92:95], s52 offen offset:0\n\t"
        "buffer_load_dwordx4 v[36:39], v22, s[92:95], s52 offen offset:1024\n\t"
        "buffer_load_dwordx4 v[40:43], v22, s[92:95], s52 offen offset:2048\n\t"
        "buffer_load_dwordx4 v[44:47], v22, s[92:95], s52 offen offset:3072\n\t"
        "s_waitcnt vmcnt(28)\n\t"
        "s_waitcnt lgkmcnt(0)\n\t"
        "s_barrier\n\t"
        "s_add_u32 m0, s70, 0\n\t"
        "s_add_u32 s52, s58, 1048576\n\t"
        "buffer_load_dwordx4 v21, s[80:83], s52 offen lds\n\t"
        "ds_read_b64_tr_b16 v[144:145], v28 offset:0\n\t"
        "ds_read_b64_tr_b16 v[146:147], v28 offset:1024\n\t"
        "ds_read_b64_tr_b16 v[208:209], v29 offset:0\n\t"
        "ds_read_b64_tr_b16 v[210:211], v29 offset:1024\n\t"
        "v_mov_b32 v96, v48\n\t"
        "v_mov_b32 v97, v50\n\t"
        "v_mov_b32 v100, v52\n\t"
        "v_mov_b32 v101, v54\n\t"
        "v_mov_b32 v104, v56\n\t"
        "v_mov_b32 v105, v58\n\t"
        "v_mov_b32 v108, v60\n\t"
        "s_add_u32 s52, s58, 1080320\n\t"
        "buffer_load_dwordx4 v21, s[80:83], s52 offen offset:1024 lds\n\t"
        "ds_read_b64_tr_b16 v[148:149], v28 offset:8192\n\t"
        "ds_read_b64_tr_b16 v[150:151], v28 offset:9216\n\t"
        "ds_read_b64_tr_b16 v[212:213], v29 offset:8192\n\t"
        "ds_read_b64_tr_b16 v[214:215], v29 offset:9216\n\t"
        "v_mov_b32 v109, v62\n\t"
        "v_mov_b32 v98, v49\n\t"
        "v_mov_b32 v99, v51\n\t"
        "v_mov_b32 v102, v53\n\t"
        "v_mov_b32 v103, v55\n\t"
        "v_mov_b32 v106, v57\n\t"
        "v_mov_b32 v107, v59\n\t"
        "s_add_u32 m0, s70, 8192\n\t"
        "s_add_u32 s52, s58, 1310720\n\t"
        "buffer_load_dwordx4 v21, s[80:83], s52 offen lds\n\t"
        "ds_read_b64_tr_b16 v[152:153], v28 offset:32768\n\t"
        "ds_read_b64_tr_b16 v[154:155], v28 offset:33792\n\t"
        "ds_read_b64_tr_b16 v[216:217], v29 offset:32768\n\t"
        "ds_read_b64_tr_b16 v[218:219], v29 offset:33792\n\t"
        "v_mov_b32 v110, v61\n\t"
        "v_mov_b32 v111, v63\n\t"
        "v_perm_b32 v112, v100, v96, s72\n\t"
        "v_perm_b32 v113, v101, v97, s72\n\t"
        "v_perm_b32 v114, v108, v104, s72\n\t"
        "v_perm_b32 v115, v109, v105, s72\n\t"
        "v_accvgpr_write_b32 a4, v112\n\t"
        "s_add_u32 s52, s58, 1342464\n\t"
        "buffer_load_dwordx4 v21, s[80:83], s52 offen offset:1024 lds\n\t"
        "ds_read_b64_tr_b16 v[156:157], v28 offset:40960\n\t"
        "ds_read_b64_tr_b16 v[158:159], v28 offset:41984\n\t"
        "ds_read_b64_tr_b16 v[220:221], v29 offset:40960\n\t"
        "ds_read_b64_tr_b16 v[222:223], v29 offset:41984\n\t"
        "v_accvgpr_write_b32 a5, v113\n\t"
        "v_accvgpr_write_b32 a6, v114\n\t"
        "v_accvgpr_write_b32 a7, v115\n\t"
        "v_perm_b32 v116, v100, v96, s73\n\t"
        "v_perm_b32 v117, v101, v97, s73\n\t"
        "v_perm_b32 v118, v108, v104, s73\n\t"
        "v_perm_b32 v119, v109, v105, s73\n\t"
        "s_add_u32 m0, s70, 32768\n\t"
        "s_add_u32 s52, s58, 1048576\n\t"
        "buffer_load_dwordx4 v21, s[84:87], s52 offen lds\n\t"
        "v_accvgpr_write_b32 a12, v116\n\t"
        "v_accvgpr_write_b32 a13, v117\n\t"
        "v_accvgpr_write_b32 a14, v118\n\t"
        "v_accvgpr_write_b32 a15, v119\n\t"
        "v_perm_b32 v120, v102, v98, s72\n\t"
        "v_perm_b32 v121, v103, v99, s72\n\t"
        "v_perm_b32 v122, v110, v106, s72\n\t"
        "v_perm_b32 v123, v111, v107, s72\n\t"
        "v_accvgpr_write_b32 a68, v120\n\t"
        "s_add_u32 s52, s58, 1080320\n\t"
        "buffer_load_dwordx4 v21, s[84:87], s52 offen offset:1024 lds\n\t"
        "v_accvgpr_write_b32 a69, v121\n\t"
        "v_accvgpr_write_b32 a70, v122\n\t"
        "v_accvgpr_write_b32 a71, v123\n\t"
        "v_perm_b32 v124, v102, v98, s73\n\t"
        "v_perm_b32 v125, v103, v99, s73\n\t"
        "v_perm_b32 v126, v110, v106, s73\n\t"
        "v_perm_b32 v127, v111, v107, s73\n\t"
        "v_accvgpr_write_b32 a76, v124\n\t"
        "v_accvgpr_write_b32 a77, v125\n\t"
        "s_add_u32 m0, s70, 40960\n\t"
        "s_add_u32 s52, s58, 1310720\n\t"
        "buffer_load_dwordx4 v21, s[84:87], s52 offen lds\n\t"
        "v_accvgpr_write_b32 a78, v126\n\t"
        "v_accvgpr_write_b32 a79, v127\n\t"
        "s_add_u32 s52, s58, 1342464\n\t"
        "buffer_load_dwordx4 v21, s[84:87], s52 offen offset:1024 lds\n\t"
        "s_xor_b32 s52, s59, 73728\n\t"
        "buffer_load_dwordx4 v[48:51], v22, s[92:95], s52 offen offset:0\n\t"
        "buffer_load_dwordx4 v[52:55], v22, s[92:95], s52 offen offset:1024\n\t"
        "buffer_load_dwordx4 v[56:59], v22, s[92:95], s52 offen offset:2048\n\t"
        "buffer_load_dwordx4 v[60:63], v22, s[92:95], s52 offen offset:3072\n\t"
        "s_waitcnt vmcnt(28)\n\t"
        "s_waitcnt lgkmcnt(0)\n\t"
        "s_barrier\n\t"
        "s_add_u32 m0, s70, 65536\n\t"
        "s_add_u32 s52, s58, 1572864\n\t"
        "buffer_load_dwordx4 v21, s[80:83], s52 offen lds\n\t"
        "ds_read_b64_tr_b16 v[160:161], v26 offset:16384\n\t"
        "ds_read_b64_tr_b16 v[162:163], v26 offset:17408\n\t"
        "ds_read_b64_tr_b16 v[224:225], v27 offset:16384\n\t"
        "ds_read_b64_tr_b16 v[226:227], v27 offset:17408\n\t"
        "v_mov_b32 v96, v64\n\t"
        "v_mov_b32 v97, v66\n\t"
        "v_mov_b32 v100, v68\n\t"
        "v_mov_b32 v101, v70\n\t"
        "v_mov_b32 v104, v72\n\t"
        "v_mov_b32 v105, v74\n\t"
        "v_mov_b32 v108, v76\n\t"
        "s_add_u32 s52, s58, 1604608\n\t"
        "buffer_load_dwordx4 v21, s[80:83], s52 offen offset:1024 lds\n\t"
        "ds_read_b64_tr_b16 v[164:165], v26 offset:24576\n\t"
        "ds_read_b64_tr_b16 v[166:167], v26 offset:25600\n\t"
        "ds_read_b64_tr_b16 v[228:229], v27 offset:24576\n\t"
        "ds_read_b64_tr_b16 v[230:231], v27 offset:25600\n\t"
        "v_mov_b32 v109, v78\n\t"
        "v_mov_b32 v98, v65\n\t"
        "v_mov_b32 v99, v67\n\t"
        "v_mov_b32 v102, v69\n\t"
        "v_mov_b32 v103, v71\n\t"
        "v_mov_b32 v106, v73\n\t"
        "v_mov_b32 v107, v75\n\t"
        "s_add_u32 m0, s70, 73728\n\t"
        "s_add_u32 s52, s58, 1835008\n\t"
        "buffer_load_dwordx4 v21, s[80:83], s52 offen lds\n\t"
        "ds_read_b64_tr_b16 v[168:169], v26 offset:49152\n\t"
        "ds_read_b64_tr_b16 v[170:171], v26 offset:50176\n\t"
        "ds_read_b64_tr_b16 v[232:233], v27 offset:49152\n\t"
        "ds_read_b64_tr_b16 v[234:235], v27 offset:50176\n\t"
        "v_mov_b32 v110, v77\n\t"
        "v_mov_b32 v111, v79\n\t"
        "v_perm_b32 v112, v100, v96, s72\n\t"
        "v_perm_b32 v113, v101, v97, s72\n\t"
        "v_perm_b32 v114, v108, v104, s72\n\t"
        "v_perm_b32 v115, v109, v105, s72\n\t"
        "v_accvgpr_write_b32 a128, v112\n\t"
        "s_add_u32 s52, s58, 1866752\n\t"
        "buffer_load_dwordx4 v21, s[80:83], s52 offen offset:1024 lds\n\t"
        "ds_read_b64_tr_b16 v[172:173], v26 offset:57344\n\t"
        "ds_read_b64_tr_b16 v[174:175], v26 offset:58368\n\t"
        "ds_read_b64_tr_b16 v[236:237], v27 offset:57344\n\t"
        "ds_read_b64_tr_b16 v[238:239], v27 offset:58368\n\t"
        "v_accvgpr_write_b32 a129, v113\n\t"
        "v_accvgpr_write_b32 a130, v114\n\t"
        "v_accvgpr_write_b32 a131, v115\n\t"
        "v_perm_b32 v116, v100, v96, s73\n\t"
        "v_perm_b32 v117, v101, v97, s73\n\t"
        "v_perm_b32 v118, v108, v104, s73\n\t"
        "v_perm_b32 v119, v109, v105, s73\n\t"
        "s_add_u32 m0, s70, 98304\n\t"
        "s_add_u32 s52, s58, 1572864\n\t"
        "buffer_load_dwordx4 v21, s[84:87], s52 offen lds\n\t"
        "v_accvgpr_write_b32 a136, v116\n\t"
        "v_accvgpr_write_b32 a137, v117\n\t"
        "v_accvgpr_write_b32 a138, v118\n\t"
        "v_accvgpr_write_b32 a139, v119\n\t"
        "v_perm_b32 v120, v102, v98, s72\n\t"
        "v_perm_b32 v121, v103, v99, s72\n\t"
        "v_perm_b32 v122, v110, v106, s72\n\t"
        "v_perm_b32 v123, v111, v107, s72\n\t"
        "v_accvgpr_write_b32 a192, v120\n\t"
        "s_add_u32 s52, s58, 1604608\n\t"
        "buffer_load_dwordx4 v21, s[84:87], s52 offen offset:1024 lds\n\t"
        "v_accvgpr_write_b32 a193, v121\n\t"
        "v_accvgpr_write_b32 a194, v122\n\t"
        "v_accvgpr_write_b32 a195, v123\n\t"
        "v_perm_b32 v124, v102, v98, s73\n\t"
        "v_perm_b32 v125, v103, v99, s73\n\t"
        "v_perm_b32 v126, v110, v106, s73\n\t"
        "v_perm_b32 v127, v111, v107, s73\n\t"
        "v_accvgpr_write_b32 a200, v124\n\t"
        "v_accvgpr_write_b32 a201, v125\n\t"
        "s_add_u32 m0, s70, 106496\n\t"
        "s_add_u32 s52, s58, 1835008\n\t"
        "buffer_load_dwordx4 v21, s[84:87], s52 offen lds\n\t"
        "v_accvgpr_write_b32 a202, v126\n\t"
        "v_accvgpr_write_b32 a203, v127\n\t"
        "s_add_u32 s52, s58, 1866752\n\t"
        "buffer_load_dwordx4 v21, s[84:87], s52 offen offset:1024 lds\n\t"
        "s_xor_b32 s52, s59, 98304\n\t"
        "buffer_load_dwordx4 v[64:67], v22, s[92:95], s52 offen offset:0\n\t"
        "buffer_load_dwordx4 v[68:71], v22, s[92:95], s52 offen offset:1024\n\t"
        "buffer_load_dwordx4 v[72:75], v22, s[92:95], s52 offen offset:2048\n\t"
        "buffer_load_dwordx4 v[76:79], v22, s[92:95], s52 offen offset:3072\n\t"
        "s_waitcnt vmcnt(28)\n\t"
        "s_waitcnt lgkmcnt(0)\n\t"
        "s_barrier\n\t"
        "s_add_u32 m0, s70, 16384\n\t"
        "s_add_u32 s52, s58, 2097152\n\t"
        "buffer_load_dwordx4 v21, s[80:83], s52 offen lds\n\t"
        "ds_read_b64_tr_b16 v[176:177], v28 offset:16384\n\t"
        "ds_read_b64_tr_b16 v[178:179], v28 offset:17408\n\t"
        "ds_read_b64_tr_b16 v[240:241], v29 offset:16384\n\t"
        "ds_read_b64_tr_b16 v[242:243], v29 offset:17408\n\t"
        "v_mov_b32 v96, v80\n\t"
        "v_mov_b32 v97, v82\n\t"
        "v_mov_b32 v100, v84\n\t"
        "v_mov_b32 v101, v86\n\t"
        "v_mov_b32 v104, v88\n\t"
        "v_mov_b32 v105, v90\n\t"
        "v_mov_b32 v108, v92\n\t"
        "s_add_u32 s52, s58, 2128896\n\t"
        "buffer_load_dwordx4 v21, s[80:83], s52 offen offset:1024 lds\n\t"
        "ds_read_b64_tr_b16 v[180:181], v28 offset:24576\n\t"
        "ds_read_b64_tr_b16 v[182:183], v28 offset:25600\n\t"
        "ds_read_b64_tr_b16 v[244:245], v29 offset:24576\n\t"
        "ds_read_b64_tr_b16 v[246:247], v29 offset:25600\n\t"
        "v_mov_b32 v109, v94\n\t"
        "v_mov_b32 v98, v81\n\t"
        "v_mov_b32 v99, v83\n\t"
        "v_mov_b32 v102, v85\n\t"
        "v_mov_b32 v103, v87\n\t"
        "v_mov_b32 v106, v89\n\t"
        "v_mov_b32 v107, v91\n\t"
        "s_add_u32 m0, s70, 24576\n\t"
        "s_add_u32 s52, s58, 2359296\n\t"
        "buffer_load_dwordx4 v21, s[80:83], s52 offen lds\n\t"
        "ds_read_b64_tr_b16 v[184:185], v28 offset:49152\n\t"
        "ds_read_b64_tr_b16 v[186:187], v28 offset:50176\n\t"
        "ds_read_b64_tr_b16 v[248:249], v29 offset:49152\n\t"
        "ds_read_b64_tr_b16 v[250:251], v29 offset:50176\n\t"
        "v_mov_b32 v110, v93\n\t"
        "v_mov_b32 v111, v95\n\t"
        "v_perm_b32 v112, v100, v96, s72\n\t"
        "v_perm_b32 v113, v101, v97, s72\n\t"
        "v_perm_b32 v114, v108, v104, s72\n\t"
        "v_perm_b32 v115, v109, v105, s72\n\t"
        "v_accvgpr_write_b32 a132, v112\n\t"
        "s_add_u32 s52, s58, 2391040\n\t"
        "buffer_load_dwordx4 v21, s[80:83], s52 offen offset:1024 lds\n\t"
        "ds_read_b64_tr_b16 v[188:189], v28 offset:57344\n\t"
        "ds_read_b64_tr_b16 v[190:191], v28 offset:58368\n\t"
        "ds_read_b64_tr_b16 v[252:253], v29 offset:57344\n\t"
        "ds_read_b64_tr_b16 v[254:255], v29 offset:58368\n\t"
        "v_accvgpr_write_b32 a133, v113\n\t"
        "v_accvgpr_write_b32 a134, v114\n\t"
        "v_accvgpr_write_b32 a135, v115\n\t"
        "v_perm_b32 v116, v100, v96, s73\n\t"
        "v_perm_b32 v117, v101, v97, s73\n\t"
        "v_perm_b32 v118, v108, v104, s73\n\t"
        "v_perm_b32 v119, v109, v105, s73\n\t"
        "s_add_u32 m0, s70, 49152\n\t"
        "s_add_u32 s52, s58, 2097152\n\t"
        "buffer_load_dwordx4 v21, s[84:87], s52 offen lds\n\t"
        "v_accvgpr_write_b32 a140, v116\n\t"
        "v_accvgpr_write_b32 a141, v117\n\t"
        "v_accvgpr_write_b32 a142, v118\n\t"
        "v_accvgpr_write_b32 a143, v119\n\t"
        "v_perm_b32 v120, v102, v98, s72\n\t"
        "v_perm_b32 v121, v103, v99, s72\n\t"
        "v_perm_b32 v122, v110, v106, s72\n\t"
        "v_perm_b32 v123, v111, v107, s72\n\t"
        "v_accvgpr_write_b32 a196, v120\n\t"
        "s_add_u32 s52, s58, 2128896\n\t"
        "buffer_load_dwordx4 v21, s[84:87], s52 offen offset:1024 lds\n\t"
        "v_accvgpr_write_b32 a197, v121\n\t"
        "v_accvgpr_write_b32 a198, v122\n\t"
        "v_accvgpr_write_b32 a199, v123\n\t"
        "v_perm_b32 v124, v102, v98, s73\n\t"
        "v_perm_b32 v125, v103, v99, s73\n\t"
        "v_perm_b32 v126, v110, v106, s73\n\t"
        "v_perm_b32 v127, v111, v107, s73\n\t"
        "v_accvgpr_write_b32 a204, v124\n\t"
        "v_accvgpr_write_b32 a205, v125\n\t"
        "s_add_u32 m0, s70, 57344\n\t"
        "s_add_u32 s52, s58, 2359296\n\t"
        "buffer_load_dwordx4 v21, s[84:87], s52 offen lds\n\t"
        "v_accvgpr_write_b32 a206, v126\n\t"
        "v_accvgpr_write_b32 a207, v127\n\t"
        "s_add_u32 s52, s58, 2391040\n\t"
        "buffer_load_dwordx4 v21, s[84:87], s52 offen offset:1024 lds\n\t"
        "s_xor_b32 s52, s59, 106496\n\t"
        "buffer_load_dwordx4 v[80:83], v22, s[92:95], s52 offen offset:0\n\t"
        "buffer_load_dwordx4 v[84:87], v22, s[92:95], s52 offen offset:1024\n\t"
        "buffer_load_dwordx4 v[88:91], v22, s[92:95], s52 offen offset:2048\n\t"
        "buffer_load_dwordx4 v[92:95], v22, s[92:95], s52 offen offset:3072\n\t"
        "s_waitcnt vmcnt(28)\n\t"
        "s_waitcnt lgkmcnt(0)\n\t"
        "s_barrier\n\t"
        "s_add_u32 m0, s70, 81920\n\t"
        "s_add_u32 s52, s58, 2621440\n\t"
        "buffer_load_dwordx4 v21, s[80:83], s52 offen lds\n\t"
        "ds_read_b64_tr_b16 a[16:17], v26 offset:0\n\t"
        "ds_read_b64_tr_b16 a[18:19], v26 offset:1024\n\t"
        "ds_read_b64_tr_b16 a[80:81], v27 offset:0\n\t"
        "ds_read_b64_tr_b16 a[82:83], v27 offset:1024\n\t"
        "v_mov_b32 v96, v32\n\t"
        "v_mov_b32 v97, v34\n\t"
        "v_mov_b32 v100, v36\n\t"
        "v_mov_b32 v101, v38\n\t"
        "v_mov_b32 v104, v40\n\t"
        "v_mov_b32 v105, v42\n\t"
        "v_mov_b32 v108, v44\n\t"
        "s_add_u32 s52, s58, 2653184\n\t"
        "buffer_load_dwordx4 v21, s[80:83], s52 offen offset:1024 lds\n\t"
        "ds_read_b64_tr_b16 a[20:21], v26 offset:8192\n\t"
        "ds_read_b64_tr_b16 a[22:23], v26 offset:9216\n\t"
        "ds_read_b64_tr_b16 a[84:85], v27 offset:8192\n\t"
        "ds_read_b64_tr_b16 a[86:87], v27 offset:9216\n\t"
        "v_mov_b32 v109, v46\n\t"
        "v_mov_b32 v98, v33\n\t"
        "v_mov_b32 v99, v35\n\t"
        "v_mov_b32 v102, v37\n\t"
        "v_mov_b32 v103, v39\n\t"
        "v_mov_b32 v106, v41\n\t"
        "v_mov_b32 v107, v43\n\t"
        "s_add_u32 m0, s70, 90112\n\t"
        "s_add_u32 s52, s58, 2883584\n\t"
        "buffer_load_dwordx4 v21, s[80:83], s52 offen lds\n\t"
        "ds_read_b64_tr_b16 a[144:145], v26 offset:32768\n\t"
        "ds_read_b64_tr_b16 a[146:147], v26 offset:33792\n\t"
        "ds_read_b64_tr_b16 a[208:209], v27 offset:32768\n\t"
        "ds_read_b64_tr_b16 a[210:211], v27 offset:33792\n\t"
        "v_mov_b32 v110, v45\n\t"
        "v_mov_b32 v111, v47\n\t"
        "v_perm_b32 v32, v100, v96, s72\n\t"
        "v_perm_b32 v33, v101, v97, s72\n\t"
        "v_perm_b32 v34, v108, v104, s72\n\t"
        "v_perm_b32 v35, v109, v105, s72\n\t"
        "v_perm_b32 v36, v100, v96, s73\n\t"
        "s_add_u32 s52, s58, 2915328\n\t"
        "buffer_load_dwordx4 v21, s[80:83], s52 offen offset:1024 lds\n\t"
        "ds_read_b64_tr_b16 a[148:149], v26 offset:40960\n\t"
        "ds_read_b64_tr_b16 a[150:151], v26 offset:41984\n\t"
        "ds_read_b64_tr_b16 a[212:213], v27 offset:40960\n\t"
        "ds_read_b64_tr_b16 a[214:215], v27 offset:41984\n\t"
        "v_perm_b32 v37, v101, v97, s73\n\t"
        "v_perm_b32 v38, v108, v104, s73\n\t"
        "v_perm_b32 v39, v109, v105, s73\n\t"
        "v_perm_b32 v40, v102, v98, s72\n\t"
        "v_perm_b32 v41, v103, v99, s72\n\t"
        "v_perm_b32 v42, v110, v106, s72\n\t"
        "v_perm_b32 v43, v111, v107, s72\n\t"
        "s_add_u32 m0, s70, 114688\n\t"
        "s_add_u32 s52, s58, 2621440\n\t"
        "buffer_load_dwordx4 v21, s[84:87], s52 offen lds\n\t"
        "v_perm_b32 v44, v102, v98, s73\n\t"
        "v_perm_b32 v45, v103, v99, s73\n\t"
        "v_perm_b32 v46, v110, v106, s73\n\t"
        "v_perm_b32 v47, v111, v107, s73\n\t"
        "s_add_u32 s52, s58, 2653184\n\t"
        "buffer_load_dwordx4 v21, s[84:87], s52 offen offset:1024 lds\n\t"
        "s_add_u32 m0, s70, 122880\n\t"
        "s_add_u32 s52, s58, 2883584\n\t"
        "buffer_load_dwordx4 v21, s[84:87], s52 offen lds\n\t"
        "s_add_u32 s52, s58, 2915328\n\t"
        "buffer_load_dwordx4 v21, s[84:87], s52 offen offset:1024 lds\n\t"
        "s_waitcnt vmcnt(24)\n\t"
        "s_waitcnt lgkmcnt(0)\n\t"
        "s_barrier\n\t"
        "s_add_u32 m0, s70, 0\n\t"
        "s_add_u32 s52, s58, 3145728\n\t"
        "buffer_load_dwordx4 v21, s[80:83], s52 offen lds\n\t"
        "ds_read_b64_tr_b16 a[24:25], v28 offset:0\n\t"
        "ds_read_b64_tr_b16 a[26:27], v28 offset:1024\n\t"
        "ds_read_b64_tr_b16 a[88:89], v29 offset:0\n\t"
        "ds_read_b64_tr_b16 a[90:91], v29 offset:1024\n\t"
        "v_mov_b32 v96, v48\n\t"
        "v_mov_b32 v97, v50\n\t"
        "v_mov_b32 v100, v52\n\t"
        "v_mov_b32 v101, v54\n\t"
        "v_mov_b32 v104, v56\n\t"
        "v_mov_b32 v105, v58\n\t"
        "v_mov_b32 v108, v60\n\t"
        "s_add_u32 s52, s58, 3177472\n\t"
        "buffer_load_dwordx4 v21, s[80:83], s52 offen offset:1024 lds\n\t"
        "ds_read_b64_tr_b16 a[28:29], v28 offset:8192\n\t"
        "ds_read_b64_tr_b16 a[30:31], v28 offset:9216\n\t"
        "ds_read_b64_tr_b16 a[92:93], v29 offset:8192\n\t"
        "ds_read_b64_tr_b16 a[94:95], v29 offset:9216\n\t"
        "v_mov_b32 v109, v62\n\t"
        "v_mov_b32 v98, v49\n\t"
        "v_mov_b32 v99, v51\n\t"
        "v_mov_b32 v102, v53\n\t"
        "v_mov_b32 v103, v55\n\t"
        "v_mov_b32 v106, v57\n\t"
        "v_mov_b32 v107, v59\n\t"
        "s_add_u32 m0, s70, 8192\n\t"
        "s_add_u32 s52, s58, 3407872\n\t"
        "buffer_load_dwordx4 v21, s[80:83], s52 offen lds\n\t"
        "ds_read_b64_tr_b16 a[152:153], v28 offset:32768\n\t"
        "ds_read_b64_tr_b16 a[154:155], v28 offset:33792\n\t"
        "ds_read_b64_tr_b16 a[216:217], v29 offset:32768\n\t"
        "ds_read_b64_tr_b16 a[218:219], v29 offset:33792\n\t"
        "v_mov_b32 v110, v61\n\t"
        "v_mov_b32 v111, v63\n\t"
        "v_perm_b32 v48, v100, v96, s72\n\t"
        "v_perm_b32 v49, v101, v97, s72\n\t"
        "v_perm_b32 v50, v108, v104, s72\n\t"
        "v_perm_b32 v51, v109, v105, s72\n\t"
        "v_perm_b32 v52, v100, v96, s73\n\t"
        "s_add_u32 s52, s58, 3439616\n\t"
        "buffer_load_dwordx4 v21, s[80:83], s52 offen offset:1024 lds\n\t"
        "ds_read_b64_tr_b16 a[156:157], v28 offset:40960\n\t"
        "ds_read_b64_tr_b16 a[158:159], v28 offset:41984\n\t"
        "ds_read_b64_tr_b16 a[220:221], v29 offset:40960\n\t"
        "ds_read_b64_tr_b16 a[222:223], v29 offset:41984\n\t"
        "v_perm_b32 v53, v101, v97, s73\n\t"
        "v_perm_b32 v54, v108, v104, s73\n\t"
        "v_perm_b32 v55, v109, v105, s73\n\t"
        "v_perm_b32 v56, v102, v98, s72\n\t"
        "v_perm_b32 v57, v103, v99, s72\n\t"
        "v_perm_b32 v58, v110, v106, s72\n\t"
        "v_perm_b32 v59, v111, v107, s72\n\t"
        "s_add_u32 m0, s70, 32768\n\t"
        "s_add_u32 s52, s58, 3145728\n\t"
        "buffer_load_dwordx4 v21, s[84:87], s52 offen lds\n\t"
        "v_perm_b32 v60, v102, v98, s73\n\t"
        "v_perm_b32 v61, v103, v99, s73\n\t"
        "v_perm_b32 v62, v110, v106, s73\n\t"
        "v_perm_b32 v63, v111, v107, s73\n\t"
        "s_add_u32 s52, s58, 3177472\n\t"
        "buffer_load_dwordx4 v21, s[84:87], s52 offen offset:1024 lds\n\t"
        "s_add_u32 m0, s70, 40960\n\t"
        "s_add_u32 s52, s58, 3407872\n\t"
        "buffer_load_dwordx4 v21, s[84:87], s52 offen lds\n\t"
        "s_add_u32 s52, s58, 3439616\n\t"
        "buffer_load_dwordx4 v21, s[84:87], s52 offen offset:1024 lds\n\t"
        "s_waitcnt vmcnt(20)\n\t"
        "s_waitcnt lgkmcnt(0)\n\t"
        "s_barrier\n\t"
        "s_add_u32 m0, s70, 65536\n\t"
        "s_add_u32 s52, s58, 3670016\n\t"
        "buffer_load_dwordx4 v21, s[80:83], s52 offen lds\n\t"
        "ds_read_b64_tr_b16 a[32:33], v26 offset:16384\n\t"
        "ds_read_b64_tr_b16 a[34:35], v26 offset:17408\n\t"
        "ds_read_b64_tr_b16 a[96:97], v27 offset:16384\n\t"
        "ds_read_b64_tr_b16 a[98:99], v27 offset:17408\n\t"
        "v_mov_b32 v96, v64\n\t"
        "v_mov_b32 v97, v66\n\t"
        "v_mov_b32 v100, v68\n\t"
        "v_mov_b32 v101, v70\n\t"
        "v_mov_b32 v104, v72\n\t"
        "v_mov_b32 v105, v74\n\t"
        "v_mov_b32 v108, v76\n\t"
        "s_add_u32 s52, s58, 3701760\n\t"
        "buffer_load_dwordx4 v21, s[80:83], s52 offen offset:1024 lds\n\t"
        "ds_read_b64_tr_b16 a[36:37], v26 offset:24576\n\t"
        "ds_read_b64_tr_b16 a[38:39], v26 offset:25600\n\t"
        "ds_read_b64_tr_b16 a[100:101], v27 offset:24576\n\t"
        "ds_read_b64_tr_b16 a[102:103], v27 offset:25600\n\t"
        "v_mov_b32 v109, v78\n\t"
        "v_mov_b32 v98, v65\n\t"
        "v_mov_b32 v99, v67\n\t"
        "v_mov_b32 v102, v69\n\t"
        "v_mov_b32 v103, v71\n\t"
        "v_mov_b32 v106, v73\n\t"
        "v_mov_b32 v107, v75\n\t"
        "s_add_u32 m0, s70, 73728\n\t"
        "s_add_u32 s52, s58, 3932160\n\t"
        "buffer_load_dwordx4 v21, s[80:83], s52 offen lds\n\t"
        "ds_read_b64_tr_b16 a[160:161], v26 offset:49152\n\t"
        "ds_read_b64_tr_b16 a[162:163], v26 offset:50176\n\t"
        "ds_read_b64_tr_b16 a[224:225], v27 offset:49152\n\t"
        "ds_read_b64_tr_b16 a[226:227], v27 offset:50176\n\t"
        "v_mov_b32 v110, v77\n\t"
        "v_mov_b32 v111, v79\n\t"
        "v_perm_b32 v64, v100, v96, s72\n\t"
        "v_perm_b32 v65, v101, v97, s72\n\t"
        "v_perm_b32 v66, v108, v104, s72\n\t"
        "v_perm_b32 v67, v109, v105, s72\n\t"
        "v_perm_b32 v68, v100, v96, s73\n\t"
        "s_add_u32 s52, s58, 3963904\n\t"
        "buffer_load_dwordx4 v21, s[80:83], s52 offen offset:1024 lds\n\t"
        "ds_read_b64_tr_b16 a[164:165], v26 offset:57344\n\t"
        "ds_read_b64_tr_b16 a[166:167], v26 offset:58368\n\t"
        "ds_read_b64_tr_b16 a[228:229], v27 offset:57344\n\t"
        "ds_read_b64_tr_b16 a[230:231], v27 offset:58368\n\t"
        "v_perm_b32 v69, v101, v97, s73\n\t"
        "v_perm_b32 v70, v108, v104, s73\n\t"
        "v_perm_b32 v71, v109, v105, s73\n\t"
        "v_perm_b32 v72, v102, v98, s72\n\t"
        "v_perm_b32 v73, v103, v99, s72\n\t"
        "v_perm_b32 v74, v110, v106, s72\n\t"
        "v_perm_b32 v75, v111, v107, s72\n\t"
        "s_add_u32 m0, s70, 98304\n\t"
        "s_add_u32 s52, s58, 3670016\n\t"
        "buffer_load_dwordx4 v21, s[84:87], s52 offen lds\n\t"
        "v_perm_b32 v76, v102, v98, s73\n\t"
        "v_perm_b32 v77, v103, v99, s73\n\t"
        "v_perm_b32 v78, v110, v106, s73\n\t"
        "v_perm_b32 v79, v111, v107, s73\n\t"
        "s_add_u32 s52, s58, 3701760\n\t"
        "buffer_load_dwordx4 v21, s[84:87], s52 offen offset:1024 lds\n\t"
        "s_add_u32 m0, s70, 106496\n\t"
        "s_add_u32 s52, s58, 3932160\n\t"
        "buffer_load_dwordx4 v21, s[84:87], s52 offen lds\n\t"
        "s_add_u32 s52, s58, 3963904\n\t"
        "buffer_load_dwordx4 v21, s[84:87], s52 offen offset:1024 lds\n\t"
        "s_waitcnt vmcnt(16)\n\t"
        "s_waitcnt lgkmcnt(0)\n\t"
        "s_barrier\n\t"
        "s_add_u32 m0, s70, 16384\n\t"
        "s_add_u32 s52, s58, 0\n\t"
        "buffer_load_dwordx4 v21, s[80:83], s52 offen lds\n\t"
        "ds_read_b64_tr_b16 a[40:41], v28 offset:16384\n\t"
        "ds_read_b64_tr_b16 a[42:43], v28 offset:17408\n\t"
        "ds_read_b64_tr_b16 a[104:105], v29 offset:16384\n\t"
        "ds_read_b64_tr_b16 a[106:107], v29 offset:17408\n\t"
        "v_mov_b32 v96, v80\n\t"
        "v_mov_b32 v97, v82\n\t"
        "v_mov_b32 v100, v84\n\t"
        "v_mov_b32 v101, v86\n\t"
        "v_mov_b32 v104, v88\n\t"
        "v_mov_b32 v105, v90\n\t"
        "v_mov_b32 v108, v92\n\t"
        "s_add_u32 s52, s58, 31744\n\t"
        "buffer_load_dwordx4 v21, s[80:83], s52 offen offset:1024 lds\n\t"
        "ds_read_b64_tr_b16 a[44:45], v28 offset:24576\n\t"
        "ds_read_b64_tr_b16 a[46:47], v28 offset:25600\n\t"
        "ds_read_b64_tr_b16 a[108:109], v29 offset:24576\n\t"
        "ds_read_b64_tr_b16 a[110:111], v29 offset:25600\n\t"
        "v_mov_b32 v109, v94\n\t"
        "v_mov_b32 v98, v81\n\t"
        "v_mov_b32 v99, v83\n\t"
        "v_mov_b32 v102, v85\n\t"
        "v_mov_b32 v103, v87\n\t"
        "v_mov_b32 v106, v89\n\t"
        "v_mov_b32 v107, v91\n\t"
        "s_add_u32 m0, s70, 24576\n\t"
        "s_add_u32 s52, s58, 262144\n\t"
        "buffer_load_dwordx4 v21, s[80:83], s52 offen lds\n\t"
        "ds_read_b64_tr_b16 a[168:169], v28 offset:49152\n\t"
        "ds_read_b64_tr_b16 a[170:171], v28 offset:50176\n\t"
        "ds_read_b64_tr_b16 a[232:233], v29 offset:49152\n\t"
        "ds_read_b64_tr_b16 a[234:235], v29 offset:50176\n\t"
        "v_mov_b32 v110, v93\n\t"
        "v_mov_b32 v111, v95\n\t"
        "v_perm_b32 v80, v100, v96, s72\n\t"
        "v_perm_b32 v81, v101, v97, s72\n\t"
        "v_perm_b32 v82, v108, v104, s72\n\t"
        "v_perm_b32 v83, v109, v105, s72\n\t"
        "v_perm_b32 v84, v100, v96, s73\n\t"
        "s_add_u32 s52, s58, 293888\n\t"
        "buffer_load_dwordx4 v21, s[80:83], s52 offen offset:1024 lds\n\t"
        "ds_read_b64_tr_b16 a[172:173], v28 offset:57344\n\t"
        "ds_read_b64_tr_b16 a[174:175], v28 offset:58368\n\t"
        "ds_read_b64_tr_b16 a[236:237], v29 offset:57344\n\t"
        "ds_read_b64_tr_b16 a[238:239], v29 offset:58368\n\t"
        "v_perm_b32 v85, v101, v97, s73\n\t"
        "v_perm_b32 v86, v108, v104, s73\n\t"
        "v_perm_b32 v87, v109, v105, s73\n\t"
        "v_perm_b32 v88, v102, v98, s72\n\t"
        "v_perm_b32 v89, v103, v99, s72\n\t"
        "v_perm_b32 v90, v110, v106, s72\n\t"
        "v_perm_b32 v91, v111, v107, s72\n\t"
        "s_add_u32 m0, s70, 49152\n\t"
        "s_add_u32 s52, s58, 0\n\t"
        "buffer_load_dwordx4 v21, s[84:87], s52 offen lds\n\t"
        "v_perm_b32 v92, v102, v98, s73\n\t"
        "v_perm_b32 v93, v103, v99, s73\n\t"
        "v_perm_b32 v94, v110, v106, s73\n\t"
        "v_perm_b32 v95, v111, v107, s73\n\t"
        "s_add_u32 s52, s58, 31744\n\t"
        "buffer_load_dwordx4 v21, s[84:87], s52 offen offset:1024 lds\n\t"
        "s_add_u32 m0, s70, 57344\n\t"
        "s_add_u32 s52, s58, 262144\n\t"
        "buffer_load_dwordx4 v21, s[84:87], s52 offen lds\n\t"
        "s_add_u32 s52, s58, 293888\n\t"
        "buffer_load_dwordx4 v21, s[84:87], s52 offen offset:1024 lds\n\t"
        "s_waitcnt vmcnt(16)\n\t"
        "s_waitcnt lgkmcnt(0)\n\t"
        "s_barrier\n\t"
        "s_add_u32 m0, s70, 81920\n\t"
        "s_add_u32 s52, s58, 524288\n\t"
        "buffer_load_dwordx4 v21, s[80:83], s52 offen lds\n\t"
        "ds_read_b64_tr_b16 a[48:49], v26 offset:0\n\t"
        "ds_read_b64_tr_b16 a[50:51], v26 offset:1024\n\t"
        "ds_read_b64_tr_b16 a[112:113], v27 offset:0\n\t"
        "ds_read_b64_tr_b16 a[114:115], v27 offset:1024\n\t"
        "s_add_u32 s52, s58, 556032\n\t"
        "buffer_load_dwordx4 v21, s[80:83], s52 offen offset:1024 lds\n\t"
        "ds_read_b64_tr_b16 a[52:53], v26 offset:8192\n\t"
        "ds_read_b64_tr_b16 a[54:55], v26 offset:9216\n\t"
        "ds_read_b64_tr_b16 a[116:117], v27 offset:8192\n\t"
        "ds_read_b64_tr_b16 a[118:119], v27 offset:9216\n\t"
        "s_add_u32 m0, s70, 90112\n\t"
        "s_add_u32 s52, s58, 786432\n\t"
        "buffer_load_dwordx4 v21, s[80:83], s52 offen lds\n\t"
        "ds_read_b64_tr_b16 a[176:177], v26 offset:32768\n\t"
        "ds_read_b64_tr_b16 a[178:179], v26 offset:33792\n\t"
        "ds_read_b64_tr_b16 a[240:241], v27 offset:32768\n\t"
        "ds_read_b64_tr_b16 a[242:243], v27 offset:33792\n\t"
        "s_add_u32 s52, s58, 818176\n\t"
        "buffer_load_dwordx4 v21, s[80:83], s52 offen offset:1024 lds\n\t"
        "ds_read_b64_tr_b16 a[180:181], v26 offset:40960\n\t"
        "ds_read_b64_tr_b16 a[182:183], v26 offset:41984\n\t"
        "ds_read_b64_tr_b16 a[244:245], v27 offset:40960\n\t"
        "ds_read_b64_tr_b16 a[246:247], v27 offset:41984\n\t"
        "s_add_u32 m0, s70, 114688\n\t"
        "s_add_u32 s52, s58, 524288\n\t"
        "buffer_load_dwordx4 v21, s[84:87], s52 offen lds\n\t"
        "s_add_u32 s52, s58, 556032\n\t"
        "buffer_load_dwordx4 v21, s[84:87], s52 offen offset:1024 lds\n\t"
        "s_add_u32 m0, s70, 122880\n\t"
        "s_add_u32 s52, s58, 786432\n\t"
        "buffer_load_dwordx4 v21, s[84:87], s52 offen lds\n\t"
        "s_add_u32 s52, s58, 818176\n\t"
        "buffer_load_dwordx4 v21, s[84:87], s52 offen offset:1024 lds\n\t"
        "s_waitcnt vmcnt(16)\n\t"
        "s_waitcnt lgkmcnt(0)\n\t"
        "s_barrier\n\t"
        "ds_read_b64_tr_b16 a[56:57], v28 offset:0\n\t"
        "ds_read_b64_tr_b16 a[58:59], v28 offset:1024\n\t"
        "ds_read_b64_tr_b16 a[120:121], v29 offset:0\n\t"
        "ds_read_b64_tr_b16 a[122:123], v29 offset:1024\n\t"
        "ds_read_b64_tr_b16 a[60:61], v28 offset:8192\n\t"
        "ds_read_b64_tr_b16 a[62:63], v28 offset:9216\n\t"
        "ds_read_b64_tr_b16 a[124:125], v29 offset:8192\n\t"
        "ds_read_b64_tr_b16 a[126:127], v29 offset:9216\n\t"
        "ds_read_b64_tr_b16 a[184:185], v28 offset:32768\n\t"
        "ds_read_b64_tr_b16 a[186:187], v28 offset:33792\n\t"
        "ds_read_b64_tr_b16 a[248:249], v29 offset:32768\n\t"
        "ds_read_b64_tr_b16 a[250:251], v29 offset:33792\n\t"
        "ds_read_b64_tr_b16 a[188:189], v28 offset:40960\n\t"
        "ds_read_b64_tr_b16 a[190:191], v28 offset:41984\n\t"
        "ds_read_b64_tr_b16 a[252:253], v29 offset:40960\n\t"
        "ds_read_b64_tr_b16 a[254:255], v29 offset:41984\n\t"
        "s_waitcnt vmcnt(8)\n\t"
        "s_waitcnt lgkmcnt(0)\n\t"
        "s_barrier\n\t"
        "ds_read_b64_tr_b16 v[96:97], v26 offset:16384\n\t"
        "ds_read_b64_tr_b16 v[98:99], v26 offset:17408\n\t"
        "ds_read_b64_tr_b16 v[100:101], v27 offset:16384\n\t"
        "ds_read_b64_tr_b16 v[102:103], v27 offset:17408\n\t"
        "ds_read_b64_tr_b16 v[104:105], v26 offset:24576\n\t"
        "ds_read_b64_tr_b16 v[106:107], v26 offset:25600\n\t"
        "ds_read_b64_tr_b16 v[108:109], v27 offset:24576\n\t"
        "ds_read_b64_tr_b16 v[110:111], v27 offset:25600\n\t"
        "ds_read_b64_tr_b16 v[112:113], v26 offset:49152\n\t"
        "ds_read_b64_tr_b16 v[114:115], v26 offset:50176\n\t"
        "ds_read_b64_tr_b16 v[116:117], v27 offset:49152\n\t"
        "ds_read_b64_tr_b16 v[118:119], v27 offset:50176\n\t"
        "ds_read_b64_tr_b16 v[120:121], v26 offset:57344\n\t"
        "ds_read_b64_tr_b16 v[122:123], v26 offset:58368\n\t"
        "ds_read_b64_tr_b16 v[124:125], v27 offset:57344\n\t"
        "ds_read_b64_tr_b16 v[126:127], v27 offset:58368\n\t"
        "s_waitcnt vmcnt(0)\n\t"
        "s_waitcnt lgkmcnt(0)\n\t"
        "s_barrier\n\t"
        "ds_write_b128 v1, v[96:99] offset:0\n\t"
        "ds_write_b128 v1, v[100:103] offset:4096\n\t"
        "ds_write_b128 v1, v[104:107] offset:1024\n\t"
        "ds_write_b128 v1, v[108:111] offset:5120\n\t"
        "ds_write_b128 v1, v[112:115] offset:8192\n\t"
        "ds_write_b128 v1, v[116:119] offset:12288\n\t"
        "ds_write_b128 v1, v[120:123] offset:9216\n\t"
        "ds_write_b128 v1, v[124:127] offset:13312\n\t"
        "ds_read_b64_tr_b16 v[96:97], v28 offset:16384\n\t"
        "ds_read_b64_tr_b16 v[98:99], v28 offset:17408\n\t"
        "ds_read_b64_tr_b16 v[100:101], v29 offset:16384\n\t"
        "ds_read_b64_tr_b16 v[102:103], v29 offset:17408\n\t"
        "ds_read_b64_tr_b16 v[104:105], v28 offset:24576\n\t"
        "ds_read_b64_tr_b16 v[106:107], v28 offset:25600\n\t"
        "ds_read_b64_tr_b16 v[108:109], v29 offset:24576\n\t"
        "ds_read_b64_tr_b16 v[110:111], v29 offset:25600\n\t"
        "ds_read_b64_tr_b16 v[112:113], v28 offset:49152\n\t"
        "ds_read_b64_tr_b16 v[114:115], v28 offset:50176\n\t"
        "ds_read_b64_tr_b16 v[116:117], v29 offset:49152\n\t"
        "ds_read_b64_tr_b16 v[118:119], v29 offset:50176\n\t"
        "ds_read_b64_tr_b16 v[120:121], v28 offset:57344\n\t"
        "ds_read_b64_tr_b16 v[122:123], v28 offset:58368\n\t"
        "ds_read_b64_tr_b16 v[124:125], v29 offset:57344\n\t"
        "ds_read_b64_tr_b16 v[126:127], v29 offset:58368\n\t"
        "s_waitcnt lgkmcnt(0)\n\t"
        "s_barrier\n\t"
        "ds_write_b128 v1, v[96:99] offset:2048\n\t"
        "ds_write_b128 v1, v[100:103] offset:6144\n\t"
        "ds_write_b128 v1, v[104:107] offset:3072\n\t"
        "ds_write_b128 v1, v[108:111] offset:7168\n\t"
        "ds_write_b128 v1, v[112:115] offset:10240\n\t"
        "ds_write_b128 v1, v[116:119] offset:14336\n\t"
        "ds_write_b128 v1, v[120:123] offset:11264\n\t"
        "ds_write_b128 v1, v[124:127] offset:15360\n\t"
        "v_and_b32 v26, 0xf0, v0\n\t"
        "v_lshrrev_b32 v27, 4, v0\n\t"
        "v_and_b32 v27, 0x30, v27\n\t"
        "v_xor_b32 v27, v27, v26\n\t"
        "v_lshl_add_u32 v26, v26, 4, v27\n\t"
        "v_xor_b32 v27, 64, v26\n\t"
        "v_xor_b32 v28, 128, v26\n\t"
        "v_xor_b32 v29, 192, v26\n\t"
        "v_lshlrev_b32 v126, 10, v0\n\t"
        "v_and_b32 v126, 0x4000, v126\n\t"
        "v_lshrrev_b32 v24, 4, v9\n\t"
        "v_xor_b32 v24, v24, v0\n\t"
        "v_and_b32 v25, 0x380, v24\n\t"
        "v_and_b32 v24, 0x70, v24\n\t"
        "v_lshl_or_b32 v24, v25, 3, v24\n\t"
        "ds_write_b128 v1, v[32:35] offset:16384\n\t"
        "ds_write_b128 v1, v[36:39] offset:18432\n\t"
        "ds_write_b128 v1, v[40:43] offset:20480\n\t"
        "ds_write_b128 v1, v[44:47] offset:22528\n\t"
        "ds_write_b128 v1, v[48:51] offset:17408\n\t"
        "ds_write_b128 v1, v[52:55] offset:19456\n\t"
        "ds_write_b128 v1, v[56:59] offset:21504\n\t"
        "ds_write_b128 v1, v[60:63] offset:23552\n\t"
        "ds_write_b128 v1, v[64:67] offset:24576\n\t"
        "ds_write_b128 v1, v[68:71] offset:26624\n\t"
        "ds_write_b128 v1, v[72:75] offset:28672\n\t"
        "ds_write_b128 v1, v[76:79] offset:30720\n\t"
        "ds_write_b128 v1, v[80:83] offset:25600\n\t"
        "ds_write_b128 v1, v[84:87] offset:27648\n\t"
        "ds_write_b128 v1, v[88:91] offset:29696\n\t"
        "ds_write_b128 v1, v[92:95] offset:31744\n\t"
        "s_waitcnt lgkmcnt(0)\n\t"
        "ds_read_b128 v[30:33], v1 offset:0\n\t"
        "ds_read_b128 v[34:37], v1 offset:4096\n\t"
        "ds_read_b128 v[38:41], v1 offset:8192\n\t"
        "ds_read_b128 v[42:45], v1 offset:12288\n\t"
        "s_memrealtime s[2:3]\n\t"
        "s_memtime s[22:23]\n\t"
        "s_waitcnt lgkmcnt(0)\n\t"
        "s_mul_i32 s25, s2, 5\n\t"
        "s_sub_u32 s25, s25, 90\n\t"
        "s_mov_b32 s30, s25\n\t"
        "v_lshlrev_b32 v20, 19, v0\n\t"
        "v_add_u32 v20, 0x1fff80, v20\n\t"
        "s_mov_b64 exec, 0x39\n\t"
        "buffer_load_dword v127, v20, s[40:43], 0 offen sc1\n\t"
        "s_mov_b64 exec, -1\n\t"
        "buffer_load_dword v127, off, s[40:43], s78 sc1\n\t"
        "s_cmp_eq_u32 s46, 0\n\t"
        "s_cbranch_scc1 Ltask_%=\n\t"
        "s_mov_b32 s54, 0\n\t"
        "Lg1_%=:\n\t"
        "buffer_load_dword v20, off, s[40:43], s78 sc1\n\t"
        "s_waitcnt vmcnt(0)\n\t"
        "v_readfirstlane_b32 s49, v20\n\t"
        "s_nop 0\n\t"
        "s_xor_b32 s49, s49, s79\n\t"
        "s_cmp_lt_u32 s49, 2\n\t"
        "s_cbranch_scc1 Lg1d_%=\n\t"
        "s_sleep 4\n\t"
        "s_add_u32 s54, s54, 1\n\t"
        "s_cmp_lt_u32 s54, s53\n\t"
        "s_cbranch_scc1 Lg1_%=\n\t"
        "Lg1d_%=:\n\t"
        "s_mov_b32 s54, 0\n\t"
        "s_and_b32 s49, s49, 1\n\t"
        "s_lshl_b32 s49, s49, 13\n\t"
        "s_xor_b32 s12, s12, s49\n\t"
        "s_xor_b32 s31, s31, s49\n\t"
        "s_xor_b32 s74, s74, s49\n\t"
        "s_xor_b32 s77, s77, s49\n\t"
        "s_xor_b32 s76, s76, s49\n\t"
        "s_branch Ldef0_%=\n\t"
        ".p2align 6\n\t"
        "Ltask_%=:\n\t"
        "s_barrier\n\t"
        "ds_read_b128 v[98:101], v26 offset:0\n\t"
        "ds_read_b128 v[102:105], v28 offset:0\n\t"
        "ds_read_b128 v[106:109], v27 offset:0\n\t"
        "ds_read_b128 v[110:113], v29 offset:0\n\t"
        "s_add_u32 m0, s35, 23808\n\t"
        "s_not_b32 exec_lo, s56\n\t"
        "s_not_b32 exec_hi, s56\n\t"
        "buffer_load_dwordx4 v24, s[40:43], s74 offen offset:768 sc1 lds\n\t"
        "s_mov_b64 exec, -1\n\t"
        "s_waitcnt lgkmcnt(3)\n\t"
        "v_mfma_f32_16x16x32_bf16 v[82:85], v[30:33], v[98:101], 0\n\t"
        "ds_read_b128 v[114:117], v26 offset:4096\n\t"
        "s_waitcnt lgkmcnt(3)\n\t"
        "v_mfma_f32_16x16x32_bf16 v[90:93], v[30:33], v[102:105], 0\n\t"
        "ds_read_b128 v[30:33], v1 offset:1024\n\t"
        "v_mfma_f32_16x16x32_bf16 v[86:89], v[34:37], v[98:101], 0\n\t"
        "ds_read_b128 v[118:121], v28 offset:4096\n\t"
        "v_mfma_f32_16x16x32_bf16 v[94:97], v[34:37], v[102:105], 0\n\t"
        "ds_read_b128 v[34:37], v1 offset:5120\n\t"
        "v_mfma_f32_16x16x32_bf16 v[66:69], v[38:41], v[98:101], 0\n\t"
        "v_mfma_f32_16x16x32_bf16 v[74:77], v[38:41], v[102:105], 0\n\t"
        "ds_read_b128 v[38:41], v1 offset:9216\n\t"
        "v_mfma_f32_16x16x32_bf16 v[70:73], v[42:45], v[98:101], 0\n\t"
        "v_mfma_f32_16x16x32_bf16 v[78:81], v[42:45], v[102:105], 0\n\t"
        "ds_read_b128 v[42:45], v1 offset:13312\n\t"
        "v_mfma_f32_16x16x32_bf16 v[50:53], v[98:101], v[128:131], 0\n\t"
        "v_mfma_f32_16x16x32_bf16 v[54:57], v[102:105], v[128:131], 0\n\t"
        "v_mfma_f32_16x16x32_bf16 v[58:61], v[98:101], v[192:195], 0\n\t"
        "v_mfma_f32_16x16x32_bf16 v[62:65], v[102:105], v[192:195], 0\n\t"
        "s_waitcnt lgkmcnt(4)\n\t"
        "v_mfma_f32_16x16x32_bf16 v[82:85], v[30:33], v[106:109], v[82:85]\n\t"
        "ds_read_b128 v[98:101], v27 offset:4096\n\t"
        "v_mfma_f32_16x16x32_bf16 v[90:93], v[30:33], v[110:113], v[90:93]\n\t"
        "ds_read_b128 v[30:33], v1 offset:2048\n\t"
        "s_waitcnt lgkmcnt(4)\n\t"
        "v_mfma_f32_16x16x32_bf16 v[86:89], v[34:37], v[106:109], v[86:89]\n\t"
        "ds_read_b128 v[102:105], v29 offset:4096\n\t"
        "v_mfma_f32_16x16x32_bf16 v[94:97], v[34:37], v[110:113], v[94:97]\n\t"
        "ds_read_b128 v[34:37], v1 offset:6144\n\t"
        "s_waitcnt lgkmcnt(5)\n\t"
        "v_mfma_f32_16x16x32_bf16 v[66:69], v[38:41], v[106:109], v[66:69]\n\t"
        "v_mfma_f32_16x16x32_bf16 v[74:77], v[38:41], v[110:113], v[74:77]\n\t"
        "ds_read_b128 v[38:41], v1 offset:10240\n\t"
        "s_waitcnt lgkmcnt(5)\n\t"
        "v_mfma_f32_16x16x32_bf16 v[70:73], v[42:45], v[106:109], v[70:73]\n\t"
        "v_mfma_f32_16x16x32_bf16 v[78:81], v[42:45], v[110:113], v[78:81]\n\t"
        "ds_read_b128 v[42:45], v1 offset:14336\n\t"
        "v_mfma_f32_16x16x32_bf16 v[50:53], v[106:109], v[132:135], v[50:53]\n\t"
        "v_mfma_f32_16x16x32_bf16 v[54:57], v[110:113], v[132:135], v[54:57]\n\t"
        "v_mfma_f32_16x16x32_bf16 v[58:61], v[106:109], v[196:199], v[58:61]\n\t"
        "v_mfma_f32_16x16x32_bf16 v[62:65], v[110:113], v[196:199], v[62:65]\n\t"
        "s_waitcnt lgkmcnt(4)\n\t"
        "v_mfma_f32_16x16x32_bf16 v[82:85], v[30:33], v[114:117], v[82:85]\n\t"
        "ds_read_b128 v[106:109], v26 offset:8192\n\t"
        "v_mfma_f32_16x16x32_bf16 v[90:93], v[30:33], v[118:121], v[90:93]\n\t"
        "ds_read_b128 v[30:33], v1 offset:3072\n\t"
        "s_waitcnt lgkmcnt(4)\n\t"
        "v_mfma_f32_16x16x32_bf16 v[86:89], v[34:37], v[114:117], v[86:89]\n\t"
        "ds_read_b128 v[110:113], v28 offset:8192\n\t"
        "v_mfma_f32_16x16x32_bf16 v[94:97], v[34:37], v[118:121], v[94:97]\n\t"
        "ds_read_b128 v[34:37], v1 offset:7168\n\t"
        "s_waitcnt lgkmcnt(5)\n\t"
        "v_mfma_f32_16x16x32_bf16 v[66:69], v[38:41], v[114:117], v[66:69]\n\t"
        "v_mfma_f32_16x16x32_bf16 v[74:77], v[38:41], v[118:121], v[74:77]\n\t"
        "ds_read_b128 v[38:41], v1 offset:11264\n\t"
        "s_waitcnt lgkmcnt(5)\n\t"
        "v_mfma_f32_16x16x32_bf16 v[70:73], v[42:45], v[114:117], v[70:73]\n\t"
        "v_mfma_f32_16x16x32_bf16 v[78:81], v[42:45], v[118:121], v[78:81]\n\t"
        "ds_read_b128 v[42:45], v1 offset:15360\n\t"
        "v_mfma_f32_16x16x32_bf16 v[50:53], v[114:117], v[136:139], v[50:53]\n\t"
        "s_memrealtime s[72:73]\n\t"
        "v_lshlrev_b32 v20, 3, v0\n\t"
        "v_mfma_f32_16x16x32_bf16 v[54:57], v[118:121], v[136:139], v[54:57]\n\t"
        "s_sub_i32 s28, s39, 33554432\n\t"
        "v_mfma_f32_16x16x32_bf16 v[58:61], v[114:117], v[200:203], v[58:61]\n\t"
        "s_max_i32 s28, s28, 0\n\t"
        "v_mfma_f32_16x16x32_bf16 v[62:65], v[118:121], v[200:203], v[62:65]\n\t"
        "s_waitcnt lgkmcnt(0)\n\t"
        "s_and_b32 s72, s72, 0xffffff\n\t"
        "s_or_b32 s72, s72, s28\n\t"
        "v_mov_b32 v21, s72\n\t"
        "v_mfma_f32_16x16x32_bf16 v[82:85], v[30:33], v[98:101], v[82:85]\n\t"
        "ds_read_b128 v[114:117], v27 offset:8192\n\t"
        "v_mfma_f32_16x16x32_bf16 v[90:93], v[30:33], v[102:105], v[90:93]\n\t"
        "v_mfma_f32_16x16x32_bf16 v[86:89], v[34:37], v[98:101], v[86:89]\n\t"
        "ds_read_b128 v[118:121], v29 offset:8192\n\t"
        "v_mfma_f32_16x16x32_bf16 v[94:97], v[34:37], v[102:105], v[94:97]\n\t"
        "s_waitcnt vmcnt(0)\n\t"
        "s_barrier\n\t"
        "v_mfma_f32_16x16x32_bf16 v[66:69], v[38:41], v[98:101], v[66:69]\n\t"
        "s_mov_b64 exec, 3\n\t"
        "buffer_store_dword v21, v20, s[40:43], s76 offen sc1\n\t"
        "s_mov_b64 exec, -1\n\t"
        "s_nop 0\n\t"
        "v_mfma_f32_16x16x32_bf16 v[74:77], v[38:41], v[102:105], v[74:77]\n\t"
        "v_mfma_f32_16x16x32_bf16 v[70:73], v[42:45], v[98:101], v[70:73]\n\t"
        "v_mfma_f32_16x16x32_bf16 v[78:81], v[42:45], v[102:105], v[78:81]\n\t"
        "v_mfma_f32_16x16x32_bf16 v[50:53], v[98:101], v[140:143], v[50:53]\n\t"
        "v_mfma_f32_16x16x32_bf16 v[54:57], v[102:105], v[140:143], v[54:57]\n\t"
        "v_mfma_f32_16x16x32_bf16 v[58:61], v[98:101], v[204:207], v[58:61]\n\t"
        "v_mfma_f32_16x16x32_bf16 v[62:65], v[102:105], v[204:207], v[62:65]\n\t"
        "v_mfma_f32_16x16x32_bf16 v[82:85], a[16:19], v[106:109], v[82:85]\n\t"
        "ds_read_b128 v[98:101], v26 offset:12288\n\t"
        "v_mfma_f32_16x16x32_bf16 v[90:93], a[16:19], v[110:113], v[90:93]\n\t"
        "v_mfma_f32_16x16x32_bf16 v[86:89], a[80:83], v[106:109], v[86:89]\n\t"
        "ds_read_b128 v[102:105], v28 offset:12288\n\t"
        "v_mfma_f32_16x16x32_bf16 v[94:97], a[80:83], v[110:113], v[94:97]\n\t"
        "s_and_b32 s49, s47, 3\n\t"
        "s_lshl_b32 s49, s49, 19\n\t"
        "v_mfma_f32_16x16x32_bf16 v[66:69], a[144:147], v[106:109], v[66:69]\n\t"
        "s_add_u32 s49, s49, s12\n\t"
        "v_mfma_f32_16x16x32_bf16 v[74:77], a[144:147], v[110:113], v[74:77]\n\t"
        "s_add_u32 s49, s49, 25165824\n\t"
        "v_mfma_f32_16x16x32_bf16 v[70:73], a[208:211], v[106:109], v[70:73]\n\t"
        "s_lshl_b32 s51, s44, 17\n\t"
        "v_mfma_f32_16x16x32_bf16 v[78:81], a[208:211], v[110:113], v[78:81]\n\t"
        "s_add_u32 s59, s49, s51\n\t"
        "s_lshl_b32 s51, s45, 8\n\t"
        "v_mfma_f32_16x16x32_bf16 v[50:53], v[106:109], v[144:147], v[50:53]\n\t"
        "s_add_u32 s59, s59, s51\n\t"
        "v_mfma_f32_16x16x32_bf16 v[54:57], v[110:113], v[144:147], v[54:57]\n\t"
        "s_lshl_b32 s49, s47, 17\n\t"
        "v_mfma_f32_16x16x32_bf16 v[58:61], v[106:109], v[208:211], v[58:61]\n\t"
        "s_add_u32 s49, s49, s12\n\t"
        "v_mfma_f32_16x16x32_bf16 v[62:65], v[110:113], v[208:211], v[62:65]\n\t"
        "s_add_u32 s49, s49, 33554432\n\t"
        "s_waitcnt lgkmcnt(3)\n\t"
        "v_mfma_f32_16x16x32_bf16 v[82:85], a[20:23], v[114:117], v[82:85]\n\t"
        "ds_read_b128 v[106:109], v27 offset:12288\n\t"
        "s_waitcnt lgkmcnt(3)\n\t"
        "v_mfma_f32_16x16x32_bf16 v[90:93], a[20:23], v[118:121], v[90:93]\n\t"
        "v_mfma_f32_16x16x32_bf16 v[86:89], a[84:87], v[114:117], v[86:89]\n\t"
        "ds_read_b128 v[110:113], v29 offset:12288\n\t"
        "v_mfma_f32_16x16x32_bf16 v[94:97], a[84:87], v[118:121], v[94:97]\n\t"
        "s_lshr_b32 s51, s45, 2\n\t"
        "s_lshl_b32 s51, s51, 14\n\t"
        "v_mfma_f32_16x16x32_bf16 v[66:69], a[148:151], v[114:117], v[66:69]\n\t"
        "s_add_u32 s58, s49, s51\n\t"
        "v_mfma_f32_16x16x32_bf16 v[74:77], a[148:151], v[118:121], v[74:77]\n\t"
        "s_lshl_b32 s51, s44, 11\n\t"
        "v_mfma_f32_16x16x32_bf16 v[70:73], a[212:215], v[114:117], v[70:73]\n\t"
        "s_add_u32 s58, s58, s51\n\t"
        "v_mfma_f32_16x16x32_bf16 v[78:81], a[212:215], v[118:121], v[78:81]\n\t"
        "s_lshr_b32 s51, s58, 8\n\t"
        "s_and_b32 s51, s51, 0x2000\n\t"
        "v_mfma_f32_16x16x32_bf16 v[50:53], v[114:117], v[148:151], v[50:53]\n\t"
        "s_xor_b32 s58, s58, s51\n\t"
        "v_mfma_f32_16x16x32_bf16 v[54:57], v[118:121], v[148:151], v[54:57]\n\t"
        "s_lshl_b32 m0, s35, 1\n\t"
        "v_mfma_f32_16x16x32_bf16 v[58:61], v[114:117], v[212:215], v[58:61]\n\t"
        "s_mov_b32 s70, -2\n\t"
        "v_mfma_f32_16x16x32_bf16 v[62:65], v[118:121], v[212:215], v[62:65]\n\t"
        "s_lshl_b32 s49, s47, 7\n\t"
        "s_waitcnt lgkmcnt(3)\n\t"
        "v_mfma_f32_16x16x32_bf16 v[82:85], a[24:27], v[98:101], v[82:85]\n\t"
        "ds_read_b128 v[114:117], v26 offset:16384\n\t"
        "s_waitcnt lgkmcnt(3)\n\t"
        "v_mfma_f32_16x16x32_bf16 v[90:93], a[24:27], v[102:105], v[90:93]\n\t"
        "v_mfma_f32_16x16x32_bf16 v[86:89], a[88:91], v[98:101], v[86:89]\n\t"
        "ds_read_b128 v[118:121], v28 offset:16384\n\t"
        "v_mfma_f32_16x16x32_bf16 v[94:97], a[88:91], v[102:105], v[94:97]\n\t"
        "s_add_u32 s49, s49, s12\n\t"
        "s_add_u32 s27, s49, 29392768\n\t"
        "v_mfma_f32_16x16x32_bf16 v[66:69], a[152:155], v[98:101], v[66:69]\n\t"
        "s_add_u32 s49, s49, s38\n\t"
        "v_mfma_f32_16x16x32_bf16 v[74:77], a[152:155], v[102:105], v[74:77]\n\t"
        "s_cmp_eq_u32 s44, 0\n\t"
        "s_cselect_b32 s26, s49, 0x7ff00000\n\t"
        "s_cselect_b32 s27, s27, 0x7ff00000\n\t"
        "v_mfma_f32_16x16x32_bf16 v[70:73], a[216:219], v[98:101], v[70:73]\n\t"
        "s_cmp_eq_u32 s47, 63\n\t"
        "s_cselect_b32 s59, 0x7ff00000, s59\n\t"
        "s_cselect_b32 s21, -1.0, s21\n\t"
        "v_mfma_f32_16x16x32_bf16 v[78:81], a[216:219], v[102:105], v[78:81]\n\t"
        "s_lshl_b32 s50, s47, 5\n\t"
        "s_add_u32 s50, s50, s45\n\t"
        "v_mfma_f32_16x16x32_bf16 v[50:53], v[98:101], v[152:155], v[50:53]\n\t"
        "s_lshl_b32 s75, s50, 14\n\t"
        "v_mfma_f32_16x16x32_bf16 v[54:57], v[102:105], v[152:155], v[54:57]\n\t"
        "s_add_u32 s75, s75, s12\n\t"
        "v_mfma_f32_16x16x32_bf16 v[58:61], v[98:101], v[216:219], v[58:61]\n\t"
        "s_add_u32 s75, s75, 41943040\n\t"
        "v_mfma_f32_16x16x32_bf16 v[62:65], v[102:105], v[216:219], v[62:65]\n\t"
        "s_lshr_b32 s50, s75, 8\n\t"
        "s_waitcnt lgkmcnt(3)\n\t"
        "v_mfma_f32_16x16x32_bf16 v[82:85], a[28:31], v[106:109], v[82:85]\n\t"
        "ds_read_b128 v[98:101], v27 offset:16384\n\t"
        "s_waitcnt lgkmcnt(3)\n\t"
        "v_mfma_f32_16x16x32_bf16 v[90:93], a[28:31], v[110:113], v[90:93]\n\t"
        "v_mfma_f32_16x16x32_bf16 v[86:89], a[92:95], v[106:109], v[86:89]\n\t"
        "ds_read_b128 v[102:105], v29 offset:16384\n\t"
        "v_mfma_f32_16x16x32_bf16 v[94:97], a[92:95], v[110:113], v[94:97]\n\t"
        "s_and_b32 s50, s50, 0x2000\n\t"
        "s_xor_b32 s75, s75, s50\n\t"
        "v_mfma_f32_16x16x32_bf16 v[66:69], a[156:159], v[106:109], v[66:69]\n\t"
        "s_add_u32 s50, s47, 2\n\t"
        "v_mfma_f32_16x16x32_bf16 v[74:77], a[156:159], v[110:113], v[74:77]\n\t"
        "s_lshl_b32 s74, s50, 16\n\t"
        "v_mfma_f32_16x16x32_bf16 v[70:73], a[220:223], v[106:109], v[70:73]\n\t"
        "s_add_u32 s74, s74, s31\n\t"
        "v_mfma_f32_16x16x32_bf16 v[78:81], a[220:223], v[110:113], v[78:81]\n\t"
        "s_lshr_b32 s51, s74, 8\n\t"
        "v_mfma_f32_16x16x32_bf16 v[50:53], v[106:109], v[156:159], v[50:53]\n\t"
        "s_and_b32 s51, s51, 0x2000\n\t"
        "v_mfma_f32_16x16x32_bf16 v[54:57], v[110:113], v[156:159], v[54:57]\n\t"
        "s_xor_b32 s74, s74, s51\n\t"
        "v_mfma_f32_16x16x32_bf16 v[58:61], v[106:109], v[220:223], v[58:61]\n\t"
        "s_add_u32 s39, s39, 33554432\n\t"
        "v_mfma_f32_16x16x32_bf16 v[62:65], v[110:113], v[220:223], v[62:65]\n\t"
        "s_cmp_gt_u32 s50, 63\n\t"
        "s_cselect_b32 s74, 0x7ff00000, s74\n\t"
        "s_cselect_b32 s39, 0, s39\n\t"
        "s_waitcnt lgkmcnt(3)\n\t"
        "v_mfma_f32_16x16x32_bf16 v[82:85], a[32:35], v[114:117], v[82:85]\n\t"
        "ds_read_b128 v[106:109], v26 offset:20480\n\t"
        "s_waitcnt lgkmcnt(3)\n\t"
        "v_mfma_f32_16x16x32_bf16 v[90:93], a[32:35], v[118:121], v[90:93]\n\t"
        "v_mfma_f32_16x16x32_bf16 v[86:89], a[96:99], v[114:117], v[86:89]\n\t"
        "ds_read_b128 v[110:113], v28 offset:20480\n\t"
        "v_mfma_f32_16x16x32_bf16 v[94:97], a[96:99], v[118:121], v[94:97]\n\t"
        "v_mfma_f32_16x16x32_bf16 v[66:69], a[160:163], v[114:117], v[66:69]\n\t"
        "v_mfma_f32_16x16x32_bf16 v[74:77], a[160:163], v[118:121], v[74:77]\n\t"
        "v_mfma_f32_16x16x32_bf16 v[70:73], a[224:227], v[114:117], v[70:73]\n\t"
        "v_mfma_f32_16x16x32_bf16 v[78:81], a[224:227], v[118:121], v[78:81]\n\t"
        "v_mfma_f32_16x16x32_bf16 v[50:53], v[114:117], v[160:163], v[50:53]\n\t"
        "v_mfma_f32_16x16x32_bf16 v[54:57], v[118:121], v[160:163], v[54:57]\n\t"
        "v_mfma_f32_16x16x32_bf16 v[58:61], v[114:117], v[224:227], v[58:61]\n\t"
        "v_mfma_f32_16x16x32_bf16 v[62:65], v[118:121], v[224:227], v[62:65]\n\t"
        "s_waitcnt lgkmcnt(3)\n\t"
        "v_mfma_f32_16x16x32_bf16 v[82:85], a[36:39], v[98:101], v[82:85]\n\t"
        "ds_read_b128 v[114:117], v27 offset:20480\n\t"
        "s_waitcnt lgkmcnt(3)\n\t"
        "v_mfma_f32_16x16x32_bf16 v[90:93], a[36:39], v[102:105], v[90:93]\n\t"
        "v_mfma_f32_16x16x32_bf16 v[86:89], a[100:103], v[98:101], v[86:89]\n\t"
        "ds_read_b128 v[118:121], v29 offset:20480\n\t"
        "v_mfma_f32_16x16x32_bf16 v[94:97], a[100:103], v[102:105], v[94:97]\n\t"
        "v_bfe_u32 v24, v0, 2, 7\n\t"
        "v_mfma_f32_16x16x32_bf16 v[66:69], a[164:167], v[98:101], v[66:69]\n\t"
        "v_mul_u32_u24 v24, 4097, v24\n\t"
        "v_mfma_f32_16x16x32_bf16 v[74:77], a[164:167], v[102:105], v[74:77]\n\t"
        "v_and_b32 v24, 0xf007c, v24\n\t"
        "v_mfma_f32_16x16x32_bf16 v[70:73], a[228:231], v[98:101], v[70:73]\n\t"
        "buffer_load_dword v22, v24, s[40:43], s27 offen sc1\n\t"
        "v_mfma_f32_16x16x32_bf16 v[78:81], a[228:231], v[102:105], v[78:81]\n\t"
        "v_lshlrev_b32 v25, 7, v0\n\t"
        "v_mfma_f32_16x16x32_bf16 v[50:53], v[98:101], v[164:167], v[50:53]\n\t"
        "v_mfma_f32_16x16x32_bf16 v[54:57], v[102:105], v[164:167], v[54:57]\n\t"
        "v_mfma_f32_16x16x32_bf16 v[58:61], v[98:101], v[228:231], v[58:61]\n\t"
        "v_mfma_f32_16x16x32_bf16 v[62:65], v[102:105], v[228:231], v[62:65]\n\t"
        "s_waitcnt lgkmcnt(3)\n\t"
        "v_mfma_f32_16x16x32_bf16 v[82:85], a[40:43], v[106:109], v[82:85]\n\t"
        "ds_read_b128 v[98:101], v26 offset:24576\n\t"
        "s_waitcnt lgkmcnt(3)\n\t"
        "v_mfma_f32_16x16x32_bf16 v[90:93], a[40:43], v[110:113], v[90:93]\n\t"
        "v_mfma_f32_16x16x32_bf16 v[86:89], a[104:107], v[106:109], v[86:89]\n\t"
        "ds_read_b128 v[102:105], v28 offset:24576\n\t"
        "v_mfma_f32_16x16x32_bf16 v[94:97], a[104:107], v[110:113], v[94:97]\n\t"
        "s_add_u32 s30, s25, 90\n\t"
        "v_mfma_f32_16x16x32_bf16 v[66:69], a[168:171], v[106:109], v[66:69]\n\t"
        "s_mov_b32 s24, s22\n\t"
        "v_mfma_f32_16x16x32_bf16 v[74:77], a[168:171], v[110:113], v[74:77]\n\t"
        "v_mfma_f32_16x16x32_bf16 v[70:73], a[232:235], v[106:109], v[70:73]\n\t"
        "v_mfma_f32_16x16x32_bf16 v[78:81], a[232:235], v[110:113], v[78:81]\n\t"
        "v_mfma_f32_16x16x32_bf16 v[50:53], v[106:109], v[168:171], v[50:53]\n\t"
        "v_mfma_f32_16x16x32_bf16 v[54:57], v[110:113], v[168:171], v[54:57]\n\t"
        "v_mfma_f32_16x16x32_bf16 v[58:61], v[106:109], v[232:235], v[58:61]\n\t"
        "v_mfma_f32_16x16x32_bf16 v[62:65], v[110:113], v[232:235], v[62:65]\n\t"
        "s_waitcnt lgkmcnt(3)\n\t"
        "v_mfma_f32_16x16x32_bf16 v[82:85], a[44:47], v[114:117], v[82:85]\n\t"
        "ds_read_b128 v[106:109], v27 offset:24576\n\t"
        "s_waitcnt lgkmcnt(3)\n\t"
        "v_mfma_f32_16x16x32_bf16 v[90:93], a[44:47], v[118:121], v[90:93]\n\t"
        "v_mfma_f32_16x16x32_bf16 v[86:89], a[108:111], v[114:117], v[86:89]\n\t"
        "ds_read_b128 v[110:113], v29 offset:24576\n\t"
        "v_mfma_f32_16x16x32_bf16 v[94:97], a[108:111], v[118:121], v[94:97]\n\t"
        "v_mfma_f32_16x16x32_bf16 v[66:69], a[172:175], v[114:117], v[66:69]\n\t"
        "v_mfma_f32_16x16x32_bf16 v[74:77], a[172:175], v[118:121], v[74:77]\n\t"
        "v_mfma_f32_16x16x32_bf16 v[70:73], a[236:239], v[114:117], v[70:73]\n\t"
        "v_mfma_f32_16x16x32_bf16 v[78:81], a[236:239], v[118:121], v[78:81]\n\t"
        "v_mfma_f32_16x16x32_bf16 v[50:53], v[114:117], v[172:175], v[50:53]\n\t"
        "v_mfma_f32_16x16x32_bf16 v[54:57], v[118:121], v[172:175], v[54:57]\n\t"
        "v_mfma_f32_16x16x32_bf16 v[58:61], v[114:117], v[236:239], v[58:61]\n\t"
        "v_mfma_f32_16x16x32_bf16 v[62:65], v[118:121], v[236:239], v[62:65]\n\t"
        "s_waitcnt lgkmcnt(3)\n\t"
        "v_mfma_f32_16x16x32_bf16 v[82:85], a[48:51], v[98:101], v[82:85]\n\t"
        "ds_read_b128 v[114:117], v26 offset:28672\n\t"
        "s_waitcnt lgkmcnt(3)\n\t"
        "v_mfma_f32_16x16x32_bf16 v[90:93], a[48:51], v[102:105], v[90:93]\n\t"
        "v_mfma_f32_16x16x32_bf16 v[86:89], a[112:115], v[98:101], v[86:89]\n\t"
        "ds_read_b128 v[118:121], v28 offset:28672\n\t"
        "v_mfma_f32_16x16x32_bf16 v[94:97], a[112:115], v[102:105], v[94:97]\n\t"
        "v_mfma_f32_16x16x32_bf16 v[66:69], a[176:179], v[98:101], v[66:69]\n\t"
        "v_mfma_f32_16x16x32_bf16 v[74:77], a[176:179], v[102:105], v[74:77]\n\t"
        "v_mfma_f32_16x16x32_bf16 v[70:73], a[240:243], v[98:101], v[70:73]\n\t"
        "v_mfma_f32_16x16x32_bf16 v[78:81], a[240:243], v[102:105], v[78:81]\n\t"
        "v_mfma_f32_16x16x32_bf16 v[50:53], v[98:101], v[176:179], v[50:53]\n\t"
        "v_mfma_f32_16x16x32_bf16 v[54:57], v[102:105], v[176:179], v[54:57]\n\t"
        "v_mfma_f32_16x16x32_bf16 v[58:61], v[98:101], v[240:243], v[58:61]\n\t"
        "v_mfma_f32_16x16x32_bf16 v[62:65], v[102:105], v[240:243], v[62:65]\n\t"
        "s_waitcnt lgkmcnt(3)\n\t"
        "v_mfma_f32_16x16x32_bf16 v[66:69], a[180:183], v[106:109], v[66:69]\n\t"
        "ds_read_b128 v[98:101], v27 offset:28672\n\t"
        "s_waitcnt vmcnt(0)\n\t"
        "s_waitcnt lgkmcnt(3)\n\t"
        "v_max_u32_dpp v22, v22, v22 row_ror:1 row_mask:0xf bank_mask:0xf\n\t"
        "v_mfma_f32_16x16x32_bf16 v[74:77], a[180:183], v[110:113], v[74:77]\n\t"
        "s_nop 0\n\t"
        "v_max_u32_dpp v22, v22, v22 row_ror:2 row_mask:0xf bank_mask:0xf\n\t"
        "s_nop 0\n\t"
        "v_mfma_f32_16x16x32_bf16 v[70:73], a[244:247], v[106:109], v[70:73]\n\t"
        "ds_read_b128 v[102:105], v29 offset:28672\n\t"
        "v_max_u32_dpp v22, v22, v22 row_ror:4 row_mask:0xf bank_mask:0xf\n\t"
        "v_mfma_f32_16x16x32_bf16 v[78:81], a[244:247], v[110:113], v[78:81]\n\t"
        "s_nop 0\n\t"
        "s_waitcnt lgkmcnt(3)\n\t"
        "v_max_u32_dpp v22, v22, v22 row_ror:8 row_mask:0xf bank_mask:0xf\n\t"
        "v_mov_b32 v23, v22\n\t"
        "v_mfma_f32_16x16x32_bf16 v[66:69], a[184:187], v[114:117], v[66:69]\n\t"
        "s_nop 0\n\t"
        "s_waitcnt lgkmcnt(2)\n\t"
        "v_permlane16_swap_b32 v22, v23\n\t"
        "v_mfma_f32_16x16x32_bf16 v[74:77], a[184:187], v[118:121], v[74:77]\n\t"
        "v_max_u32 v23, v22, v23\n\t"
        "v_mfma_f32_16x16x32_bf16 v[70:73], a[248:251], v[114:117], v[70:73]\n\t"
        "s_mov_b64 exec, s[14:15]\n\t"
        "ds_write_b32 v25, v23 offset:0\n\t"
        "s_mov_b64 exec, -1\n\t"
        "s_nop 0\n\t"
        "v_mfma_f32_16x16x32_bf16 v[78:81], a[248:251], v[118:121], v[78:81]\n\t"
        "s_waitcnt lgkmcnt(2)\n\t"
        "v_mfma_f32_16x16x32_bf16 v[66:69], a[188:191], v[98:101], v[66:69]\n\t"
        "s_waitcnt lgkmcnt(1)\n\t"
        "v_mfma_f32_16x16x32_bf16 v[74:77], a[188:191], v[102:105], v[74:77]\n\t"
        "s_waitcnt lgkmcnt(0)\n\t"
        "s_barrier\n\t"
        "v_mfma_f32_16x16x32_bf16 v[70:73], a[252:255], v[98:101], v[70:73]\n\t"
        "s_memrealtime s[2:3]\n\t"
        "ds_read_b32 v19, v2 offset:0\n\t"
        "v_mfma_f32_16x16x32_bf16 v[78:81], a[252:255], v[102:105], v[78:81]\n\t"
        "s_memrealtime s[4:5]\n\t"
        "v_mfma_f32_16x16x32_bf16 v[82:85], a[52:55], v[106:109], v[82:85]\n\t"
        "s_memrealtime s[6:7]\n\t"
        "s_memtime s[22:23]\n\t"
        "v_mfma_f32_16x16x32_bf16 v[90:93], a[52:55], v[110:113], v[90:93]\n\t"
        "s_memrealtime s[8:9]\n\t"
        "v_mul_f32 v66, s55, v66\n\t"
        "v_mul_f32 v67, s55, v67\n\t"
        "v_mfma_f32_16x16x32_bf16 v[86:89], a[116:119], v[106:109], v[86:89]\n\t"
        "s_memrealtime s[10:11]\n\t"
        "v_mul_f32 v68, s55, v68\n\t"
        "v_mul_f32 v69, s55, v69\n\t"
        "v_mul_f32 v74, s55, v74\n\t"
        "v_mul_f32 v76, s55, v76\n\t"
        "v_mul_f32 v70, s55, v70\n\t"
        "v_mul_f32 v81, s55, v81\n\t"
        "v_mfma_f32_16x16x32_bf16 v[82:85], a[60:63], v[98:101], v[82:85]\n\t"
        "v_mul_f32 v71, s55, v71\n\t"
        "v_cvt_pk_bf16_f32 v40, v70, v71\n\t"
        "v_mul_f32 v80, s55, v80\n\t"
        "v_cvt_pk_bf16_f32 v38, v66, v67\n\t"
        "v_mul_f32 v77, s55, v77\n\t"
        "v_cvt_pk_bf16_f32 v45, v80, v81\n\t"
        "v_cvt_pk_bf16_f32 v39, v68, v69\n\t"
        "v_mfma_f32_16x16x32_bf16 v[58:61], v[106:109], v[244:247], v[58:61]\n\t"
        "s_waitcnt lgkmcnt(0)\n\t"
        "s_add_u32 s2, s2, s4\n\t"
        "v_mul_f32 v72, s55, v72\n\t"
        "v_mfma_f32_16x16x32_bf16 v[86:89], a[120:123], v[114:117], v[86:89]\n\t"
        "s_add_u32 s6, s6, s8\n\t"
        "v_cvt_pk_bf16_f32 v43, v76, v77\n\t"
        "v_readfirstlane_b32 s28, v19\n\t"
        "v_mfma_f32_16x16x32_bf16 v[82:85], a[56:59], v[114:117], v[82:85]\n\t"
        "ds_read_b128 v[66:69], v1 offset:2048\n\t"
        "v_mul_f32 v79, s55, v79\n\t"
        "v_mfma_f32_16x16x32_bf16 v[86:89], a[124:127], v[98:101], v[86:89]\n\t"
        "s_add_u32 s2, s2, s10\n\t"
        "s_or_b32 s28, s28, s56\n\t"
        "s_add_u32 s2, s2, s6\n\t"
        "v_mfma_f32_16x16x32_bf16 v[54:57], v[102:105], v[188:191], v[54:57]\n\t"
        "s_add_u32 s5, s28, s19\n\t"
        "v_cvt_pk_bf16_f32 v30, v82, v83\n\t"
        "s_sub_u32 s4, s5, s2\n\t"
        "v_mfma_f32_16x16x32_bf16 v[58:61], v[114:117], v[248:251], v[58:61]\n\t"
        "s_sub_u32 s30, s2, s30\n\t"
        "v_cvt_f32_u32 v18, s30\n\t"
        "v_cvt_pk_bf16_f32 v31, v84, v85\n\t"
        "v_mul_f32 v78, s55, v78\n\t"
        "v_mfma_f32_16x16x32_bf16 v[90:93], a[56:59], v[118:121], v[90:93]\n\t"
        "s_cmp_lt_u32 s4, 6000\n\t"
        "s_cselect_b32 s4, s4, 0\n\t"
        "v_mul_f32 v75, s55, v75\n\t"
        "v_mfma_f32_16x16x32_bf16 v[94:97], a[120:123], v[118:121], v[94:97]\n\t"
        "s_mul_i32 s6, s4, s18\n\t"
        "v_cvt_pk_bf16_f32 v42, v74, v75\n\t"
        "v_cvt_pk_bf16_f32 v32, v86, v87\n\t"
        "s_lshr_b32 s6, s6, 9\n\t"
        "v_mfma_f32_16x16x32_bf16 v[54:57], v[118:121], v[184:187], v[54:57]\n\t"
        "s_min_u32 s6, s6, 64\n\t"
        "v_cvt_pk_bf16_f32 v44, v78, v79\n\t"
        "s_lshl_b32 s6, s6, 2\n\t"
        "v_mfma_f32_16x16x32_bf16 v[90:93], a[60:63], v[102:105], v[90:93]\n\t"
        "s_sub_u32 s24, s22, s24\n\t"
        "v_mul_f32 v73, s55, v73\n\t"
        "v_cvt_pk_bf16_f32 v41, v72, v73\n\t"
        "v_mfma_f32_16x16x32_bf16 v[50:53], v[114:117], v[184:187], v[50:53]\n\t"
        "ds_read_b128 v[78:81], v1 offset:7168\n\t"
        "v_cvt_pk_bf16_f32 v33, v88, v89\n\t"
        "v_mfma_f32_16x16x32_bf16 v[58:61], v[98:101], v[252:255], v[58:61]\n\t"
        "s_add_u32 s25, s2, s4\n\t"
        "v_rcp_f32 v20, v18\n\t"
        "v_mfma_f32_16x16x32_bf16 v[54:57], v[110:113], v[180:183], v[54:57]\n\t"
        "s_sub_u32 s6, s60, s6\n\t"
        "s_subb_u32 s7, s61, 0\n\t"
        "s_cmp_eq_u32 s28, -1\n\t"
        "s_cselect_b32 s4, -1, s4\n\t"
        "s_cmp_gt_u32 s4, 96\n\t"
        "s_cbranch_scc1 Lslow_%=\n\t"
        "s_setpc_b64 s[6:7]\n\t"
        ".fill 64, 4, 0xbf800001\n\t"
        "Lgo_%=:\n\t"
        "buffer_load_dwordx4 v[22:25], v0, s[40:43], s58 offen offset:0 sc1\n\t"
        "buffer_load_dwordx4 v[26:29], v0, s[40:43], s58 offen offset:1024 sc1\n\t"
        "s_memtime s[22:23]\n\t"
        "v_mfma_f32_16x16x32_bf16 v[94:97], a[116:119], v[110:113], v[94:97]\n\t"
        "ds_read_b128 v[70:73], v1 offset:3072\n\t"
        "v_cvt_pk_bf16_f32 v35, v92, v93\n\t"
        "v_cvt_f32_u32 v19, s24\n\t"
        "v_rcp_f32 v21, v19\n\t"
        "v_mfma_f32_16x16x32_bf16 v[94:97], a[124:127], v[102:105], v[94:97]\n\t"
        "s_mov_b32 s54, 0\n\t"
        "v_mul_f32 v18, 0x47800000, v18\n\t"
        "v_mul_f32 v19, 0x42800000, v19\n\t"
        "v_cvt_pk_bf16_f32 v14, v58, v59\n\t"
        "v_mov_b64 v[92:93], 0\n\t"
        "v_mul_f32 v21, v21, v18\n\t"
        "v_cvt_pk_bf16_f32 v15, v60, v61\n\t"
        "v_cvt_u32_f32 v21, v21\n\t"
        "v_cvt_pk_bf16_f32 v36, v94, v95\n\t"
        "v_mfma_f32_16x16x32_bf16 v[50:53], v[98:101], v[188:191], v[50:53]\n\t"
        "ds_read_b128 v[74:77], v1 offset:6144\n\t"
        "v_mov_b64 v[86:87], 0\n\t"
        "v_mov_b64 v[94:95], 0\n\t"
        "v_mfma_f32_16x16x32_bf16 v[62:65], v[118:121], v[248:251], v[62:65]\n\t"
        "ds_write_b128 v9, v[30:33] offset:16384\n\t"
        "v_mul_f32 v20, v20, v19\n\t"
        "v_cvt_u32_f32 v20, v20\n\t"
        "v_cvt_pk_bf16_f32 v13, v56, v57\n\t"
        "v_mfma_f32_16x16x32_bf16 v[50:53], v[106:109], v[180:183], v[50:53]\n\t"
        "buffer_load_dwordx4 v[106:109], v0, s[40:43], s58 offen offset:0 sc1\n\t"
        "v_mfma_f32_16x16x32_bf16 v[62:65], v[110:113], v[244:247], v[62:65]\n\t"
        "buffer_load_dwordx4 v[110:113], v0, s[40:43], s58 offen offset:1024 sc1\n\t"
        "ds_read_b128 v[58:61], v1 offset:4096\n\t"
        "v_readfirstlane_b32 s50, v21\n\t"
        "v_cvt_pk_bf16_f32 v12, v54, v55\n\t"
        "v_mfma_f32_16x16x32_bf16 v[62:65], v[102:105], v[252:255], v[62:65]\n\t"
        "ds_write_b128 v9, v[38:41] offset:8192\n\t"
        "v_cvt_pk_bf16_f32 v37, v96, v97\n\t"
        "v_readfirstlane_b32 s49, v20\n\t"
        "v_mov_b64 v[96:97], 0\n\t"
        "v_cvt_pk_bf16_f32 v34, v90, v91\n\t"
        "v_mov_b64 v[90:91], 0\n\t"
        "s_sub_u32 s51, s49, 224\n\t"
        "s_cmp_lt_u32 s51, 112\n\t"
        "s_cselect_b32 s18, s49, s18\n\t"
        "s_cselect_b32 s17, s50, s17\n\t"
        "v_cvt_pk_bf16_f32 v10, v50, v51\n\t"
        "s_mul_i32 s16, s17, 2018\n\t"
        "s_lshr_b32 s16, s16, 16\n\t"
        "ds_read_b128 v[54:57], v1 offset:1024\n\t"
        "ds_write_b128 v9, v[42:45] offset:12288\n\t"
        "ds_write_b128 v9, v[34:37] offset:20480\n\t"
        "v_cvt_pk_bf16_f32 v17, v64, v65\n\t"
        "v_cvt_pk_bf16_f32 v16, v62, v63\n\t"
        "v_cvt_pk_bf16_f32 v11, v52, v53\n\t"
        "s_add_u32 s16, s16, s25\n\t"
        "ds_read_b128 v[62:65], v1 offset:5120\n\t"
        "ds_read_b128 v[50:53], v1 offset:0\n\t"
        "v_mov_b64 v[82:83], 0\n\t"
        "v_mov_b64 v[84:85], 0\n\t"
        "v_mov_b64 v[88:89], 0\n\t"
        ".p2align 5\n\t"
        "s_nop 0\n\t"
        "s_nop 0\n\t"
        "s_waitcnt vmcnt(3)\n\t"
        "ds_write_b128 v2, v[22:25] offset:0\n\t"
        "s_waitcnt vmcnt(2)\n\t"
        "ds_write_b128 v2, v[26:29] offset:1024\n\t"
        "v_max3_u32 v122, v22, v23, v24\n\t"
        "v_max3_u32 v122, v122, v25, v26\n\t"
        "v_max3_u32 v122, v122, v27, v28\n\t"
        "v_max_u32 v122, v122, v29\n\t"
        "v_cmp_eq_u32 vcc, -1, v122\n\t"
        "s_cbranch_vccnz Lmp_%=\n\t"
        "Lgot_%=:\n\t"
        "s_waitcnt lgkmcnt(0)\n\t"
        "s_barrier\n\t"
        "ds_read_b128 v[18:21], v4 offset:0\n\t"
        "ds_read_b128 v[26:29], v4 offset:2048\n\t"
        "ds_read_b128 v[30:33], v4 offset:3072\n\t"
        "ds_read_b128 v[38:41], v4 offset:5120\n\t"
        "ds_read_b128 v[34:37], v4 offset:4096\n\t"
        "s_waitcnt lgkmcnt(4)\n\t"
        "v_smfmac_f32_16x16x64_bf16 v[86:89], v[18:21], v[58:65], v8\n\t"
        "ds_read_b128 v[22:25], v4 offset:1024\n\t"
        "s_waitcnt lgkmcnt(4)\n\t"
        "v_smfmac_f32_16x16x64_bf16 v[94:97], v[26:29], a[208:215], v8\n\t"
        "s_memtime s[78:79]\n\t"
        "v_smfmac_f32_16x16x64_bf16 v[82:85], v[18:21], v[50:57], v8\n\t"
        "ds_read_b128 v[42:45], v4 offset:6144\n\t"
        "v_smfmac_f32_16x16x64_bf16 v[86:89], v[26:29], a[80:87], v8\n\t"
        "s_waitcnt lgkmcnt(4)\n\t"
        "v_smfmac_f32_16x16x64_bf16 v[82:85], v[30:33], a[24:31], v8\n\t"
        "v_smfmac_f32_16x16x64_bf16 v[82:85], v[26:29], a[16:23], v8\n\t"
        "s_waitcnt lgkmcnt(0)\n\t"
        "v_smfmac_f32_16x16x64_bf16 v[86:89], v[42:45], a[112:119], v8\n\t"
        "s_sub_u32 s49, s78, s22\n\t"
        "ds_read_b128 v[54:57], v1 offset:9216\n\t"
        "v_smfmac_f32_16x16x64_bf16 v[86:89], v[22:25], v[74:81], v8\n\t"
        "s_mul_i32 s49, s49, s17\n\t"
        "ds_read_b128 v[46:49], v4 offset:7168\n\t"
        "v_smfmac_f32_16x16x64_bf16 v[86:89], v[38:41], a[104:111], v8\n\t"
        "s_lshr_b32 s49, s49, 16\n\t"
        "s_add_u32 s49, s49, s16\n\t"
        "s_min_u32 s49, s49, -2\n\t"
        "v_smfmac_f32_16x16x64_bf16 v[86:89], v[34:37], a[96:103], v8\n\t"
        "ds_read_b128 v[50:53], v1 offset:8192\n\t"
        "v_mov_b32 v124, s49\n\t"
        "v_smfmac_f32_16x16x64_bf16 v[86:89], v[30:33], a[88:95], v8\n\t"
        "s_mov_b64 exec, 0x10001\n\t"
        "buffer_store_dword v124, v7, s[40:43], s26 offen sc1\n\t"
        "s_mov_b64 exec, -1\n\t"
        "s_waitcnt lgkmcnt(1)\n\t"
        "v_smfmac_f32_16x16x64_bf16 v[86:89], v[46:49], a[120:127], v8\n\t"
        "s_nop 0\n\t"
        "ds_read_b128 v[58:61], v1 offset:12288\n\t"
        "v_smfmac_f32_16x16x64_bf16 v[82:85], v[22:25], v[66:73], v8\n\t"
        "ds_read_b128 v[66:69], v1 offset:10240\n\t"
        "v_smfmac_f32_16x16x64_bf16 v[82:85], v[34:37], a[32:39], v8\n\t"
        "ds_read_b128 v[62:65], v1 offset:13312\n\t"
        "v_smfmac_f32_16x16x64_bf16 v[82:85], v[38:41], a[40:47], v8\n\t"
        "ds_read_b128 v[78:81], v1 offset:15360\n\t"
        "v_add_f32 v87, v87, v89\n\t"
        "v_smfmac_f32_16x16x64_bf16 v[82:85], v[46:49], a[56:63], v8\n\t"
        "ds_read_b128 v[74:77], v1 offset:14336\n\t"
        "v_add_f32 v86, v86, v88\n\t"
        "v_smfmac_f32_16x16x64_bf16 v[82:85], v[42:45], a[48:55], v8\n\t"
        "ds_read_b128 v[70:73], v1 offset:11264\n\t"
        "v_cvt_pk_bf16_f32 v88, v86, v87\n\t"
        "v_smfmac_f32_16x16x64_bf16 v[94:97], v[30:33], a[216:223], v8\n\t"
        "s_waitcnt lgkmcnt(3)\n\t"
        "v_mov_b64 v[98:99], 0\n\t"
        "v_smfmac_f32_16x16x64_bf16 v[94:97], v[18:21], v[58:65], v8\n\t"
        "ds_read_b128 v[58:61], v0 offset:9216\n\t"
        "v_cvt_pk_bf16_f32 v89, v86, v87\n\t"
        "v_add_f32 v82, v82, v84\n\t"
        "v_smfmac_f32_16x16x64_bf16 v[90:93], v[30:33], a[152:159], v8\n\t"
        "s_waitcnt lgkmcnt(2)\n\t"
        "v_add_f32 v83, v83, v85\n\t"
        "v_smfmac_f32_16x16x64_bf16 v[94:97], v[22:25], v[74:81], v8\n\t"
        "ds_read_b128 v[78:81], v0 offset:15360\n\t"
        "v_cvt_pk_bf16_f32 v84, v82, v83\n\t"
        "v_smfmac_f32_16x16x64_bf16 v[90:93], v[18:21], v[50:57], v8\n\t"
        "ds_read_b128 v[54:57], v0 offset:12288\n\t"
        "v_cvt_pk_bf16_f32 v85, v82, v83\n\t"
        "v_smfmac_f32_16x16x64_bf16 v[94:97], v[46:49], a[248:255], v8\n\t"
        "ds_write2st64_b64 v5, v[84:85], v[88:89] offset0:0 offset1:1\n\t"
        "v_smfmac_f32_16x16x64_bf16 v[90:93], v[46:49], a[184:191], v8\n\t"
        "ds_read_b128 v[74:77], v0 offset:11264\n\t"
        "s_waitcnt lgkmcnt(5)\n\t"
        "v_smfmac_f32_16x16x64_bf16 v[90:93], v[22:25], v[66:73], v8\n\t"
        "ds_read_b128 v[66:69], v0 offset:10240\n\t"
        "v_mov_b64 v[104:105], 0\n\t"
        "v_smfmac_f32_16x16x64_bf16 v[90:93], v[26:29], a[144:151], v8\n\t"
        "ds_read_b128 v[70:73], v0 offset:14336\n\t"
        "v_smfmac_f32_16x16x64_bf16 v[94:97], v[42:45], a[240:247], v8\n\t"
        "s_waitcnt lgkmcnt(3)\n\t"
        "s_barrier\n\t"
        "v_smfmac_f32_16x16x64_bf16 v[90:93], v[38:41], a[168:175], v8\n\t"
        "s_waitcnt vmcnt(1)\n\t"
        "ds_read_b64_tr_b16 v[118:119], v6 offset:27648\n\t"
        "v_smfmac_f32_16x16x64_bf16 v[94:97], v[38:41], a[232:239], v8\n\t"
        "ds_read_b64_tr_b16 v[106:107], v6 offset:24576\n\t"
        "v_mov_b64 v[100:101], 0\n\t"
        "v_smfmac_f32_16x16x64_bf16 v[94:97], v[34:37], a[224:231], v8\n\t"
        "ds_read_b64_tr_b16 v[114:115], v6 offset:26624\n\t"
        "v_mov_b64 v[102:103], 0\n\t"
        "v_smfmac_f32_16x16x64_bf16 v[90:93], v[34:37], a[160:167], v8\n\t"
        "ds_read_b128 v[50:53], v0 offset:8192\n\t"
        "ds_read_b64_tr_b16 v[108:109], v6 offset:25088\n\t"
        "v_smfmac_f32_16x16x64_bf16 v[90:93], v[42:45], a[176:183], v8\n\t"
        "s_nop 1\n\t"
        "v_add_f32 v95, v95, v97\n\t"
        "v_smfmac_f32_16x16x64_bf16 v[102:105], v[34:37], v[224:231], v8\n\t"
        "ds_read_b64_tr_b16 v[110:111], v6 offset:25600\n\t"
        "v_add_f32 v94, v94, v96\n\t"
        "v_smfmac_f32_16x16x64_bf16 v[102:105], v[26:29], v[208:215], v8\n\t"
        "ds_read_b64_tr_b16 v[112:113], v6 offset:26112\n\t"
        "v_cvt_pk_bf16_f32 v96, v94, v95\n\t"
        "v_smfmac_f32_16x16x64_bf16 v[98:101], v[26:29], v[144:151], v8\n\t"
        "ds_read_b64_tr_b16 v[116:117], v6 offset:27136\n\t"
        "v_add_f32 v91, v91, v93\n\t"
        "v_add_f32 v90, v90, v92\n\t"
        "v_cvt_pk_bf16_f32 v92, v90, v91\n\t"
        "v_smfmac_f32_16x16x64_bf16 v[102:105], v[22:25], v[200:207], v8\n\t"
        "ds_write2st64_b32 v5, v92, v96 offset0:16 offset1:18\n\t"
        "v_smfmac_f32_16x16x64_bf16 v[102:105], v[30:33], v[216:223], v8\n\t"
        "ds_read_b64_tr_b16 v[120:121], v6 offset:28160\n\t"
        "s_waitcnt lgkmcnt(5)\n\t"
        "v_mfma_f32_16x16x32_bf16 v[82:85], v[50:53], v[106:109], 0\n\t"
        "s_waitcnt lgkmcnt(3)\n\t"
        "v_mov_b64 v[50:51], s[96:97]\n\t"
        "v_mov_b64 v[52:53], s[96:97]\n\t"
        "v_mfma_f32_16x16x32_bf16 v[82:85], v[58:61], v[110:113], v[82:85]\n\t"
        "ds_read_b128 v[62:65], v0 offset:13312\n\t"
        "s_waitcnt lgkmcnt(3)\n\t"
        "v_mfma_f32_16x16x32_bf16 v[82:85], v[66:69], v[114:117], v[82:85]\n\t"
        "ds_read_b128 v[26:29], v1 offset:18432\n\t"
        "s_waitcnt lgkmcnt(2)\n\t"
        "v_mfma_f32_16x16x32_bf16 v[82:85], v[74:77], v[118:121], v[82:85]\n\t"
        "s_barrier\n\t"
        "v_mfma_f32_16x16x32_bf16 v[86:89], v[54:57], v[106:109], 0\n\t"
        "ds_read_b64_tr_b16 v[66:67], v6 offset:28672\n\t"
        "v_mov_b64 v[54:55], s[98:99]\n\t"
        "v_mfma_f32_16x16x32_bf16 v[86:89], v[78:81], v[118:121], v[86:89]\n\t"
        "ds_read_b64_tr_b16 v[74:75], v6 offset:30720\n\t"
        "ds_read_b64_tr_b16 v[68:69], v6 offset:29184\n\t"
        "v_mfma_f32_16x16x32_bf16 v[86:89], v[70:73], v[114:117], v[86:89]\n\t"
        "s_waitcnt lgkmcnt(4)\n\t"
        "v_exp_f32 v82, v82\n\t"
        "v_mfma_f32_16x16x32_bf16 v[86:89], v[62:65], v[110:113], v[86:89]\n\t"
        "v_exp_f32 v83, v83\n\t"
        "v_smfmac_f32_16x16x64_bf16 v[98:101], v[42:45], v[176:183], v8\n\t"
        "ds_read_b64_tr_b16 v[78:79], v6 offset:31744\n\t"
        "v_exp_f32 v85, v85\n\t"
        "v_smfmac_f32_16x16x64_bf16 v[102:105], v[46:49], v[248:255], v8\n\t"
        "ds_read_b64_tr_b16 v[72:73], v6 offset:30208\n\t"
        "v_exp_f32 v84, v84\n\t"
        "v_smfmac_f32_16x16x64_bf16 v[102:105], v[18:21], v[192:199], v8\n\t"
        "ds_read_b64_tr_b16 v[80:81], v6 offset:32256\n\t"
        "ds_read_b64_tr_b16 v[76:77], v6 offset:31232\n\t"
        "v_smfmac_f32_16x16x64_bf16 v[98:101], v[46:49], v[184:191], v8\n\t"
        "ds_read_b64_tr_b16 v[70:71], v6 offset:29696\n\t"
        "v_exp_f32 v86, v86\n\t"
        "v_smfmac_f32_16x16x64_bf16 v[102:105], v[42:45], v[240:247], v8\n\t"
        "ds_read_b128 v[42:45], v1 offset:30720\n\t"
        "v_cvt_pk_bf16_f32 v95, v84, v85\n\t"
        "v_smfmac_f32_16x16x64_bf16 v[98:101], v[30:33], v[152:159], v8\n\t"
        "ds_read_b128 v[30:33], v1 offset:19456\n\t"
        "s_waitcnt lgkmcnt(7)\n\t"
        "v_mfma_f32_16x16x32_bf16 v[90:93], v[66:69], v[106:109], 0\n\t"
        "s_waitcnt lgkmcnt(2)\n\t"
        "v_exp_f32 v89, v89\n\t"
        "v_mfma_f32_16x16x32_bf16 v[90:93], v[70:73], v[110:113], v[90:93]\n\t"
        "v_exp_f32 v88, v88\n\t"
        "v_mfma_f32_16x16x32_bf16 v[90:93], v[78:81], v[118:121], v[90:93]\n\t"
        "v_exp_f32 v87, v87\n\t"
        "v_mfma_f32_16x16x32_bf16 v[90:93], v[74:77], v[114:117], v[90:93]\n\t"
        "ds_read_b128 v[114:117], v1 offset:22528\n\t"
        "v_cvt_pk_bf16_f32 v96, v86, v87\n\t"
        "v_smfmac_f32_16x16x64_bf16 v[98:101], v[38:41], v[168:175], v8\n\t"
        "ds_read_b128 v[70:73], v1 offset:21504\n\t"
        "v_cvt_pk_bf16_f32 v94, v82, v83\n\t"
        "v_smfmac_f32_16x16x64_bf16 v[102:105], v[38:41], v[232:239], v8\n\t"
        "ds_read_b128 v[74:77], v1 offset:28672\n\t"
        "v_cvt_pk_bf16_f32 v97, v88, v89\n\t"
        "v_smfmac_f32_16x16x64_bf16 v[98:101], v[18:21], v[128:135], v8\n\t"
        "ds_read_b128 v[66:69], v1 offset:20480\n\t"
        "v_mul_f32 v90, s55, v90\n\t"
        "v_smfmac_f32_16x16x64_bf16 v[98:101], v[34:37], v[160:167], v8\n\t"
        "ds_read_b128 v[18:21], v1 offset:16384\n\t"
        "v_exp_f32 v90, v90\n\t"
        "v_mul_f32 v91, s55, v91\n\t"
        "v_smfmac_f32_16x16x64_bf16 v[98:101], v[22:25], v[136:143], v8\n\t"
        "ds_read_b128 v[22:25], v1 offset:17408\n\t"
        "v_exp_f32 v91, v91\n\t"
        "v_mfma_f32_16x16x32_bf16 v[62:65], v[14:17], v[94:97], 0\n\t"
        "ds_read_b128 v[46:49], v1 offset:31744\n\t"
        "v_cvt_pk_bf16_f32 v56, v90, v91\n\t"
        "v_cvt_pk_bf16_f32 v57, v90, v91\n\t"
        "v_mfma_f32_16x16x32_bf16 v[90:93], v[50:53], v[94:97], 0\n\t"
        "s_nop 0\n\t"
        "v_cvt_pk_bf16_f32 v123, v100, v101\n\t"
        "v_cvt_pk_bf16_f32 v124, v102, v103\n\t"
        "v_mfma_f32_16x16x32_bf16 v[58:61], v[10:13], v[94:97], 0\n\t"
        "v_xor_b32 v96, 2048, v9\n\t"
        "v_cvt_pk_bf16_f32 v125, v104, v105\n\t"
        "v_mfma_f32_16x16x16_bf16 v[90:93], v[54:55], v[56:57], v[90:93]\n\t"
        "s_nop 0\n\t"
        "v_cvt_pk_bf16_f32 v122, v98, v99\n\t"
        "v_mov_b64 v[102:103], 0\n\t"
        "v_mfma_f32_16x16x16_bf16 v[62:65], v[124:125], v[56:57], v[62:65]\n\t"
        "ds_read_b128 v[78:81], v1 offset:29696\n\t"
        "v_mov_b64 v[82:83], 0\n\t"
        "v_mfma_f32_16x16x16_bf16 v[58:61], v[122:123], v[56:57], v[58:61]\n\t"
        "s_nop 0\n\t"
        "v_rcp_f32 v54, v90\n\t"
        "v_mov_b64 v[88:89], 0\n\t"
        "v_max_f32 v55, v54, v90\n\t"
        "v_cmp_ngt_f32 vcc, s21, v55\n\t"
        "s_cbranch_vccnz Lfb2_%=\n\t"
        "Lfb2r_%=:\n\t"
        "v_pk_mul_f32 v[64:65], v[64:65], v[54:55] op_sel_hi:[1,0]\n\t"
        "v_pk_mul_f32 v[62:63], v[62:63], v[54:55] op_sel_hi:[1,0]\n\t"
        "v_pk_mul_f32 v[60:61], v[60:61], v[54:55] op_sel_hi:[1,0]\n\t"
        "v_cvt_pk_bf16_f32 v51, v60, v61\n\t"
        "v_pk_mul_f32 v[58:59], v[58:59], v[54:55] op_sel_hi:[1,0]\n\t"
        "v_cvt_pk_bf16_f32 v50, v58, v59\n\t"
        "v_cvt_pk_bf16_f32 v53, v64, v65\n\t"
        "v_cvt_pk_bf16_f32 v52, v62, v63\n\t"
        "ds_write_b128 v9, v[50:53] offset:24576\n\t"
        "v_xor_b32 v95, 1024, v9\n\t"
        "v_mov_b64 v[104:105], 0\n\t"
        "v_mov_b64 v[84:85], 0\n\t"
        "v_mov_b64 v[98:99], 0\n\t"
        "v_mov_b64 v[100:101], 0\n\t"
        "v_mov_b64 v[86:87], 0\n\t"
        "v_smfmac_f32_16x16x64_bf16 v[82:85], v[50:53], a[0:7], v8\n\t"
        "ds_read_b128 v[118:121], v1 offset:23552\n\t"
        "v_xor_b32 v97, 3072, v9\n\t"
        "v_smfmac_f32_16x16x64_bf16 v[86:89], v[50:53], a[8:15], v8\n\t"
        "s_waitcnt lgkmcnt(1)\n\t"
        "s_barrier\n\t"
        "ds_read_b128 v[90:93], v97 offset:24576\n\t"
        "v_smfmac_f32_16x16x64_bf16 v[98:101], v[50:53], a[64:71], v8\n\t"
        "ds_read_b128 v[62:65], v96 offset:24576\n\t"
        "ds_read_b128 v[58:61], v95 offset:24576\n\t"
        "v_smfmac_f32_16x16x64_bf16 v[102:105], v[50:53], a[72:79], v8\n\t"
        "buffer_load_dword v127, v126, s[40:43], s77 offen sc1\n\t"
        "s_waitcnt lgkmcnt(2)\n\t"
        "v_smfmac_f32_16x16x64_bf16 v[102:105], v[90:93], v[42:49], v8\n\t"
        "ds_read_b128 v[106:109], v1 offset:24576\n\t"
        "v_smfmac_f32_16x16x64_bf16 v[98:101], v[90:93], v[74:81], v8\n\t"
        "ds_read_b128 v[110:113], v1 offset:25600\n\t"
        "s_waitcnt lgkmcnt(2)\n\t"
        "v_smfmac_f32_16x16x64_bf16 v[98:101], v[58:61], a[192:199], v8\n\t"
        "ds_read_b128 v[34:37], v1 offset:26624\n\t"
        "v_add_u32 v94, s13, v0\n\t"
        "v_smfmac_f32_16x16x64_bf16 v[102:105], v[62:65], v[114:121], v8\n\t"
        "ds_read_b128 v[38:41], v1 offset:27648\n\t"
        "v_smfmac_f32_16x16x64_bf16 v[102:105], v[58:61], a[200:207], v8\n\t"
        "v_smfmac_f32_16x16x64_bf16 v[98:101], v[62:65], v[66:73], v8\n\t"
        "s_nop 0\n\t"
        "s_add_u32 s70, s59, 16384\n\t"
        "s_nop 0\n\t"
        "v_smfmac_f32_16x16x64_bf16 v[86:89], v[62:65], v[26:33], v8\n\t"
        "ds_read_b128 v[30:33], v94 offset:17408\n\t"
        "s_waitcnt lgkmcnt(1)\n\t"
        "v_smfmac_f32_16x16x64_bf16 v[86:89], v[90:93], v[34:41], v8\n\t"
        "ds_read_b128 v[38:41], v94 offset:19456\n\t"
        "v_cvt_pk_bf16_f32 v57, v101, v105\n\t"
        "v_smfmac_f32_16x16x64_bf16 v[82:85], v[62:65], v[18:25], v8\n\t"
        "ds_read_b128 v[34:37], v94 offset:18432\n\t"
        "v_cvt_pk_bf16_f32 v54, v98, v102\n\t"
        "v_cvt_pk_bf16_f32 v56, v99, v103\n\t"
        "v_cvt_pk_bf16_f32 v55, v100, v104\n\t"
        "v_smfmac_f32_16x16x64_bf16 v[82:85], v[58:61], a[128:135], v8\n\t"
        "buffer_store_dwordx4 v[54:57], v7, s[40:43], s70 offen offset:0 sc1\n\t"
        "v_smfmac_f32_16x16x64_bf16 v[86:89], v[58:61], a[136:143], v8\n\t"
        "ds_read_b128 v[26:29], v94 offset:16384\n\t"
        "v_add_u32 v94, s71, v94\n\t"
        "v_smfmac_f32_16x16x64_bf16 v[82:85], v[90:93], v[106:113], v8\n\t"
        "ds_read_b128 v[78:81], v94 offset:11264\n\t"
        "ds_read_b128 v[74:77], v94 offset:10240\n\t"
        "ds_read_b128 v[66:69], v94 offset:8192\n\t"
        "ds_read_b128 v[70:73], v94 offset:9216\n\t"
        "s_nop 3\n\t"
        "v_cvt_pk_bf16_f32 v52, v83, v87\n\t"
        "v_cvt_pk_bf16_f32 v50, v82, v86\n\t"
        "v_cvt_pk_bf16_f32 v53, v85, v89\n\t"
        "v_cvt_pk_bf16_f32 v51, v84, v88\n\t"
        "buffer_store_dwordx4 v[50:53], v7, s[40:43], s59 offen offset:0 sc1\n\t"
        "Lp3_%=:\n\t"
        "s_cmp_eq_u32 s44, 3\n\t"
        "s_cbranch_scc1 Lwm_%=\n\t"
        "s_waitcnt lgkmcnt(1)\n\t"
        "v_mfma_f32_16x16x32_bf16 v[106:109], v[66:69], v[26:29], 0\n\t"
        "s_waitcnt lgkmcnt(0)\n\t"
        "v_lshrrev_b32 v24, 4, v9\n\t"
        "v_xor_b32 v24, v24, v0\n\t"
        "v_mfma_f32_16x16x32_bf16 v[106:109], v[70:73], v[30:33], v[106:109]\n\t"
        "v_and_b32 v25, 0x380, v24\n\t"
        "v_and_b32 v24, 0x70, v24\n\t"
        "v_mfma_f32_16x16x32_bf16 v[106:109], v[74:77], v[34:37], v[106:109]\n\t"
        "v_lshl_or_b32 v24, v25, 3, v24\n\t"
        "v_mov_b32 v20, s57\n\t"
        "v_mfma_f32_16x16x32_bf16 v[106:109], v[78:81], v[38:41], v[106:109]\n\t"
        "s_waitcnt lgkmcnt(0)\n\t"
        "s_barrier\n\t"
        "s_nop 5\n\t"
        "v_cndmask_b32 v114, v20, v106, s[62:63]\n\t"
        "v_cndmask_b32 v115, v20, v107, s[64:65]\n\t"
        "v_cndmask_b32 v116, v20, v108, s[66:67]\n\t"
        "v_cndmask_b32 v117, v20, v109, s[68:69]\n\t"
        "v_exp_f32 v118, v114\n\t"
        "v_exp_f32 v119, v115\n\t"
        "v_exp_f32 v120, v116\n\t"
        "v_exp_f32 v121, v117\n\t"
        "v_cvt_pk_bf16_f32 v46, v118, v119\n\t"
        "v_cvt_pk_bf16_f32 v47, v120, v121\n\t"
        "ds_write_b64 v9, v[46:47] offset:24576\n\t"
        "Lp3b_%=:\n\t"
        "s_waitcnt vmcnt(2)\n\t"
        "v_cmp_gt_u32 s[72:73], s39, v127\n\t"
        "s_nop 0\n\t"
        "s_cmp_lg_u32 s72, 0\n\t"
        "s_cselect_b32 s29, 1, 0\n\t"
        "s_cselect_b32 s52, 0x7ff00000, s74\n\t"
        "v_mov_b64 v[72:73], s[98:99]\n\t"
        "s_add_u32 m0, s35, 0\n\t"
        "s_nop 0\n\t"
        "buffer_load_dwordx4 v24, s[40:43], s52 offen offset:0 sc1 lds\n\t"
        "s_add_u32 m0, s35, 3968\n\t"
        "s_nop 0\n\t"
        "buffer_load_dwordx4 v24, s[40:43], s52 offen offset:128 sc1 lds\n\t"
        "s_add_u32 m0, s35, 7936\n\t"
        "s_nop 0\n\t"
        "buffer_load_dwordx4 v24, s[40:43], s52 offen offset:256 sc1 lds\n\t"
        "s_add_u32 m0, s35, 11904\n\t"
        "s_nop 0\n\t"
        "buffer_load_dwordx4 v24, s[40:43], s52 offen offset:384 sc1 lds\n\t"
        "s_waitcnt lgkmcnt(0)\n\t"
        "s_barrier\n\t"
        "ds_read_b64 v[62:63], v0 offset:26624\n\t"
        "ds_read_b64 v[58:59], v0 offset:24576\n\t"
        "ds_read_b64 v[60:61], v0 offset:25600\n\t"
        "buffer_load_dword v127, v126, s[40:43], s77 offen sc1\n\t"
        "v_mov_b64 v[68:69], s[96:97]\n\t"
        "v_lshrrev_b32 v27, 4, v0\n\t"
        "v_mov_b64 v[70:71], s[96:97]\n\t"
        "v_and_b32 v26, 0xf0, v0\n\t"
        "s_waitcnt lgkmcnt(0)\n\t"
        "v_mfma_f32_16x16x32_bf16 v[78:81], v[68:71], v[60:63], 0\n\t"
        "ds_read_b128 v[38:41], v1 offset:8192\n\t"
        "v_and_b32 v27, 0x30, v27\n\t"
        "v_mfma_f32_16x16x16_bf16 v[82:85], v[10:11], v[58:59], 0\n\t"
        "ds_read_b128 v[64:67], v0 offset:27648\n\t"
        "v_xor_b32 v27, v27, v26\n\t"
        "v_mfma_f32_16x16x16_bf16 v[86:89], v[14:15], v[58:59], 0\n\t"
        "s_add_u32 m0, s35, 15872\n\t"
        "s_nop 0\n\t"
        "buffer_load_dwordx4 v24, s[40:43], s52 offen offset:512 sc1 lds\n\t"
        "v_mfma_f32_16x16x32_bf16 v[90:93], v[10:13], v[60:63], 0\n\t"
        "s_waitcnt lgkmcnt(0)\n\t"
        "v_lshl_add_u32 v26, v26, 4, v27\n\t"
        "v_xor_b32 v28, 128, v26\n\t"
        "v_mfma_f32_16x16x16_bf16 v[78:81], v[72:73], v[66:67], v[78:81]\n\t"
        "ds_read_b128 v[42:45], v1 offset:12288\n\t"
        "v_xor_b32 v27, 64, v26\n\t"
        "v_mfma_f32_16x16x16_bf16 v[74:77], v[72:73], v[64:65], 0\n\t"
        "s_add_u32 m0, s35, 19840\n\t"
        "s_nop 0\n\t"
        "buffer_load_dwordx4 v24, s[40:43], s52 offen offset:640 sc1 lds\n\t"
        "v_mfma_f32_16x16x16_bf16 v[74:77], v[68:69], v[58:59], v[74:77]\n\t"
        "ds_read_b128 v[30:33], v1 offset:0\n\t"
        "v_xor_b32 v29, 192, v26\n\t"
        "v_mfma_f32_16x16x32_bf16 v[94:97], v[14:17], v[60:63], 0\n\t"
        "s_add_u32 m0, s35, 27776\n\t"
        "s_nop 0\n\t"
        "buffer_load_dwordx4 v24, s[40:43], s52 offen offset:896 sc1 lds\n\t"
        "v_mfma_f32_16x16x16_bf16 v[82:85], v[122:123], v[64:65], v[82:85]\n\t"
        "s_add_u32 s19, s19, s20\n\t"
        "v_rcp_f32 v100, v78\n\t"
        "v_mfma_f32_16x16x16_bf16 v[94:97], v[124:125], v[66:67], v[94:97]\n\t"
        "s_min_u32 s19, s19, 240\n\t"
        "v_rcp_f32 v98, v74\n\t"
        "v_mfma_f32_16x16x16_bf16 v[86:89], v[124:125], v[64:65], v[86:89]\n\t"
        "ds_read_b128 v[34:37], v1 offset:4096\n\t"
        "s_mov_b32 s20, 0\n\t"
        "v_mfma_f32_16x16x16_bf16 v[90:93], v[122:123], v[66:67], v[90:93]\n\t"
        "s_add_u32 s47, s47, 2\n\t"
        "v_max_f32 v102, v98, v74\n\t"
        "v_max3_f32 v102, v102, v100, v78\n\t"
        "v_cmp_ngt_f32 vcc, s21, v102\n\t"
        "s_cbranch_vccnz Lfb3_%=\n\t"
        "Lfb3r_%=:\n\t"
        "v_pk_mul_f32 v[84:85], v[84:85], v[98:99] op_sel_hi:[1,0]\n\t"
        "s_nop 2\n\t"
        "v_pk_mul_f32 v[86:87], v[86:87], v[98:99] op_sel_hi:[1,0]\n\t"
        "v_cvt_pk_bf16_f32 v76, v86, v87\n\t"
        "v_pk_mul_f32 v[94:95], v[94:95], v[100:101] op_sel_hi:[1,0]\n\t"
        "v_pk_mul_f32 v[92:93], v[92:93], v[100:101] op_sel_hi:[1,0]\n\t"
        "v_pk_mul_f32 v[90:91], v[90:91], v[100:101] op_sel_hi:[1,0]\n\t"
        "v_cvt_pk_bf16_f32 v78, v90, v91\n\t"
        "v_pk_mul_f32 v[88:89], v[88:89], v[98:99] op_sel_hi:[1,0]\n\t"
        "v_cvt_pk_bf16_f32 v77, v88, v89\n\t"
        "v_pk_mul_f32 v[96:97], v[96:97], v[100:101] op_sel_hi:[1,0]\n\t"
        "v_cvt_pk_bf16_f32 v81, v96, v97\n\t"
        "v_cvt_pk_bf16_f32 v80, v94, v95\n\t"
        "v_pk_mul_f32 v[82:83], v[82:83], v[98:99] op_sel_hi:[1,0]\n\t"
        "v_cvt_pk_bf16_f32 v79, v92, v93\n\t"
        "v_cvt_pk_bf16_f32 v74, v82, v83\n\t"
        "v_cvt_pk_bf16_f32 v75, v84, v85\n\t"
        "s_add_u32 s51, s75, 4096\n\t"
        "buffer_store_dwordx4 v[78:81], v9, s[40:43], s51 offen offset:0 sc1\n\t"
        "buffer_store_dwordx4 v[74:77], v9, s[40:43], s75 offen offset:0 sc1\n\t"
        "s_cmp_lg_u32 s29, 0\n\t"
        "s_cbranch_scc1 Ldef_%=\n\t"
        "s_waitcnt vmcnt(7)\n\t"
        "s_cmp_lt_u32 s47, 64\n\t"
        "s_cbranch_scc1 Ltask_%=\n\t"
        "s_branch Lend_%=\n\t"
        "Ldef_%=:\n\t"
        "s_waitcnt vmcnt(5)\n\t"
        "v_cmp_gt_u32 vcc, s39, v127\n\t"
        "s_cbranch_vccz Lfxd_%=\n\t"
        "Ldef0_%=:\n\t"
        "s_mov_b32 s54, 0\n\t"
        "Lfx_%=:\n\t"
        "buffer_load_dword v127, v126, s[40:43], s77 offen sc1\n\t"
        "s_waitcnt vmcnt(0)\n\t"
        "v_cmp_gt_u32 vcc, s39, v127\n\t"
        "s_cbranch_vccz Lfxd_%=\n\t"
        "s_sleep 2\n\t"
        "s_add_u32 s54, s54, 1\n\t"
        "s_cmp_lt_u32 s54, s53\n\t"
        "s_cbranch_scc1 Lfx_%=\n\t"
        "s_mov_b32 s53, 0\n\t"
        "Lfxd_%=:\n\t"
        "s_memrealtime s[2:3]\n\t"
        "s_memtime s[22:23]\n\t"
        "s_add_u32 m0, s35, 0\n\t"
        "s_nop 0\n\t"
        "buffer_load_dwordx4 v24, s[40:43], s74 offen offset:0 sc1 lds\n\t"
        "s_add_u32 m0, s35, 3968\n\t"
        "s_nop 0\n\t"
        "buffer_load_dwordx4 v24, s[40:43], s74 offen offset:128 sc1 lds\n\t"
        "s_add_u32 m0, s35, 7936\n\t"
        "s_nop 0\n\t"
        "buffer_load_dwordx4 v24, s[40:43], s74 offen offset:256 sc1 lds\n\t"
        "s_add_u32 m0, s35, 11904\n\t"
        "s_nop 0\n\t"
        "buffer_load_dwordx4 v24, s[40:43], s74 offen offset:384 sc1 lds\n\t"
        "s_add_u32 m0, s35, 15872\n\t"
        "s_nop 0\n\t"
        "buffer_load_dwordx4 v24, s[40:43], s74 offen offset:512 sc1 lds\n\t"
        "s_add_u32 m0, s35, 19840\n\t"
        "s_nop 0\n\t"
        "buffer_load_dwordx4 v24, s[40:43], s74 offen offset:640 sc1 lds\n\t"
        "s_add_u32 m0, s35, 27776\n\t"
        "s_nop 0\n\t"
        "buffer_load_dwordx4 v24, s[40:43], s74 offen offset:896 sc1 lds\n\t"
        "s_waitcnt lgkmcnt(0)\n\t"
        "s_mul_i32 s25, s2, 5\n\t"
        "s_sub_u32 s25, s25, 90\n\t"
        "s_waitcnt vmcnt(4)\n\t"
        "s_branch Ltask_%=\n\t"
        "Lfb2_%=:\n\t"
        "s_waitcnt lgkmcnt(0)\n\t"
        "s_nop 7\n\t"
        "s_cmp_lg_u32 s47, 63\n\t"
        "s_cbranch_scc1 Lfb2x_%=\n\t"
        "s_mov_b32 s21, 0x53800000\n\t"
        "v_add_u32 v94, s13, v0\n\t"
        "ds_read_b128 v[26:29], v94 offset:16384\n\t"
        "ds_read_b128 v[30:33], v94 offset:17408\n\t"
        "ds_read_b128 v[34:37], v94 offset:18432\n\t"
        "ds_read_b128 v[38:41], v94 offset:19456\n\t"
        "v_add_u32 v94, s71, v94\n\t"
        "ds_read_b128 v[66:69], v94 offset:8192\n\t"
        "ds_read_b128 v[70:73], v94 offset:9216\n\t"
        "ds_read_b128 v[74:77], v94 offset:10240\n\t"
        "ds_read_b128 v[78:81], v94 offset:11264\n\t"
        "s_branch Lp3_%=\n\t"
        "Lfb2x_%=:\n\t"
        "ds_read_b128 v[18:21], v0 offset:8192\n\t"
        "ds_read_b128 v[22:25], v0 offset:12288\n\t"
        "ds_read_b128 v[26:29], v0 offset:9216\n\t"
        "ds_read_b128 v[30:33], v0 offset:13312\n\t"
        "ds_read_b128 v[34:37], v0 offset:10240\n\t"
        "ds_read_b128 v[38:41], v0 offset:14336\n\t"
        "ds_read_b128 v[42:45], v0 offset:11264\n\t"
        "ds_read_b128 v[46:49], v0 offset:15360\n\t"
        "ds_read_b64_tr_b16 v[106:107], v6 offset:24576\n\t"
        "ds_read_b64_tr_b16 v[108:109], v6 offset:25088\n\t"
        "ds_read_b64_tr_b16 v[110:111], v6 offset:25600\n\t"
        "ds_read_b64_tr_b16 v[112:113], v6 offset:26112\n\t"
        "ds_read_b64_tr_b16 v[114:115], v6 offset:26624\n\t"
        "ds_read_b64_tr_b16 v[116:117], v6 offset:27136\n\t"
        "ds_read_b64_tr_b16 v[118:119], v6 offset:27648\n\t"
        "ds_read_b64_tr_b16 v[120:121], v6 offset:28160\n\t"
        "ds_read_b64_tr_b16 v[66:67], v6 offset:28672\n\t"
        "ds_read_b64_tr_b16 v[68:69], v6 offset:29184\n\t"
        "ds_read_b64_tr_b16 v[70:71], v6 offset:29696\n\t"
        "ds_read_b64_tr_b16 v[72:73], v6 offset:30208\n\t"
        "ds_read_b64_tr_b16 v[74:75], v6 offset:30720\n\t"
        "ds_read_b64_tr_b16 v[76:77], v6 offset:31232\n\t"
        "ds_read_b64_tr_b16 v[78:79], v6 offset:31744\n\t"
        "ds_read_b64_tr_b16 v[80:81], v6 offset:32256\n\t"
        "s_waitcnt lgkmcnt(0)\n\t"
        "s_barrier\n\t"
        "v_mfma_f32_16x16x32_bf16 v[82:85], v[18:21], v[106:109], 0\n\t"
        "v_mfma_f32_16x16x32_bf16 v[86:89], v[22:25], v[106:109], 0\n\t"
        "v_mfma_f32_16x16x32_bf16 v[82:85], v[26:29], v[110:113], v[82:85]\n\t"
        "v_mfma_f32_16x16x32_bf16 v[86:89], v[30:33], v[110:113], v[86:89]\n\t"
        "v_mfma_f32_16x16x32_bf16 v[82:85], v[34:37], v[114:117], v[82:85]\n\t"
        "v_mfma_f32_16x16x32_bf16 v[86:89], v[38:41], v[114:117], v[86:89]\n\t"
        "v_mfma_f32_16x16x32_bf16 v[82:85], v[42:45], v[118:121], v[82:85]\n\t"
        "v_mfma_f32_16x16x32_bf16 v[86:89], v[46:49], v[118:121], v[86:89]\n\t"
        "v_mfma_f32_16x16x32_bf16 v[90:93], v[66:69], v[106:109], 0\n\t"
        "v_mfma_f32_16x16x32_bf16 v[90:93], v[70:73], v[110:113], v[90:93]\n\t"
        "v_mfma_f32_16x16x32_bf16 v[90:93], v[74:77], v[114:117], v[90:93]\n\t"
        "v_mfma_f32_16x16x32_bf16 v[90:93], v[78:81], v[118:121], v[90:93]\n\t"
        "s_nop 7\n\t"
        "v_mul_f32 v90, s55, v90\n\t"
        "v_mul_f32 v91, s55, v91\n\t"
        "v_max3_f32 v54, v82, v83, v84\n\t"
        "v_max3_f32 v54, v54, v85, v86\n\t"
        "v_max3_f32 v54, v54, v87, v88\n\t"
        "v_max3_f32 v54, v54, v89, v90\n\t"
        "v_max_f32 v54, v54, v91\n\t"
        "v_mov_b32 v55, v54\n\t"
        "s_nop 1\n\t"
        "v_permlane16_swap_b32 v54, v55\n\t"
        "v_max_f32 v54, v54, v55\n\t"
        "v_mov_b32 v55, v54\n\t"
        "s_nop 1\n\t"
        "v_permlane32_swap_b32 v54, v55\n\t"
        "v_max_f32 v54, v54, v55\n\t"
        "v_sub_f32 v82, v82, v54\n\t"
        "v_sub_f32 v83, v83, v54\n\t"
        "v_sub_f32 v84, v84, v54\n\t"
        "v_sub_f32 v85, v85, v54\n\t"
        "v_sub_f32 v86, v86, v54\n\t"
        "v_sub_f32 v87, v87, v54\n\t"
        "v_sub_f32 v88, v88, v54\n\t"
        "v_sub_f32 v89, v89, v54\n\t"
        "v_sub_f32 v90, v90, v54\n\t"
        "v_sub_f32 v91, v91, v54\n\t"
        "v_exp_f32 v82, v82\n\t"
        "v_exp_f32 v83, v83\n\t"
        "v_exp_f32 v84, v84\n\t"
        "v_exp_f32 v85, v85\n\t"
        "v_exp_f32 v86, v86\n\t"
        "v_exp_f32 v87, v87\n\t"
        "v_exp_f32 v88, v88\n\t"
        "v_exp_f32 v89, v89\n\t"
        "v_exp_f32 v90, v90\n\t"
        "v_exp_f32 v91, v91\n\t"
        "v_cvt_pk_bf16_f32 v94, v82, v83\n\t"
        "v_cvt_pk_bf16_f32 v95, v84, v85\n\t"
        "v_cvt_pk_bf16_f32 v96, v86, v87\n\t"
        "v_cvt_pk_bf16_f32 v97, v88, v89\n\t"
        "v_cvt_pk_bf16_f32 v56, v90, v91\n\t"
        "v_cvt_pk_bf16_f32 v57, v90, v91\n\t"
        "v_mov_b64 v[54:55], s[98:99]\n\t"
        "v_mfma_f32_16x16x32_bf16 v[90:93], v[50:53], v[94:97], 0\n\t"
        "s_nop 5\n\t"
        "v_mfma_f32_16x16x16_bf16 v[90:93], v[54:55], v[56:57], v[90:93]\n\t"
        "v_mfma_f32_16x16x32_bf16 v[58:61], v[10:13], v[94:97], 0\n\t"
        "v_mfma_f32_16x16x32_bf16 v[62:65], v[14:17], v[94:97], 0\n\t"
        "s_nop 4\n\t"
        "v_mfma_f32_16x16x16_bf16 v[58:61], v[122:123], v[56:57], v[58:61]\n\t"
        "v_mfma_f32_16x16x16_bf16 v[62:65], v[124:125], v[56:57], v[62:65]\n\t"
        "v_rcp_f32 v54, v90\n\t"
        "ds_read_b128 v[26:29], v1 offset:18432\n\t"
        "ds_read_b128 v[42:45], v1 offset:30720\n\t"
        "ds_read_b128 v[30:33], v1 offset:19456\n\t"
        "ds_read_b128 v[114:117], v1 offset:22528\n\t"
        "ds_read_b128 v[70:73], v1 offset:21504\n\t"
        "ds_read_b128 v[74:77], v1 offset:28672\n\t"
        "ds_read_b128 v[66:69], v1 offset:20480\n\t"
        "ds_read_b128 v[18:21], v1 offset:16384\n\t"
        "ds_read_b128 v[22:25], v1 offset:17408\n\t"
        "ds_read_b128 v[46:49], v1 offset:31744\n\t"
        "v_xor_b32 v96, 2048, v9\n\t"
        "ds_read_b128 v[78:81], v1 offset:29696\n\t"
        "v_mov_b64 v[82:83], 0\n\t"
        "v_mov_b64 v[88:89], 0\n\t"
        "s_branch Lfb2r_%=\n\t"
        "Lmp_%=:\n\t"
        "buffer_load_dwordx4 v[114:117], v0, s[40:43], s58 offen offset:0 sc1\n\t"
        "buffer_load_dwordx4 v[118:121], v0, s[40:43], s58 offen offset:1024 sc1\n\t"
        "s_mov_b32 s20, 0\n\t"
        "Lpoll_%=:\n\t"
        "s_waitcnt vmcnt(3)\n\t"
        "ds_write_b128 v2, v[106:109] offset:0\n\t"
        "s_waitcnt vmcnt(2)\n\t"
        "v_max3_u32 v122, v106, v107, v108\n\t"
        "ds_write_b128 v2, v[110:113] offset:1024\n\t"
        "v_max3_u32 v122, v122, v109, v110\n\t"
        "v_max3_u32 v122, v122, v111, v112\n\t"
        "v_max_u32 v122, v122, v113\n\t"
        "v_cmp_eq_u32 vcc, -1, v122\n\t"
        "s_cbranch_vccz Lgot_%=\n\t"
        "s_waitcnt lgkmcnt(0)\n\t"
        "buffer_load_dwordx4 v[106:109], v0, s[40:43], s58 offen offset:0 sc1\n\t"
        "buffer_load_dwordx4 v[110:113], v0, s[40:43], s58 offen offset:1024 sc1\n\t"
        "s_waitcnt vmcnt(3)\n\t"
        "ds_write_b128 v2, v[114:117] offset:0\n\t"
        "s_waitcnt vmcnt(2)\n\t"
        "v_max3_u32 v122, v114, v115, v116\n\t"
        "ds_write_b128 v2, v[118:121] offset:1024\n\t"
        "v_max3_u32 v122, v122, v117, v118\n\t"
        "v_max3_u32 v122, v122, v119, v120\n\t"
        "v_max_u32 v122, v122, v121\n\t"
        "v_cmp_eq_u32 vcc, -1, v122\n\t"
        "s_cbranch_vccz Lgot_%=\n\t"
        "s_waitcnt lgkmcnt(0)\n\t"
        "buffer_load_dwordx4 v[114:117], v0, s[40:43], s58 offen offset:0 sc1\n\t"
        "buffer_load_dwordx4 v[118:121], v0, s[40:43], s58 offen offset:1024 sc1\n\t"
        "s_add_u32 s54, s54, 1\n\t"
        "s_cmp_lt_u32 s54, s53\n\t"
        "s_cbranch_scc1 Lpoll_%=\n\t"
        "s_mov_b32 s53, 0\n\t"
        "s_branch Lgot_%=\n\t"
        "Lslow_%=:\n\t"
        "s_cmp_lg_u32 s28, -1\n\t"
        "s_cbranch_scc1 Ltsok_%=\n\t"
        "s_mov_b32 s54, 0\n\t"
        "s_cmp_eq_u32 s56, 0\n\t"
        "s_cbranch_scc1 Lg0e_%=\n\t"
        "v_readfirstlane_b32 s49, v127\n\t"
        "s_nop 0\n\t"
        "s_xor_b32 s49, s49, s79\n\t"
        "s_cmp_lt_u32 s49, 2\n\t"
        "s_cbranch_scc1 Lg0d_%=\n\t"
        "s_mov_b32 s54, 0\n\t"
        "Lg0_%=:\n\t"
        "buffer_load_dword v22, off, s[40:43], s78 sc1\n\t"
        "s_waitcnt vmcnt(0)\n\t"
        "v_readfirstlane_b32 s49, v22\n\t"
        "s_nop 0\n\t"
        "s_xor_b32 s49, s49, s79\n\t"
        "s_cmp_lt_u32 s49, 2\n\t"
        "s_cbranch_scc1 Lg0d_%=\n\t"
        "s_sleep 4\n\t"
        "s_add_u32 s54, s54, 1\n\t"
        "s_cmp_lt_u32 s54, s53\n\t"
        "s_cbranch_scc1 Lg0_%=\n\t"
        "Lg0d_%=:\n\t"
        "s_mov_b32 s54, 0\n\t"
        "s_and_b32 s49, s49, 1\n\t"
        "s_lshl_b32 s49, s49, 13\n\t"
        "s_xor_b32 s12, s12, s49\n\t"
        "s_xor_b32 s31, s31, s49\n\t"
        "s_xor_b32 s74, s74, s49\n\t"
        "s_xor_b32 s77, s77, s49\n\t"
        "s_xor_b32 s76, s76, s49\n\t"
        "s_xor_b32 s59, s59, s49\n\t"
        "s_xor_b32 s58, s58, s49\n\t"
        "s_xor_b32 s27, s27, s49\n\t"
        "s_xor_b32 s26, s26, s49\n\t"
        "s_xor_b32 s75, s75, s49\n\t"
        "Lg0e_%=:\n\t"
        "s_mov_b32 s56, 0\n\t"
        "s_sub_u32 s28, s25, 1000\n\t"
        "s_cmp_eq_u32 s47, 0\n\t"
        "s_cbranch_scc1 Ltsok_%=\n\t"
        "s_cmp_lg_u32 s44, 0\n\t"
        "s_cbranch_scc1 Ltsr_%=\n\t"
        "Ltsl_%=:\n\t"
        "buffer_load_dword v22, v24, s[40:43], s27 offen sc1\n\t"
        "s_waitcnt vmcnt(0)\n\t"
        "v_max_u32_dpp v22, v22, v22 row_ror:1 row_mask:0xf bank_mask:0xf\n\t"
        "s_nop 1\n\t"
        "v_max_u32_dpp v22, v22, v22 row_ror:2 row_mask:0xf bank_mask:0xf\n\t"
        "s_nop 1\n\t"
        "v_max_u32_dpp v22, v22, v22 row_ror:4 row_mask:0xf bank_mask:0xf\n\t"
        "s_nop 1\n\t"
        "v_max_u32_dpp v22, v22, v22 row_ror:8 row_mask:0xf bank_mask:0xf\n\t"
        "v_mov_b32 v23, v22\n\t"
        "s_nop 1\n\t"
        "v_permlane16_swap_b32 v22, v23\n\t"
        "v_max_u32 v23, v22, v23\n\t"
        "s_nop 0\n\t"
        "v_readfirstlane_b32 s49, v23\n\t"
        "s_nop 0\n\t"
        "s_cmp_lg_u32 s49, -1\n\t"
        "s_cbranch_scc1 Ltsw_%=\n\t"
        "s_add_u32 s54, s54, 1\n\t"
        "s_cmp_lt_u32 s54, s53\n\t"
        "s_cbranch_scc1 Ltsl_%=\n\t"
        "s_mov_b32 s53, 0\n\t"
        "Ltsw_%=:\n\t"
        "s_mov_b64 exec, s[14:15]\n\t"
        "ds_write_b32 v25, v23 offset:1024\n\t"
        "s_mov_b64 exec, -1\n\t"
        "s_waitcnt lgkmcnt(0)\n\t"
        "Ltsr_%=:\n\t"
        "s_barrier\n\t"
        "ds_read_b32 v19, v2 offset:1024\n\t"
        "s_waitcnt lgkmcnt(0)\n\t"
        "v_readfirstlane_b32 s28, v19\n\t"
        "Ltsok_%=:\n\t"
        "s_nop 0\n\t"
        "s_add_u32 s28, s28, s19\n\t"
        "Lgow_%=:\n\t"
        "s_memrealtime s[2:3]\n\t"
        "s_waitcnt lgkmcnt(0)\n\t"
        "s_mul_i32 s2, s2, 5\n\t"
        "s_sub_u32 s2, s28, s2\n\t"
        "s_sub_u32 s2, s2, 20\n\t"
        "s_cmp_lt_u32 s2, 6000\n\t"
        "s_cbranch_scc1 Lgow_%=\n\t"
        "s_memrealtime s[2:3]\n\t"
        "s_nop 2\n\t"
        "s_memrealtime s[4:5]\n\t"
        "s_nop 2\n\t"
        "s_memrealtime s[6:7]\n\t"
        "s_nop 2\n\t"
        "s_memrealtime s[8:9]\n\t"
        "s_nop 2\n\t"
        "s_memrealtime s[10:11]\n\t"
        "s_waitcnt lgkmcnt(0)\n\t"
        "s_add_u32 s2, s2, s4\n\t"
        "s_add_u32 s6, s6, s8\n\t"
        "s_add_u32 s2, s2, s10\n\t"
        "s_add_u32 s2, s2, s6\n\t"
        "s_sub_u32 s2, s2, 44\n\t"
        "s_sub_u32 s4, s28, s2\n\t"
        "s_cmp_lt_u32 s4, 6000\n\t"
        "s_cselect_b32 s4, s4, 0\n\t"
        "s_mul_i32 s6, s4, s18\n\t"
        "s_lshr_b32 s6, s6, 9\n\t"
        "s_min_u32 s6, s6, 64\n\t"
        "s_lshl_b32 s6, s6, 2\n\t"
        "s_sub_u32 s6, s60, s6\n\t"
        "s_subb_u32 s7, s61, 0\n\t"
        "s_add_u32 s25, s2, s4\n\t"
        "s_setpc_b64 s[6:7]\n\t"
        "Lwm_%=:\n\t"
        "ds_read_b64_tr_b16 v[90:91], v6 offset:28672\n\t"
        "ds_read_b64_tr_b16 v[92:93], v6 offset:29184\n\t"
        "ds_read_b64_tr_b16 v[94:95], v6 offset:29696\n\t"
        "ds_read_b64_tr_b16 v[96:97], v6 offset:30208\n\t"
        "ds_read_b64_tr_b16 v[98:99], v6 offset:30720\n\t"
        "ds_read_b64_tr_b16 v[100:101], v6 offset:31232\n\t"
        "ds_read_b64_tr_b16 v[102:103], v6 offset:31744\n\t"
        "ds_read_b64_tr_b16 v[104:105], v6 offset:32256\n\t"
        "s_waitcnt lgkmcnt(6)\n\t"
        "v_mfma_f32_16x16x32_bf16 v[106:109], v[90:93], v[26:29], 0\n\t"
        "v_mfma_f32_16x16x32_bf16 v[110:113], v[90:93], v[66:69], 0\n\t"
        "s_waitcnt lgkmcnt(4)\n\t"
        "v_mfma_f32_16x16x32_bf16 v[106:109], v[94:97], v[30:33], v[106:109]\n\t"
        "v_mfma_f32_16x16x32_bf16 v[110:113], v[94:97], v[70:73], v[110:113]\n\t"
        "s_waitcnt lgkmcnt(0)\n\t"
        "s_barrier\n\t"
        "v_mfma_f32_16x16x32_bf16 v[106:109], v[98:101], v[34:37], v[106:109]\n\t"
        "v_lshrrev_b32 v24, 4, v9\n\t"
        "v_xor_b32 v24, v24, v0\n\t"
        "v_mfma_f32_16x16x32_bf16 v[110:113], v[98:101], v[74:77], v[110:113]\n\t"
        "v_and_b32 v25, 0x380, v24\n\t"
        "v_and_b32 v24, 0x70, v24\n\t"
        "v_mfma_f32_16x16x32_bf16 v[106:109], v[102:105], v[38:41], v[106:109]\n\t"
        "v_lshl_or_b32 v24, v25, 3, v24\n\t"
        "v_mfma_f32_16x16x32_bf16 v[110:113], v[102:105], v[78:81], v[110:113]\n\t"
        "s_nop 5\n\t"
        "v_mul_f32 v114, s55, v106\n\t"
        "v_mul_f32 v115, s55, v107\n\t"
        "v_mul_f32 v116, s55, v110\n\t"
        "v_mul_f32 v117, s55, v111\n\t"
        "v_exp_f32 v118, v114\n\t"
        "v_exp_f32 v119, v115\n\t"
        "v_exp_f32 v120, v116\n\t"
        "v_exp_f32 v121, v117\n\t"
        "v_cvt_pk_bf16_f32 v46, v118, v119\n\t"
        "v_cvt_pk_bf16_f32 v47, v118, v119\n\t"
        "v_cvt_pk_bf16_f32 v48, v120, v121\n\t"
        "v_cvt_pk_bf16_f32 v49, v120, v121\n\t"
        "ds_write_b128 v9, v[46:49] offset:24576\n\t"
        "s_branch Lp3b_%=\n\t"
        "Lfb3_%=:\n\t"
        "s_nop 7\n\t"
        "s_waitcnt lgkmcnt(0)\n\t"
        "s_barrier\n\t"
        "v_max_f32 v50, v114, v115\n\t"
        "v_max_f32 v51, v116, v117\n\t"
        "v_mov_b32 v52, v50\n\t"
        "s_nop 1\n\t"
        "v_permlane16_swap_b32 v50, v52\n\t"
        "v_max_f32 v50, v50, v52\n\t"
        "v_mov_b32 v52, v51\n\t"
        "s_nop 1\n\t"
        "v_permlane16_swap_b32 v51, v52\n\t"
        "v_max_f32 v51, v51, v52\n\t"
        "v_mov_b32 v52, v50\n\t"
        "s_nop 1\n\t"
        "v_permlane32_swap_b32 v50, v52\n\t"
        "v_max_f32 v50, v50, v52\n\t"
        "v_mov_b32 v52, v51\n\t"
        "s_nop 1\n\t"
        "v_permlane32_swap_b32 v51, v52\n\t"
        "v_max_f32 v51, v51, v52\n\t"
        "ds_write_b64 v9, v[50:51] offset:24576\n\t"
        "s_waitcnt lgkmcnt(0)\n\t"
        "s_barrier\n\t"
        "ds_read_b64 v[50:51], v0 offset:24576\n\t"
        "ds_read_b64 v[52:53], v0 offset:25600\n\t"
        "ds_read_b64 v[54:55], v0 offset:26624\n\t"
        "ds_read_b64 v[56:57], v0 offset:27648\n\t"
        "s_waitcnt lgkmcnt(0)\n\t"
        "s_barrier\n\t"
        "v_max3_f32 v50, v50, v51, v56\n\t"
        "v_max3_f32 v51, v52, v53, v57\n\t"
        "v_max3_f32 v51, v51, v54, v55\n\t"
        "s_cmp_eq_u32 s44, 0\n\t"
        "s_cselect_b64 vcc, -1, 0\n\t"
        "v_cndmask_b32 v51, v51, v50, vcc\n\t"
        "s_cmp_eq_u32 s44, 3\n\t"
        "s_cselect_b64 vcc, -1, 0\n\t"
        "v_cndmask_b32 v50, v51, v50, vcc\n\t"
        "v_sub_f32 v114, v114, v50\n\t"
        "v_sub_f32 v115, v115, v50\n\t"
        "v_sub_f32 v116, v116, v51\n\t"
        "v_sub_f32 v117, v117, v51\n\t"
        "v_exp_f32 v118, v114\n\t"
        "v_exp_f32 v119, v115\n\t"
        "v_exp_f32 v120, v116\n\t"
        "v_exp_f32 v121, v117\n\t"
        "s_cbranch_scc1 Lfb3m_%=\n\t"
        "v_cvt_pk_bf16_f32 v46, v118, v119\n\t"
        "v_cvt_pk_bf16_f32 v47, v120, v121\n\t"
        "ds_write_b64 v9, v[46:47] offset:24576\n\t"
        "s_branch Lfb3j_%=\n\t"
        "Lfb3m_%=:\n\t"
        "v_cvt_pk_bf16_f32 v46, v118, v119\n\t"
        "v_cvt_pk_bf16_f32 v47, v118, v119\n\t"
        "v_cvt_pk_bf16_f32 v48, v120, v121\n\t"
        "v_cvt_pk_bf16_f32 v49, v120, v121\n\t"
        "ds_write_b128 v9, v[46:49] offset:24576\n\t"
        "Lfb3j_%=:\n\t"
        "s_waitcnt lgkmcnt(0)\n\t"
        "s_barrier\n\t"
        "ds_read_b64 v[60:61], v0 offset:25600\n\t"
        "ds_read_b64 v[62:63], v0 offset:26624\n\t"
        "ds_read_b64 v[58:59], v0 offset:24576\n\t"
        "ds_read_b128 v[64:67], v0 offset:27648\n\t"
        "s_waitcnt lgkmcnt(2)\n\t"
        "v_mfma_f32_16x16x32_bf16 v[78:81], v[68:71], v[60:63], 0\n\t"
        "s_waitcnt lgkmcnt(1)\n\t"
        "v_mfma_f32_16x16x16_bf16 v[82:85], v[10:11], v[58:59], 0\n\t"
        "v_mfma_f32_16x16x16_bf16 v[86:89], v[14:15], v[58:59], 0\n\t"
        "v_mfma_f32_16x16x32_bf16 v[90:93], v[10:13], v[60:63], 0\n\t"
        "s_waitcnt lgkmcnt(0)\n\t"
        "s_nop 0\n\t"
        "v_mfma_f32_16x16x16_bf16 v[78:81], v[72:73], v[66:67], v[78:81]\n\t"
        "v_mfma_f32_16x16x16_bf16 v[74:77], v[72:73], v[64:65], 0\n\t"
        "v_mfma_f32_16x16x16_bf16 v[74:77], v[68:69], v[58:59], v[74:77]\n\t"
        "v_mfma_f32_16x16x32_bf16 v[94:97], v[14:17], v[60:63], 0\n\t"
        "v_mfma_f32_16x16x16_bf16 v[82:85], v[122:123], v[64:65], v[82:85]\n\t"
        "s_nop 4\n\t"
        "v_mfma_f32_16x16x16_bf16 v[94:97], v[124:125], v[66:67], v[94:97]\n\t"
        "v_mfma_f32_16x16x16_bf16 v[86:89], v[124:125], v[64:65], v[86:89]\n\t"
        "v_mfma_f32_16x16x16_bf16 v[90:93], v[122:123], v[66:67], v[90:93]\n\t"
        "v_rcp_f32 v98, v74\n\t"
        "v_rcp_f32 v100, v78\n\t"
        "s_branch Lfb3r_%=\n\t"
        "Lend_%=:\n\t"
        "v_lshlrev_b32 v20, 3, v0\n\t"
        "s_memrealtime s[72:73]\n\t"
        "s_sub_u32 s47, s47, 1\n\t"
        "s_lshl_b32 s47, s47, 24\n\t"
        "s_waitcnt lgkmcnt(0)\n\t"
        "s_and_b32 s72, s72, 0xffffff\n\t"
        "s_or_b32 s72, s72, s47\n\t"
        "v_mov_b32 v21, s72\n\t"
        "s_waitcnt vmcnt(0)\n\t"
        "s_barrier\n\t"
        "s_mov_b64 exec, 3\n\t"
        "buffer_store_dword v21, v20, s[40:43], s76 offen sc1\n\t"
        "s_mov_b64 exec, -1\n\t"
        "s_endpgm\n\t"
        ".Lmk_e:\n\t"
        :
        : [pw] "s"(ws), [id] "s"(id), [gc] "s"(gc), [pq] "s"(Wq), [pk] "s"(Wk), [pv] "s"(Wv), [po] "s"(Wo)
        : "memory", "vcc", "scc", "v255", "a255", "s40", "s41", "s42", "s43", "s44", "s45", "s46", "s47", "s78", "s79", "s80", "s81", "s82", "s83", "s84", "s85", "s86", "s87", "s88", "s89", "s90", "s91", "s92", "s93", "s94", "s95", "s96", "s97", "s98", "s99");
    __builtin_unreachable();
}

DI float bf2f(u16 v) { return __uint_as_float((unsigned)v << 16); }
#define DBGA(role, l, on, cu, on2) 0u

DI void wo_role(unsigned char* ws, const u16* Wo, int l, int kq, int nq, int w, int lane, int id, unsigned gc) {
    const int cc = lane & 15, g = lane >> 4, p = kq * 2 + nq;
    asm volatile(
        "s_mov_b64 s[40:41], %[pw]\n\t"
        "s_mov_b32 s42, 184549376\n\t"
        "s_mov_b32 s43, 0x20000\n\t"
        "s_mov_b32 s48, %[w]\n\t"
        "s_mov_b32 s52, %[fi]\n\t"
        "s_mov_b32 s53, %[in]\n\t"
        "s_mov_b32 s54, %[out]\n\t"
        "s_mov_b32 s55, %[fo]\n\t"
        "s_mov_b32 s56, %[db]\n\t"
        "s_mov_b32 s75, %[fs]\n\t"
        "s_memrealtime s[72:73]\n\t"
        "s_mov_b32 s49, 131072\n\t"
        "s_mov_b32 s51, 16777216\n\t"
        "s_waitcnt lgkmcnt(0)\n\t"
        "s_mov_b32 s74, s72\n\t"
        "s_mov_b32 s73, 1480\n\t"
        "v_mbcnt_lo_u32_b32 v38, -1, 0\n\t"
        "v_mbcnt_hi_u32_b32 v38, -1, v38\n\t"
        "v_lshlrev_b32 v32, 4, v38\n\t"
        "v_mul_u32_u24 v36, 512, v38\n\t"
        "v_mov_b32 v33, %[fa]\n\t"
        "v_mov_b32 v35, %[sv]\n\t"
        "s_mov_b64 s[44:45], %[pa]\n\t"
        "s_mov_b32 s46, 4194304\n\t"
        "s_mov_b32 s47, 0x20000\n\t"
        "s_mov_b32 s57, %[wb]\n\t"
        "v_mov_b32 v40, %[ga]\n\t"
        "s_mov_b32 s62, 0x05040100\n\t"
        "s_mov_b32 s63, 0x07060302\n\t"
        "s_add_u32 s58, s57, 0\n\t"
        "buffer_load_dwordx2 v[64:65], v40, s[44:47], s58 offen offset:0\n\t"
        "buffer_load_dwordx2 v[66:67], v40, s[44:47], s58 offen offset:1024\n\t"
        "buffer_load_dwordx2 v[68:69], v40, s[44:47], s58 offen offset:2048\n\t"
        "buffer_load_dwordx2 v[70:71], v40, s[44:47], s58 offen offset:3072\n\t"
        "s_add_u32 s58, s57, 16384\n\t"
        "buffer_load_dwordx2 v[72:73], v40, s[44:47], s58 offen offset:0\n\t"
        "buffer_load_dwordx2 v[74:75], v40, s[44:47], s58 offen offset:1024\n\t"
        "buffer_load_dwordx2 v[76:77], v40, s[44:47], s58 offen offset:2048\n\t"
        "buffer_load_dwordx2 v[78:79], v40, s[44:47], s58 offen offset:3072\n\t"
        "s_add_u32 s58, s57, 32768\n\t"
        "buffer_load_dwordx2 v[80:81], v40, s[44:47], s58 offen offset:0\n\t"
        "buffer_load_dwordx2 v[82:83], v40, s[44:47], s58 offen offset:1024\n\t"
        "buffer_load_dwordx2 v[84:85], v40, s[44:47], s58 offen offset:2048\n\t"
        "buffer_load_dwordx2 v[86:87], v40, s[44:47], s58 offen offset:3072\n\t"
        "s_add_u32 s58, s57, 49152\n\t"
        "buffer_load_dwordx2 v[88:89], v40, s[44:47], s58 offen offset:0\n\t"
        "buffer_load_dwordx2 v[90:91], v40, s[44:47], s58 offen offset:1024\n\t"
        "buffer_load_dwordx2 v[92:93], v40, s[44:47], s58 offen offset:2048\n\t"
        "buffer_load_dwordx2 v[94:95], v40, s[44:47], s58 offen offset:3072\n\t"
        "s_add_u32 s58, s57, 65536\n\t"
        "buffer_load_dwordx2 v[96:97], v40, s[44:47], s58 offen offset:0\n\t"
        "buffer_load_dwordx2 v[98:99], v40, s[44:47], s58 offen offset:1024\n\t"
        "buffer_load_dwordx2 v[100:101], v40, s[44:47], s58 offen offset:2048\n\t"
        "buffer_load_dwordx2 v[102:103], v40, s[44:47], s58 offen offset:3072\n\t"
        "s_add_u32 s58, s57, 81920\n\t"
        "buffer_load_dwordx2 v[104:105], v40, s[44:47], s58 offen offset:0\n\t"
        "buffer_load_dwordx2 v[106:107], v40, s[44:47], s58 offen offset:1024\n\t"
        "buffer_load_dwordx2 v[108:109], v40, s[44:47], s58 offen offset:2048\n\t"
        "buffer_load_dwordx2 v[110:111], v40, s[44:47], s58 offen offset:3072\n\t"
        "s_add_u32 s58, s57, 98304\n\t"
        "buffer_load_dwordx2 v[112:113], v40, s[44:47], s58 offen offset:0\n\t"
        "buffer_load_dwordx2 v[114:115], v40, s[44:47], s58 offen offset:1024\n\t"
        "buffer_load_dwordx2 v[116:117], v40, s[44:47], s58 offen offset:2048\n\t"
        "buffer_load_dwordx2 v[118:119], v40, s[44:47], s58 offen offset:3072\n\t"
        "s_add_u32 s58, s57, 114688\n\t"
        "buffer_load_dwordx2 v[120:121], v40, s[44:47], s58 offen offset:0\n\t"
        "buffer_load_dwordx2 v[122:123], v40, s[44:47], s58 offen offset:1024\n\t"
        "buffer_load_dwordx2 v[124:125], v40, s[44:47], s58 offen offset:2048\n\t"
        "buffer_load_dwordx2 v[126:127], v40, s[44:47], s58 offen offset:3072\n\t"
        "s_add_u32 s58, s57, 131072\n\t"
        "buffer_load_dwordx2 v[128:129], v40, s[44:47], s58 offen offset:0\n\t"
        "buffer_load_dwordx2 v[130:131], v40, s[44:47], s58 offen offset:1024\n\t"
        "buffer_load_dwordx2 v[132:133], v40, s[44:47], s58 offen offset:2048\n\t"
        "buffer_load_dwordx2 v[134:135], v40, s[44:47], s58 offen offset:3072\n\t"
        "s_add_u32 s58, s57, 147456\n\t"
        "buffer_load_dwordx2 v[136:137], v40, s[44:47], s58 offen offset:0\n\t"
        "buffer_load_dwordx2 v[138:139], v40, s[44:47], s58 offen offset:1024\n\t"
        "buffer_load_dwordx2 v[140:141], v40, s[44:47], s58 offen offset:2048\n\t"
        "buffer_load_dwordx2 v[142:143], v40, s[44:47], s58 offen offset:3072\n\t"
        "s_add_u32 s58, s57, 163840\n\t"
        "buffer_load_dwordx2 v[144:145], v40, s[44:47], s58 offen offset:0\n\t"
        "buffer_load_dwordx2 v[146:147], v40, s[44:47], s58 offen offset:1024\n\t"
        "buffer_load_dwordx2 v[148:149], v40, s[44:47], s58 offen offset:2048\n\t"
        "buffer_load_dwordx2 v[150:151], v40, s[44:47], s58 offen offset:3072\n\t"
        "s_add_u32 s58, s57, 180224\n\t"
        "buffer_load_dwordx2 v[152:153], v40, s[44:47], s58 offen offset:0\n\t"
        "buffer_load_dwordx2 v[154:155], v40, s[44:47], s58 offen offset:1024\n\t"
        "buffer_load_dwordx2 v[156:157], v40, s[44:47], s58 offen offset:2048\n\t"
        "buffer_load_dwordx2 v[158:159], v40, s[44:47], s58 offen offset:3072\n\t"
        "s_add_u32 s58, s57, 196608\n\t"
        "buffer_load_dwordx2 v[160:161], v40, s[44:47], s58 offen offset:0\n\t"
        "buffer_load_dwordx2 v[162:163], v40, s[44:47], s58 offen offset:1024\n\t"
        "buffer_load_dwordx2 v[164:165], v40, s[44:47], s58 offen offset:2048\n\t"
        "buffer_load_dwordx2 v[166:167], v40, s[44:47], s58 offen offset:3072\n\t"
        "s_add_u32 s58, s57, 212992\n\t"
        "buffer_load_dwordx2 v[168:169], v40, s[44:47], s58 offen offset:0\n\t"
        "buffer_load_dwordx2 v[170:171], v40, s[44:47], s58 offen offset:1024\n\t"
        "buffer_load_dwordx2 v[172:173], v40, s[44:47], s58 offen offset:2048\n\t"
        "buffer_load_dwordx2 v[174:175], v40, s[44:47], s58 offen offset:3072\n\t"
        "s_waitcnt vmcnt(54)\n\t"
        "s_waitcnt vmcnt(52)\n\t"
        "s_waitcnt vmcnt(50)\n\t"
        "v_perm_b32 v48, v66, v64, s62\n\t"
        "v_perm_b32 v49, v70, v68, s62\n\t"
        "v_perm_b32 v50, v74, v72, s62\n\t"
        "s_waitcnt vmcnt(48)\n\t"
        "v_perm_b32 v51, v78, v76, s62\n\t"
        "v_accvgpr_write_b32 a0, v48\n\t"
        "v_accvgpr_write_b32 a1, v49\n\t"
        "v_accvgpr_write_b32 a2, v50\n\t"
        "v_accvgpr_write_b32 a3, v51\n\t"
        "v_perm_b32 v52, v66, v64, s63\n\t"
        "v_perm_b32 v53, v70, v68, s63\n\t"
        "v_perm_b32 v54, v74, v72, s63\n\t"
        "v_perm_b32 v55, v78, v76, s63\n\t"
        "v_accvgpr_write_b32 a64, v52\n\t"
        "v_accvgpr_write_b32 a65, v53\n\t"
        "v_accvgpr_write_b32 a66, v54\n\t"
        "v_accvgpr_write_b32 a67, v55\n\t"
        "v_perm_b32 v48, v67, v65, s62\n\t"
        "v_perm_b32 v49, v71, v69, s62\n\t"
        "v_perm_b32 v50, v75, v73, s62\n\t"
        "v_perm_b32 v51, v79, v77, s62\n\t"
        "v_accvgpr_write_b32 a128, v48\n\t"
        "v_accvgpr_write_b32 a129, v49\n\t"
        "v_accvgpr_write_b32 a130, v50\n\t"
        "v_accvgpr_write_b32 a131, v51\n\t"
        "v_perm_b32 v52, v67, v65, s63\n\t"
        "v_perm_b32 v53, v71, v69, s63\n\t"
        "v_perm_b32 v54, v75, v73, s63\n\t"
        "v_perm_b32 v55, v79, v77, s63\n\t"
        "v_accvgpr_write_b32 a192, v52\n\t"
        "v_accvgpr_write_b32 a193, v53\n\t"
        "v_accvgpr_write_b32 a194, v54\n\t"
        "v_accvgpr_write_b32 a195, v55\n\t"
        "s_add_u32 s58, s57, 229376\n\t"
        "buffer_load_dwordx2 v[64:65], v40, s[44:47], s58 offen offset:0\n\t"
        "buffer_load_dwordx2 v[66:67], v40, s[44:47], s58 offen offset:1024\n\t"
        "buffer_load_dwordx2 v[68:69], v40, s[44:47], s58 offen offset:2048\n\t"
        "buffer_load_dwordx2 v[70:71], v40, s[44:47], s58 offen offset:3072\n\t"
        "s_add_u32 s58, s57, 245760\n\t"
        "buffer_load_dwordx2 v[72:73], v40, s[44:47], s58 offen offset:0\n\t"
        "buffer_load_dwordx2 v[74:75], v40, s[44:47], s58 offen offset:1024\n\t"
        "buffer_load_dwordx2 v[76:77], v40, s[44:47], s58 offen offset:2048\n\t"
        "buffer_load_dwordx2 v[78:79], v40, s[44:47], s58 offen offset:3072\n\t"
        "s_waitcnt vmcnt(54)\n\t"
        "s_waitcnt vmcnt(52)\n\t"
        "s_waitcnt vmcnt(50)\n\t"
        "v_perm_b32 v48, v82, v80, s62\n\t"
        "v_perm_b32 v49, v86, v84, s62\n\t"
        "v_perm_b32 v50, v90, v88, s62\n\t"
        "s_waitcnt vmcnt(48)\n\t"
        "v_perm_b32 v51, v94, v92, s62\n\t"
        "v_accvgpr_write_b32 a4, v48\n\t"
        "v_accvgpr_write_b32 a5, v49\n\t"
        "v_accvgpr_write_b32 a6, v50\n\t"
        "v_accvgpr_write_b32 a7, v51\n\t"
        "v_perm_b32 v52, v82, v80, s63\n\t"
        "v_perm_b32 v53, v86, v84, s63\n\t"
        "v_perm_b32 v54, v90, v88, s63\n\t"
        "v_perm_b32 v55, v94, v92, s63\n\t"
        "v_accvgpr_write_b32 a68, v52\n\t"
        "v_accvgpr_write_b32 a69, v53\n\t"
        "v_accvgpr_write_b32 a70, v54\n\t"
        "v_accvgpr_write_b32 a71, v55\n\t"
        "v_perm_b32 v48, v83, v81, s62\n\t"
        "v_perm_b32 v49, v87, v85, s62\n\t"
        "v_perm_b32 v50, v91, v89, s62\n\t"
        "v_perm_b32 v51, v95, v93, s62\n\t"
        "v_accvgpr_write_b32 a132, v48\n\t"
        "v_accvgpr_write_b32 a133, v49\n\t"
        "v_accvgpr_write_b32 a134, v50\n\t"
        "v_accvgpr_write_b32 a135, v51\n\t"
        "v_perm_b32 v52, v83, v81, s63\n\t"
        "v_perm_b32 v53, v87, v85, s63\n\t"
        "v_perm_b32 v54, v91, v89, s63\n\t"
        "v_perm_b32 v55, v95, v93, s63\n\t"
        "v_accvgpr_write_b32 a196, v52\n\t"
        "v_accvgpr_write_b32 a197, v53\n\t"
        "v_accvgpr_write_b32 a198, v54\n\t"
        "v_accvgpr_write_b32 a199, v55\n\t"
        "s_add_u32 s58, s57, 262144\n\t"
        "buffer_load_dwordx2 v[80:81], v40, s[44:47], s58 offen offset:0\n\t"
        "buffer_load_dwordx2 v[82:83], v40, s[44:47], s58 offen offset:1024\n\t"
        "buffer_load_dwordx2 v[84:85], v40, s[44:47], s58 offen offset:2048\n\t"
        "buffer_load_dwordx2 v[86:87], v40, s[44:47], s58 offen offset:3072\n\t"
        "s_add_u32 s58, s57, 278528\n\t"
        "buffer_load_dwordx2 v[88:89], v40, s[44:47], s58 offen offset:0\n\t"
        "buffer_load_dwordx2 v[90:91], v40, s[44:47], s58 offen offset:1024\n\t"
        "buffer_load_dwordx2 v[92:93], v40, s[44:47], s58 offen offset:2048\n\t"
        "buffer_load_dwordx2 v[94:95], v40, s[44:47], s58 offen offset:3072\n\t"
        "s_waitcnt vmcnt(54)\n\t"
        "s_waitcnt vmcnt(52)\n\t"
        "s_waitcnt vmcnt(50)\n\t"
        "v_perm_b32 v48, v98, v96, s62\n\t"
        "v_perm_b32 v49, v102, v100, s62\n\t"
        "v_perm_b32 v50, v106, v104, s62\n\t"
        "s_waitcnt vmcnt(48)\n\t"
        "v_perm_b32 v51, v110, v108, s62\n\t"
        "v_accvgpr_write_b32 a8, v48\n\t"
        "v_accvgpr_write_b32 a9, v49\n\t"
        "v_accvgpr_write_b32 a10, v50\n\t"
        "v_accvgpr_write_b32 a11, v51\n\t"
        "v_perm_b32 v52, v98, v96, s63\n\t"
        "v_perm_b32 v53, v102, v100, s63\n\t"
        "v_perm_b32 v54, v106, v104, s63\n\t"
        "v_perm_b32 v55, v110, v108, s63\n\t"
        "v_accvgpr_write_b32 a72, v52\n\t"
        "v_accvgpr_write_b32 a73, v53\n\t"
        "v_accvgpr_write_b32 a74, v54\n\t"
        "v_accvgpr_write_b32 a75, v55\n\t"
        "v_perm_b32 v48, v99, v97, s62\n\t"
        "v_perm_b32 v49, v103, v101, s62\n\t"
        "v_perm_b32 v50, v107, v105, s62\n\t"
        "v_perm_b32 v51, v111, v109, s62\n\t"
        "v_accvgpr_write_b32 a136, v48\n\t"
        "v_accvgpr_write_b32 a137, v49\n\t"
        "v_accvgpr_write_b32 a138, v50\n\t"
        "v_accvgpr_write_b32 a139, v51\n\t"
        "v_perm_b32 v52, v99, v97, s63\n\t"
        "v_perm_b32 v53, v103, v101, s63\n\t"
        "v_perm_b32 v54, v107, v105, s63\n\t"
        "v_perm_b32 v55, v111, v109, s63\n\t"
        "v_accvgpr_write_b32 a200, v52\n\t"
        "v_accvgpr_write_b32 a201, v53\n\t"
        "v_accvgpr_write_b32 a202, v54\n\t"
        "v_accvgpr_write_b32 a203, v55\n\t"
        "s_add_u32 s58, s57, 294912\n\t"
        "buffer_load_dwordx2 v[96:97], v40, s[44:47], s58 offen offset:0\n\t"
        "buffer_load_dwordx2 v[98:99], v40, s[44:47], s58 offen offset:1024\n\t"
        "buffer_load_dwordx2 v[100:101], v40, s[44:47], s58 offen offset:2048\n\t"
        "buffer_load_dwordx2 v[102:103], v40, s[44:47], s58 offen offset:3072\n\t"
        "s_add_u32 s58, s57, 311296\n\t"
        "buffer_load_dwordx2 v[104:105], v40, s[44:47], s58 offen offset:0\n\t"
        "buffer_load_dwordx2 v[106:107], v40, s[44:47], s58 offen offset:1024\n\t"
        "buffer_load_dwordx2 v[108:109], v40, s[44:47], s58 offen offset:2048\n\t"
        "buffer_load_dwordx2 v[110:111], v40, s[44:47], s58 offen offset:3072\n\t"
        "s_waitcnt vmcnt(54)\n\t"
        "s_waitcnt vmcnt(52)\n\t"
        "s_waitcnt vmcnt(50)\n\t"
        "v_perm_b32 v48, v114, v112, s62\n\t"
        "v_perm_b32 v49, v118, v116, s62\n\t"
        "v_perm_b32 v50, v122, v120, s62\n\t"
        "s_waitcnt vmcnt(48)\n\t"
        "v_perm_b32 v51, v126, v124, s62\n\t"
        "v_accvgpr_write_b32 a12, v48\n\t"
        "v_accvgpr_write_b32 a13, v49\n\t"
        "v_accvgpr_write_b32 a14, v50\n\t"
        "v_accvgpr_write_b32 a15, v51\n\t"
        "v_perm_b32 v52, v114, v112, s63\n\t"
        "v_perm_b32 v53, v118, v116, s63\n\t"
        "v_perm_b32 v54, v122, v120, s63\n\t"
        "v_perm_b32 v55, v126, v124, s63\n\t"
        "v_accvgpr_write_b32 a76, v52\n\t"
        "v_accvgpr_write_b32 a77, v53\n\t"
        "v_accvgpr_write_b32 a78, v54\n\t"
        "v_accvgpr_write_b32 a79, v55\n\t"
        "v_perm_b32 v48, v115, v113, s62\n\t"
        "v_perm_b32 v49, v119, v117, s62\n\t"
        "v_perm_b32 v50, v123, v121, s62\n\t"
        "v_perm_b32 v51, v127, v125, s62\n\t"
        "v_accvgpr_write_b32 a140, v48\n\t"
        "v_accvgpr_write_b32 a141, v49\n\t"
        "v_accvgpr_write_b32 a142, v50\n\t"
        "v_accvgpr_write_b32 a143, v51\n\t"
        "v_perm_b32 v52, v115, v113, s63\n\t"
        "v_perm_b32 v53, v119, v117, s63\n\t"
        "v_perm_b32 v54, v123, v121, s63\n\t"
        "v_perm_b32 v55, v127, v125, s63\n\t"
        "v_accvgpr_write_b32 a204, v52\n\t"
        "v_accvgpr_write_b32 a205, v53\n\t"
        "v_accvgpr_write_b32 a206, v54\n\t"
        "v_accvgpr_write_b32 a207, v55\n\t"
        "s_add_u32 s58, s57, 327680\n\t"
        "buffer_load_dwordx2 v[112:113], v40, s[44:47], s58 offen offset:0\n\t"
        "buffer_load_dwordx2 v[114:115], v40, s[44:47], s58 offen offset:1024\n\t"
        "buffer_load_dwordx2 v[116:117], v40, s[44:47], s58 offen offset:2048\n\t"
        "buffer_load_dwordx2 v[118:119], v40, s[44:47], s58 offen offset:3072\n\t"
        "s_add_u32 s58, s57, 344064\n\t"
        "buffer_load_dwordx2 v[120:121], v40, s[44:47], s58 offen offset:0\n\t"
        "buffer_load_dwordx2 v[122:123], v40, s[44:47], s58 offen offset:1024\n\t"
        "buffer_load_dwordx2 v[124:125], v40, s[44:47], s58 offen offset:2048\n\t"
        "buffer_load_dwordx2 v[126:127], v40, s[44:47], s58 offen offset:3072\n\t"
        "s_waitcnt vmcnt(54)\n\t"
        "s_waitcnt vmcnt(52)\n\t"
        "s_waitcnt vmcnt(50)\n\t"
        "v_perm_b32 v48, v130, v128, s62\n\t"
        "v_perm_b32 v49, v134, v132, s62\n\t"
        "v_perm_b32 v50, v138, v136, s62\n\t"
        "s_waitcnt vmcnt(48)\n\t"
        "v_perm_b32 v51, v142, v140, s62\n\t"
        "v_accvgpr_write_b32 a16, v48\n\t"
        "v_accvgpr_write_b32 a17, v49\n\t"
        "v_accvgpr_write_b32 a18, v50\n\t"
        "v_accvgpr_write_b32 a19, v51\n\t"
        "v_perm_b32 v52, v130, v128, s63\n\t"
        "v_perm_b32 v53, v134, v132, s63\n\t"
        "v_perm_b32 v54, v138, v136, s63\n\t"
        "v_perm_b32 v55, v142, v140, s63\n\t"
        "v_accvgpr_write_b32 a80, v52\n\t"
        "v_accvgpr_write_b32 a81, v53\n\t"
        "v_accvgpr_write_b32 a82, v54\n\t"
        "v_accvgpr_write_b32 a83, v55\n\t"
        "v_perm_b32 v48, v131, v129, s62\n\t"
        "v_perm_b32 v49, v135, v133, s62\n\t"
        "v_perm_b32 v50, v139, v137, s62\n\t"
        "v_perm_b32 v51, v143, v141, s62\n\t"
        "v_accvgpr_write_b32 a144, v48\n\t"
        "v_accvgpr_write_b32 a145, v49\n\t"
        "v_accvgpr_write_b32 a146, v50\n\t"
        "v_accvgpr_write_b32 a147, v51\n\t"
        "v_perm_b32 v52, v131, v129, s63\n\t"
        "v_perm_b32 v53, v135, v133, s63\n\t"
        "v_perm_b32 v54, v139, v137, s63\n\t"
        "v_perm_b32 v55, v143, v141, s63\n\t"
        "v_accvgpr_write_b32 a208, v52\n\t"
        "v_accvgpr_write_b32 a209, v53\n\t"
        "v_accvgpr_write_b32 a210, v54\n\t"
        "v_accvgpr_write_b32 a211, v55\n\t"
        "s_add_u32 s58, s57, 360448\n\t"
        "buffer_load_dwordx2 v[128:129], v40, s[44:47], s58 offen offset:0\n\t"
        "buffer_load_dwordx2 v[130:131], v40, s[44:47], s58 offen offset:1024\n\t"
        "buffer_load_dwordx2 v[132:133], v40, s[44:47], s58 offen offset:2048\n\t"
        "buffer_load_dwordx2 v[134:135], v40, s[44:47], s58 offen offset:3072\n\t"
        "s_add_u32 s58, s57, 376832\n\t"
        "buffer_load_dwordx2 v[136:137], v40, s[44:47], s58 offen offset:0\n\t"
        "buffer_load_dwordx2 v[138:139], v40, s[44:47], s58 offen offset:1024\n\t"
        "buffer_load_dwordx2 v[140:141], v40, s[44:47], s58 offen offset:2048\n\t"
        "buffer_load_dwordx2 v[142:143], v40, s[44:47], s58 offen offset:3072\n\t"
        "s_waitcnt vmcnt(54)\n\t"
        "s_waitcnt vmcnt(52)\n\t"
        "s_waitcnt vmcnt(50)\n\t"
        "v_perm_b32 v48, v146, v144, s62\n\t"
        "v_perm_b32 v49, v150, v148, s62\n\t"
        "v_perm_b32 v50, v154, v152, s62\n\t"
        "s_waitcnt vmcnt(48)\n\t"
        "v_perm_b32 v51, v158, v156, s62\n\t"
        "v_accvgpr_write_b32 a20, v48\n\t"
        "v_accvgpr_write_b32 a21, v49\n\t"
        "v_accvgpr_write_b32 a22, v50\n\t"
        "v_accvgpr_write_b32 a23, v51\n\t"
        "v_perm_b32 v52, v146, v144, s63\n\t"
        "v_perm_b32 v53, v150, v148, s63\n\t"
        "v_perm_b32 v54, v154, v152, s63\n\t"
        "v_perm_b32 v55, v158, v156, s63\n\t"
        "v_accvgpr_write_b32 a84, v52\n\t"
        "v_accvgpr_write_b32 a85, v53\n\t"
        "v_accvgpr_write_b32 a86, v54\n\t"
        "v_accvgpr_write_b32 a87, v55\n\t"
        "v_perm_b32 v48, v147, v145, s62\n\t"
        "v_perm_b32 v49, v151, v149, s62\n\t"
        "v_perm_b32 v50, v155, v153, s62\n\t"
        "v_perm_b32 v51, v159, v157, s62\n\t"
        "v_accvgpr_write_b32 a148, v48\n\t"
        "v_accvgpr_write_b32 a149, v49\n\t"
        "v_accvgpr_write_b32 a150, v50\n\t"
        "v_accvgpr_write_b32 a151, v51\n\t"
        "v_perm_b32 v52, v147, v145, s63\n\t"
        "v_perm_b32 v53, v151, v149, s63\n\t"
        "v_perm_b32 v54, v155, v153, s63\n\t"
        "v_perm_b32 v55, v159, v157, s63\n\t"
        "v_accvgpr_write_b32 a212, v52\n\t"
        "v_accvgpr_write_b32 a213, v53\n\t"
        "v_accvgpr_write_b32 a214, v54\n\t"
        "v_accvgpr_write_b32 a215, v55\n\t"
        "s_add_u32 s58, s57, 393216\n\t"
        "buffer_load_dwordx2 v[144:145], v40, s[44:47], s58 offen offset:0\n\t"
        "buffer_load_dwordx2 v[146:147], v40, s[44:47], s58 offen offset:1024\n\t"
        "buffer_load_dwordx2 v[148:149], v40, s[44:47], s58 offen offset:2048\n\t"
        "buffer_load_dwordx2 v[150:151], v40, s[44:47], s58 offen offset:3072\n\t"
        "s_add_u32 s58, s57, 409600\n\t"
        "buffer_load_dwordx2 v[152:153], v40, s[44:47], s58 offen offset:0\n\t"
        "buffer_load_dwordx2 v[154:155], v40, s[44:47], s58 offen offset:1024\n\t"
        "buffer_load_dwordx2 v[156:157], v40, s[44:47], s58 offen offset:2048\n\t"
        "buffer_load_dwordx2 v[158:159], v40, s[44:47], s58 offen offset:3072\n\t"
        "s_waitcnt vmcnt(54)\n\t"
        "s_waitcnt vmcnt(52)\n\t"
        "s_waitcnt vmcnt(50)\n\t"
        "v_perm_b32 v48, v162, v160, s62\n\t"
        "v_perm_b32 v49, v166, v164, s62\n\t"
        "v_perm_b32 v50, v170, v168, s62\n\t"
        "s_waitcnt vmcnt(48)\n\t"
        "v_perm_b32 v51, v174, v172, s62\n\t"
        "v_accvgpr_write_b32 a24, v48\n\t"
        "v_accvgpr_write_b32 a25, v49\n\t"
        "v_accvgpr_write_b32 a26, v50\n\t"
        "v_accvgpr_write_b32 a27, v51\n\t"
        "v_perm_b32 v52, v162, v160, s63\n\t"
        "v_perm_b32 v53, v166, v164, s63\n\t"
        "v_perm_b32 v54, v170, v168, s63\n\t"
        "v_perm_b32 v55, v174, v172, s63\n\t"
        "v_accvgpr_write_b32 a88, v52\n\t"
        "v_accvgpr_write_b32 a89, v53\n\t"
        "v_accvgpr_write_b32 a90, v54\n\t"
        "v_accvgpr_write_b32 a91, v55\n\t"
        "v_perm_b32 v48, v163, v161, s62\n\t"
        "v_perm_b32 v49, v167, v165, s62\n\t"
        "v_perm_b32 v50, v171, v169, s62\n\t"
        "v_perm_b32 v51, v175, v173, s62\n\t"
        "v_accvgpr_write_b32 a152, v48\n\t"
        "v_accvgpr_write_b32 a153, v49\n\t"
        "v_accvgpr_write_b32 a154, v50\n\t"
        "v_accvgpr_write_b32 a155, v51\n\t"
        "v_perm_b32 v52, v163, v161, s63\n\t"
        "v_perm_b32 v53, v167, v165, s63\n\t"
        "v_perm_b32 v54, v171, v169, s63\n\t"
        "v_perm_b32 v55, v175, v173, s63\n\t"
        "v_accvgpr_write_b32 a216, v52\n\t"
        "v_accvgpr_write_b32 a217, v53\n\t"
        "v_accvgpr_write_b32 a218, v54\n\t"
        "v_accvgpr_write_b32 a219, v55\n\t"
        "s_add_u32 s58, s57, 425984\n\t"
        "buffer_load_dwordx2 v[160:161], v40, s[44:47], s58 offen offset:0\n\t"
        "buffer_load_dwordx2 v[162:163], v40, s[44:47], s58 offen offset:1024\n\t"
        "buffer_load_dwordx2 v[164:165], v40, s[44:47], s58 offen offset:2048\n\t"
        "buffer_load_dwordx2 v[166:167], v40, s[44:47], s58 offen offset:3072\n\t"
        "s_add_u32 s58, s57, 442368\n\t"
        "buffer_load_dwordx2 v[168:169], v40, s[44:47], s58 offen offset:0\n\t"
        "buffer_load_dwordx2 v[170:171], v40, s[44:47], s58 offen offset:1024\n\t"
        "buffer_load_dwordx2 v[172:173], v40, s[44:47], s58 offen offset:2048\n\t"
        "buffer_load_dwordx2 v[174:175], v40, s[44:47], s58 offen offset:3072\n\t"
        "s_waitcnt vmcnt(54)\n\t"
        "s_waitcnt vmcnt(52)\n\t"
        "s_waitcnt vmcnt(50)\n\t"
        "v_perm_b32 v48, v66, v64, s62\n\t"
        "v_perm_b32 v49, v70, v68, s62\n\t"
        "v_perm_b32 v50, v74, v72, s62\n\t"
        "s_waitcnt vmcnt(48)\n\t"
        "v_perm_b32 v51, v78, v76, s62\n\t"
        "v_accvgpr_write_b32 a28, v48\n\t"
        "v_accvgpr_write_b32 a29, v49\n\t"
        "v_accvgpr_write_b32 a30, v50\n\t"
        "v_accvgpr_write_b32 a31, v51\n\t"
        "v_perm_b32 v52, v66, v64, s63\n\t"
        "v_perm_b32 v53, v70, v68, s63\n\t"
        "v_perm_b32 v54, v74, v72, s63\n\t"
        "v_perm_b32 v55, v78, v76, s63\n\t"
        "v_accvgpr_write_b32 a92, v52\n\t"
        "v_accvgpr_write_b32 a93, v53\n\t"
        "v_accvgpr_write_b32 a94, v54\n\t"
        "v_accvgpr_write_b32 a95, v55\n\t"
        "v_perm_b32 v48, v67, v65, s62\n\t"
        "v_perm_b32 v49, v71, v69, s62\n\t"
        "v_perm_b32 v50, v75, v73, s62\n\t"
        "v_perm_b32 v51, v79, v77, s62\n\t"
        "v_accvgpr_write_b32 a156, v48\n\t"
        "v_accvgpr_write_b32 a157, v49\n\t"
        "v_accvgpr_write_b32 a158, v50\n\t"
        "v_accvgpr_write_b32 a159, v51\n\t"
        "v_perm_b32 v52, v67, v65, s63\n\t"
        "v_perm_b32 v53, v71, v69, s63\n\t"
        "v_perm_b32 v54, v75, v73, s63\n\t"
        "v_perm_b32 v55, v79, v77, s63\n\t"
        "v_accvgpr_write_b32 a220, v52\n\t"
        "v_accvgpr_write_b32 a221, v53\n\t"
        "v_accvgpr_write_b32 a222, v54\n\t"
        "v_accvgpr_write_b32 a223, v55\n\t"
        "s_add_u32 s58, s57, 458752\n\t"
        "buffer_load_dwordx2 v[64:65], v40, s[44:47], s58 offen offset:0\n\t"
        "buffer_load_dwordx2 v[66:67], v40, s[44:47], s58 offen offset:1024\n\t"
        "buffer_load_dwordx2 v[68:69], v40, s[44:47], s58 offen offset:2048\n\t"
        "buffer_load_dwordx2 v[70:71], v40, s[44:47], s58 offen offset:3072\n\t"
        "s_add_u32 s58, s57, 475136\n\t"
        "buffer_load_dwordx2 v[72:73], v40, s[44:47], s58 offen offset:0\n\t"
        "buffer_load_dwordx2 v[74:75], v40, s[44:47], s58 offen offset:1024\n\t"
        "buffer_load_dwordx2 v[76:77], v40, s[44:47], s58 offen offset:2048\n\t"
        "buffer_load_dwordx2 v[78:79], v40, s[44:47], s58 offen offset:3072\n\t"
        "s_waitcnt vmcnt(54)\n\t"
        "s_waitcnt vmcnt(52)\n\t"
        "s_waitcnt vmcnt(50)\n\t"
        "v_perm_b32 v48, v82, v80, s62\n\t"
        "v_perm_b32 v49, v86, v84, s62\n\t"
        "v_perm_b32 v50, v90, v88, s62\n\t"
        "s_waitcnt vmcnt(48)\n\t"
        "v_perm_b32 v51, v94, v92, s62\n\t"
        "v_accvgpr_write_b32 a32, v48\n\t"
        "v_accvgpr_write_b32 a33, v49\n\t"
        "v_accvgpr_write_b32 a34, v50\n\t"
        "v_accvgpr_write_b32 a35, v51\n\t"
        "v_perm_b32 v52, v82, v80, s63\n\t"
        "v_perm_b32 v53, v86, v84, s63\n\t"
        "v_perm_b32 v54, v90, v88, s63\n\t"
        "v_perm_b32 v55, v94, v92, s63\n\t"
        "v_accvgpr_write_b32 a96, v52\n\t"
        "v_accvgpr_write_b32 a97, v53\n\t"
        "v_accvgpr_write_b32 a98, v54\n\t"
        "v_accvgpr_write_b32 a99, v55\n\t"
        "v_perm_b32 v48, v83, v81, s62\n\t"
        "v_perm_b32 v49, v87, v85, s62\n\t"
        "v_perm_b32 v50, v91, v89, s62\n\t"
        "v_perm_b32 v51, v95, v93, s62\n\t"
        "v_accvgpr_write_b32 a160, v48\n\t"
        "v_accvgpr_write_b32 a161, v49\n\t"
        "v_accvgpr_write_b32 a162, v50\n\t"
        "v_accvgpr_write_b32 a163, v51\n\t"
        "v_perm_b32 v52, v83, v81, s63\n\t"
        "v_perm_b32 v53, v87, v85, s63\n\t"
        "v_perm_b32 v54, v91, v89, s63\n\t"
        "v_perm_b32 v55, v95, v93, s63\n\t"
        "v_accvgpr_write_b32 a224, v52\n\t"
        "v_accvgpr_write_b32 a225, v53\n\t"
        "v_accvgpr_write_b32 a226, v54\n\t"
        "v_accvgpr_write_b32 a227, v55\n\t"
        "s_add_u32 s58, s57, 491520\n\t"
        "buffer_load_dwordx2 v[80:81], v40, s[44:47], s58 offen offset:0\n\t"
        "buffer_load_dwordx2 v[82:83], v40, s[44:47], s58 offen offset:1024\n\t"
        "buffer_load_dwordx2 v[84:85], v40, s[44:47], s58 offen offset:2048\n\t"
        "buffer_load_dwordx2 v[86:87], v40, s[44:47], s58 offen offset:3072\n\t"
        "s_add_u32 s58, s57, 507904\n\t"
        "buffer_load_dwordx2 v[88:89], v40, s[44:47], s58 offen offset:0\n\t"
        "buffer_load_dwordx2 v[90:91], v40, s[44:47], s58 offen offset:1024\n\t"
        "buffer_load_dwordx2 v[92:93], v40, s[44:47], s58 offen offset:2048\n\t"
        "buffer_load_dwordx2 v[94:95], v40, s[44:47], s58 offen offset:3072\n\t"
        "s_waitcnt vmcnt(54)\n\t"
        "s_waitcnt vmcnt(52)\n\t"
        "s_waitcnt vmcnt(50)\n\t"
        "v_perm_b32 v48, v98, v96, s62\n\t"
        "v_perm_b32 v49, v102, v100, s62\n\t"
        "v_perm_b32 v50, v106, v104, s62\n\t"
        "s_waitcnt vmcnt(48)\n\t"
        "v_perm_b32 v51, v110, v108, s62\n\t"
        "v_accvgpr_write_b32 a36, v48\n\t"
        "v_accvgpr_write_b32 a37, v49\n\t"
        "v_accvgpr_write_b32 a38, v50\n\t"
        "v_accvgpr_write_b32 a39, v51\n\t"
        "v_perm_b32 v52, v98, v96, s63\n\t"
        "v_perm_b32 v53, v102, v100, s63\n\t"
        "v_perm_b32 v54, v106, v104, s63\n\t"
        "v_perm_b32 v55, v110, v108, s63\n\t"
        "v_accvgpr_write_b32 a100, v52\n\t"
        "v_accvgpr_write_b32 a101, v53\n\t"
        "v_accvgpr_write_b32 a102, v54\n\t"
        "v_accvgpr_write_b32 a103, v55\n\t"
        "v_perm_b32 v48, v99, v97, s62\n\t"
        "v_perm_b32 v49, v103, v101, s62\n\t"
        "v_perm_b32 v50, v107, v105, s62\n\t"
        "v_perm_b32 v51, v111, v109, s62\n\t"
        "v_accvgpr_write_b32 a164, v48\n\t"
        "v_accvgpr_write_b32 a165, v49\n\t"
        "v_accvgpr_write_b32 a166, v50\n\t"
        "v_accvgpr_write_b32 a167, v51\n\t"
        "v_perm_b32 v52, v99, v97, s63\n\t"
        "v_perm_b32 v53, v103, v101, s63\n\t"
        "v_perm_b32 v54, v107, v105, s63\n\t"
        "v_perm_b32 v55, v111, v109, s63\n\t"
        "v_accvgpr_write_b32 a228, v52\n\t"
        "v_accvgpr_write_b32 a229, v53\n\t"
        "v_accvgpr_write_b32 a230, v54\n\t"
        "v_accvgpr_write_b32 a231, v55\n\t"
        "s_waitcnt vmcnt(46)\n\t"
        "s_waitcnt vmcnt(44)\n\t"
        "s_waitcnt vmcnt(42)\n\t"
        "v_perm_b32 v48, v114, v112, s62\n\t"
        "v_perm_b32 v49, v118, v116, s62\n\t"
        "v_perm_b32 v50, v122, v120, s62\n\t"
        "s_waitcnt vmcnt(40)\n\t"
        "v_perm_b32 v51, v126, v124, s62\n\t"
        "v_accvgpr_write_b32 a40, v48\n\t"
        "v_accvgpr_write_b32 a41, v49\n\t"
        "v_accvgpr_write_b32 a42, v50\n\t"
        "v_accvgpr_write_b32 a43, v51\n\t"
        "v_perm_b32 v52, v114, v112, s63\n\t"
        "v_perm_b32 v53, v118, v116, s63\n\t"
        "v_perm_b32 v54, v122, v120, s63\n\t"
        "v_perm_b32 v55, v126, v124, s63\n\t"
        "v_accvgpr_write_b32 a104, v52\n\t"
        "v_accvgpr_write_b32 a105, v53\n\t"
        "v_accvgpr_write_b32 a106, v54\n\t"
        "v_accvgpr_write_b32 a107, v55\n\t"
        "v_perm_b32 v48, v115, v113, s62\n\t"
        "v_perm_b32 v49, v119, v117, s62\n\t"
        "v_perm_b32 v50, v123, v121, s62\n\t"
        "v_perm_b32 v51, v127, v125, s62\n\t"
        "v_accvgpr_write_b32 a168, v48\n\t"
        "v_accvgpr_write_b32 a169, v49\n\t"
        "v_accvgpr_write_b32 a170, v50\n\t"
        "v_accvgpr_write_b32 a171, v51\n\t"
        "v_perm_b32 v52, v115, v113, s63\n\t"
        "v_perm_b32 v53, v119, v117, s63\n\t"
        "v_perm_b32 v54, v123, v121, s63\n\t"
        "v_perm_b32 v55, v127, v125, s63\n\t"
        "v_accvgpr_write_b32 a232, v52\n\t"
        "v_accvgpr_write_b32 a233, v53\n\t"
        "v_accvgpr_write_b32 a234, v54\n\t"
        "v_accvgpr_write_b32 a235, v55\n\t"
        "s_waitcnt vmcnt(38)\n\t"
        "s_waitcnt vmcnt(36)\n\t"
        "s_waitcnt vmcnt(34)\n\t"
        "v_perm_b32 v48, v130, v128, s62\n\t"
        "v_perm_b32 v49, v134, v132, s62\n\t"
        "v_perm_b32 v50, v138, v136, s62\n\t"
        "s_waitcnt vmcnt(32)\n\t"
        "v_perm_b32 v51, v142, v140, s62\n\t"
        "v_accvgpr_write_b32 a44, v48\n\t"
        "v_accvgpr_write_b32 a45, v49\n\t"
        "v_accvgpr_write_b32 a46, v50\n\t"
        "v_accvgpr_write_b32 a47, v51\n\t"
        "v_perm_b32 v52, v130, v128, s63\n\t"
        "v_perm_b32 v53, v134, v132, s63\n\t"
        "v_perm_b32 v54, v138, v136, s63\n\t"
        "v_perm_b32 v55, v142, v140, s63\n\t"
        "v_accvgpr_write_b32 a108, v52\n\t"
        "v_accvgpr_write_b32 a109, v53\n\t"
        "v_accvgpr_write_b32 a110, v54\n\t"
        "v_accvgpr_write_b32 a111, v55\n\t"
        "v_perm_b32 v48, v131, v129, s62\n\t"
        "v_perm_b32 v49, v135, v133, s62\n\t"
        "v_perm_b32 v50, v139, v137, s62\n\t"
        "v_perm_b32 v51, v143, v141, s62\n\t"
        "v_accvgpr_write_b32 a172, v48\n\t"
        "v_accvgpr_write_b32 a173, v49\n\t"
        "v_accvgpr_write_b32 a174, v50\n\t"
        "v_accvgpr_write_b32 a175, v51\n\t"
        "v_perm_b32 v52, v131, v129, s63\n\t"
        "v_perm_b32 v53, v135, v133, s63\n\t"
        "v_perm_b32 v54, v139, v137, s63\n\t"
        "v_perm_b32 v55, v143, v141, s63\n\t"
        "v_accvgpr_write_b32 a236, v52\n\t"
        "v_accvgpr_write_b32 a237, v53\n\t"
        "v_accvgpr_write_b32 a238, v54\n\t"
        "v_accvgpr_write_b32 a239, v55\n\t"
        "s_waitcnt vmcnt(30)\n\t"
        "s_waitcnt vmcnt(28)\n\t"
        "s_waitcnt vmcnt(26)\n\t"
        "v_perm_b32 v48, v146, v144, s62\n\t"
        "v_perm_b32 v49, v150, v148, s62\n\t"
        "v_perm_b32 v50, v154, v152, s62\n\t"
        "s_waitcnt vmcnt(24)\n\t"
        "v_perm_b32 v51, v158, v156, s62\n\t"
        "v_accvgpr_write_b32 a48, v48\n\t"
        "v_accvgpr_write_b32 a49, v49\n\t"
        "v_accvgpr_write_b32 a50, v50\n\t"
        "v_accvgpr_write_b32 a51, v51\n\t"
        "v_perm_b32 v52, v146, v144, s63\n\t"
        "v_perm_b32 v53, v150, v148, s63\n\t"
        "v_perm_b32 v54, v154, v152, s63\n\t"
        "v_perm_b32 v55, v158, v156, s63\n\t"
        "v_accvgpr_write_b32 a112, v52\n\t"
        "v_accvgpr_write_b32 a113, v53\n\t"
        "v_accvgpr_write_b32 a114, v54\n\t"
        "v_accvgpr_write_b32 a115, v55\n\t"
        "v_perm_b32 v48, v147, v145, s62\n\t"
        "v_perm_b32 v49, v151, v149, s62\n\t"
        "v_perm_b32 v50, v155, v153, s62\n\t"
        "v_perm_b32 v51, v159, v157, s62\n\t"
        "v_accvgpr_write_b32 a176, v48\n\t"
        "v_accvgpr_write_b32 a177, v49\n\t"
        "v_accvgpr_write_b32 a178, v50\n\t"
        "v_accvgpr_write_b32 a179, v51\n\t"
        "v_perm_b32 v52, v147, v145, s63\n\t"
        "v_perm_b32 v53, v151, v149, s63\n\t"
        "v_perm_b32 v54, v155, v153, s63\n\t"
        "v_perm_b32 v55, v159, v157, s63\n\t"
        "v_accvgpr_write_b32 a240, v52\n\t"
        "v_accvgpr_write_b32 a241, v53\n\t"
        "v_accvgpr_write_b32 a242, v54\n\t"
        "v_accvgpr_write_b32 a243, v55\n\t"
        "s_waitcnt vmcnt(22)\n\t"
        "s_waitcnt vmcnt(20)\n\t"
        "s_waitcnt vmcnt(18)\n\t"
        "v_perm_b32 v48, v162, v160, s62\n\t"
        "v_perm_b32 v49, v166, v164, s62\n\t"
        "v_perm_b32 v50, v170, v168, s62\n\t"
        "s_waitcnt vmcnt(16)\n\t"
        "v_perm_b32 v51, v174, v172, s62\n\t"
        "v_accvgpr_write_b32 a52, v48\n\t"
        "v_accvgpr_write_b32 a53, v49\n\t"
        "v_accvgpr_write_b32 a54, v50\n\t"
        "v_accvgpr_write_b32 a55, v51\n\t"
        "v_perm_b32 v52, v162, v160, s63\n\t"
        "v_perm_b32 v53, v166, v164, s63\n\t"
        "v_perm_b32 v54, v170, v168, s63\n\t"
        "v_perm_b32 v55, v174, v172, s63\n\t"
        "v_accvgpr_write_b32 a116, v52\n\t"
        "v_accvgpr_write_b32 a117, v53\n\t"
        "v_accvgpr_write_b32 a118, v54\n\t"
        "v_accvgpr_write_b32 a119, v55\n\t"
        "v_perm_b32 v48, v163, v161, s62\n\t"
        "v_perm_b32 v49, v167, v165, s62\n\t"
        "v_perm_b32 v50, v171, v169, s62\n\t"
        "v_perm_b32 v51, v175, v173, s62\n\t"
        "v_accvgpr_write_b32 a180, v48\n\t"
        "v_accvgpr_write_b32 a181, v49\n\t"
        "v_accvgpr_write_b32 a182, v50\n\t"
        "v_accvgpr_write_b32 a183, v51\n\t"
        "v_perm_b32 v52, v163, v161, s63\n\t"
        "v_perm_b32 v53, v167, v165, s63\n\t"
        "v_perm_b32 v54, v171, v169, s63\n\t"
        "v_perm_b32 v55, v175, v173, s63\n\t"
        "v_accvgpr_write_b32 a244, v52\n\t"
        "v_accvgpr_write_b32 a245, v53\n\t"
        "v_accvgpr_write_b32 a246, v54\n\t"
        "v_accvgpr_write_b32 a247, v55\n\t"
        "s_waitcnt vmcnt(14)\n\t"
        "s_waitcnt vmcnt(12)\n\t"
        "s_waitcnt vmcnt(10)\n\t"
        "v_perm_b32 v48, v66, v64, s62\n\t"
        "v_perm_b32 v49, v70, v68, s62\n\t"
        "v_perm_b32 v50, v74, v72, s62\n\t"
        "s_waitcnt vmcnt(8)\n\t"
        "v_perm_b32 v51, v78, v76, s62\n\t"
        "v_accvgpr_write_b32 a56, v48\n\t"
        "v_accvgpr_write_b32 a57, v49\n\t"
        "v_accvgpr_write_b32 a58, v50\n\t"
        "v_accvgpr_write_b32 a59, v51\n\t"
        "v_perm_b32 v52, v66, v64, s63\n\t"
        "v_perm_b32 v53, v70, v68, s63\n\t"
        "v_perm_b32 v54, v74, v72, s63\n\t"
        "v_perm_b32 v55, v78, v76, s63\n\t"
        "v_accvgpr_write_b32 a120, v52\n\t"
        "v_accvgpr_write_b32 a121, v53\n\t"
        "v_accvgpr_write_b32 a122, v54\n\t"
        "v_accvgpr_write_b32 a123, v55\n\t"
        "v_perm_b32 v48, v67, v65, s62\n\t"
        "v_perm_b32 v49, v71, v69, s62\n\t"
        "v_perm_b32 v50, v75, v73, s62\n\t"
        "v_perm_b32 v51, v79, v77, s62\n\t"
        "v_accvgpr_write_b32 a184, v48\n\t"
        "v_accvgpr_write_b32 a185, v49\n\t"
        "v_accvgpr_write_b32 a186, v50\n\t"
        "v_accvgpr_write_b32 a187, v51\n\t"
        "v_perm_b32 v52, v67, v65, s63\n\t"
        "v_perm_b32 v53, v71, v69, s63\n\t"
        "v_perm_b32 v54, v75, v73, s63\n\t"
        "v_perm_b32 v55, v79, v77, s63\n\t"
        "v_accvgpr_write_b32 a248, v52\n\t"
        "v_accvgpr_write_b32 a249, v53\n\t"
        "v_accvgpr_write_b32 a250, v54\n\t"
        "v_accvgpr_write_b32 a251, v55\n\t"
        "s_waitcnt vmcnt(6)\n\t"
        "s_waitcnt vmcnt(4)\n\t"
        "s_waitcnt vmcnt(2)\n\t"
        "v_perm_b32 v48, v82, v80, s62\n\t"
        "v_perm_b32 v49, v86, v84, s62\n\t"
        "v_perm_b32 v50, v90, v88, s62\n\t"
        "s_waitcnt vmcnt(0)\n\t"
        "v_perm_b32 v51, v94, v92, s62\n\t"
        "v_accvgpr_write_b32 a60, v48\n\t"
        "v_accvgpr_write_b32 a61, v49\n\t"
        "v_accvgpr_write_b32 a62, v50\n\t"
        "v_accvgpr_write_b32 a63, v51\n\t"
        "v_perm_b32 v52, v82, v80, s63\n\t"
        "v_perm_b32 v53, v86, v84, s63\n\t"
        "v_perm_b32 v54, v90, v88, s63\n\t"
        "v_perm_b32 v55, v94, v92, s63\n\t"
        "v_accvgpr_write_b32 a124, v52\n\t"
        "v_accvgpr_write_b32 a125, v53\n\t"
        "v_accvgpr_write_b32 a126, v54\n\t"
        "v_accvgpr_write_b32 a127, v55\n\t"
        "v_perm_b32 v48, v83, v81, s62\n\t"
        "v_perm_b32 v49, v87, v85, s62\n\t"
        "v_perm_b32 v50, v91, v89, s62\n\t"
        "v_perm_b32 v51, v95, v93, s62\n\t"
        "v_accvgpr_write_b32 a188, v48\n\t"
        "v_accvgpr_write_b32 a189, v49\n\t"
        "v_accvgpr_write_b32 a190, v50\n\t"
        "v_accvgpr_write_b32 a191, v51\n\t"
        "v_perm_b32 v52, v83, v81, s63\n\t"
        "v_perm_b32 v53, v87, v85, s63\n\t"
        "v_perm_b32 v54, v91, v89, s63\n\t"
        "v_perm_b32 v55, v95, v93, s63\n\t"
        "v_accvgpr_write_b32 a252, v52\n\t"
        "v_accvgpr_write_b32 a253, v53\n\t"
        "v_accvgpr_write_b32 a254, v54\n\t"
        "v_accvgpr_write_b32 a255, v55\n\t"
        "s_lshl_b32 s60, s48, 12\n\t"
        "s_mov_b32 s57, %[gd]\n\t"
        "s_mov_b32 s58, %[gc]\n\t"
        "s_mov_b32 s50, 0\n\t"
        "Lg_%=:\n\t"
        "buffer_load_dword v34, off, s[40:43], s57 sc1\n\t"
        "s_waitcnt vmcnt(0)\n\t"
        "v_readfirstlane_b32 s59, v34\n\t"
        "s_nop 0\n\t"
        "s_xor_b32 s59, s59, s58\n\t"
        "s_cmp_lt_u32 s59, 2\n\t"
        "s_cbranch_scc1 Lgd_%=\n\t"
        "s_sleep 4\n\t"
        "s_add_u32 s50, s50, 1\n\t"
        "s_cmp_lt_u32 s50, s49\n\t"
        "s_cbranch_scc1 Lg_%=\n\t"
        "Lgd_%=:\n\t"
        "s_mov_b32 s50, 0\n\t"
        "s_and_b32 s59, s59, 1\n\t"
        "s_lshl_b32 s59, s59, 13\n\t"
        "s_xor_b32 s52, s52, s59\n\t"
        "s_xor_b32 s53, s53, s59\n\t"
        "s_xor_b32 s54, s54, s59\n\t"
        "s_xor_b32 s55, s55, s59\n\t"
        "v_mov_b32 v34, 0\n\t"
        "Ltask_%=:\n\t"
        "s_mov_b32 s50, 0\n\t"
        "s_waitcnt vmcnt(1)\n\t"
        "v_cmp_gt_u32 vcc, s51, v34\n\t"
        "s_cbranch_vccz Lok_%=\n\t"
        "s_cmp_eq_u32 s51, s75\n\t"
        "s_cbranch_scc1 Lp_%=\n\t"
        "s_branch Lw1_%=\n\t"
        "Lw0_%=:\n\t"
        "s_sleep 1\n\t"
        "Lw1_%=:\n\t"
        "s_memrealtime s[62:63]\n\t"
        "s_waitcnt lgkmcnt(0)\n\t"
        "s_sub_u32 s63, s74, s62\n\t"
        "s_cmp_lt_u32 s63, 600\n\t"
        "s_cbranch_scc1 Lw0_%=\n\t"
        "Lp_%=:\n\t"
        "buffer_load_dword v34, v33, s[40:43], s52 offen sc1\n\t"
        "s_waitcnt vmcnt(0)\n\t"
        "v_cmp_gt_u32 vcc, s51, v34\n\t"
        "s_cbranch_vccz Lok_%=\n\t"
        "s_add_u32 s50, s50, 1\n\t"
        "s_cmp_eq_u32 s51, s75\n\t"
        "s_cbranch_scc1 Lns_%=\n\t"
        "s_sleep 3\n\t"
        "s_cmp_lt_u32 s50, 3\n\t"
        "s_cbranch_scc1 Lp_%=\n\t"
        "s_sleep 12\n\t"
        "Lns_%=:\n\t"
        "s_cmp_lt_u32 s50, s49\n\t"
        "s_cbranch_scc1 Lp_%=\n\t"
        "s_mov_b32 s49, 0\n\t"
        "Lok_%=:\n\t"
        "s_mov_b32 m0, s60\n\t"
        "s_add_u32 s57, s53, 4096\n\t"
        "buffer_load_dwordx4 v32, s[40:43], s53 offen offset:0 sc1 lds\n\t"
        "buffer_load_dwordx4 v32, s[40:43], s53 offen offset:1024 sc1 lds\n\t"
        "buffer_load_dwordx4 v32, s[40:43], s53 offen offset:2048 sc1 lds\n\t"
        "buffer_load_dwordx4 v32, s[40:43], s53 offen offset:3072 sc1 lds\n\t"
        "s_add_u32 m0, s60, 16384\n\t"
        "s_nop 0\n\t"
        "buffer_load_dwordx4 v32, s[40:43], s57 offen offset:0 sc1 lds\n\t"
        "buffer_load_dwordx4 v32, s[40:43], s57 offen offset:1024 sc1 lds\n\t"
        "buffer_load_dwordx4 v32, s[40:43], s57 offen offset:2048 sc1 lds\n\t"
        "buffer_load_dwordx4 v32, s[40:43], s57 offen offset:3072 sc1 lds\n\t"
        "s_add_u32 s67, s53, 524288\n\t"
        "s_xor_b32 s53, s53, s67\n\t"
        "s_lshr_b32 s53, s53, 8\n\t"
        "s_and_b32 s53, s53, 0x2000\n\t"
        "s_xor_b32 s53, s53, s67\n\t"
        "s_xor_b32 s52, s52, 4\n\t"
        "v_subrev_u32 v38, s72, v34\n\t"
        "v_subrev_u32 v39, s51, v34\n\t"
        "s_lshr_b32 s74, s73, 3\n\t"
        "v_bfe_i32 v38, v38, 0, 24\n\t"
        "v_lshrrev_b32 v39, 24, v39\n\t"
        "v_mul_u32_u24 v39, s74, v39\n\t"
        "v_sub_u32 v38, v38, v39\n\t"
        "s_nop 0\n\t"
        "v_readfirstlane_b32 s62, v38\n\t"
        "s_nop 0\n\t"
        "s_add_u32 s72, s72, s62\n\t"
        "s_sub_i32 s62, s62, s74\n\t"
        "s_min_i32 s62, s62, 16\n\t"
        "s_max_i32 s62, s62, -16\n\t"
        "s_add_u32 s73, s73, s62\n\t"
        "s_add_u32 s74, s74, s72\n\t"
        "s_add_u32 s74, s74, 35\n\t"
        "Lrdy_%=:\n\t"
        "s_waitcnt vmcnt(4)\n\t"
        "s_barrier\n\t"
        "ds_read_b128 v[64:67], v32 offset:0\n\t"
        "ds_read_b128 v[68:71], v32 offset:1024\n\t"
        "ds_read_b128 v[72:75], v32 offset:2048\n\t"
        "ds_read_b128 v[76:79], v32 offset:3072\n\t"
        "s_waitcnt lgkmcnt(3)\n\t"
        "v_mfma_f32_16x16x32_bf16 v[40:43], v[64:67], a[0:3], 0\n\t"
        "ds_read_b128 v[80:83], v32 offset:4096\n\t"
        "v_mfma_f32_16x16x32_bf16 v[44:47], v[64:67], a[64:67], 0\n\t"
        "ds_read_b128 v[84:87], v32 offset:5120\n\t"
        "v_mfma_f32_16x16x32_bf16 v[48:51], v[64:67], a[128:131], 0\n\t"
        "ds_read_b128 v[88:91], v32 offset:6144\n\t"
        "v_mfma_f32_16x16x32_bf16 v[52:55], v[64:67], a[192:195], 0\n\t"
        "ds_read_b128 v[92:95], v32 offset:7168\n\t"
        "s_waitcnt lgkmcnt(6)\n\t"
        "v_mfma_f32_16x16x32_bf16 v[40:43], v[68:71], a[4:7], v[40:43]\n\t"
        "ds_read_b128 v[96:99], v32 offset:8192\n\t"
        "v_mfma_f32_16x16x32_bf16 v[44:47], v[68:71], a[68:71], v[44:47]\n\t"
        "ds_read_b128 v[100:103], v32 offset:9216\n\t"
        "v_mfma_f32_16x16x32_bf16 v[48:51], v[68:71], a[132:135], v[48:51]\n\t"
        "ds_read_b128 v[104:107], v32 offset:10240\n\t"
        "v_mfma_f32_16x16x32_bf16 v[52:55], v[68:71], a[196:199], v[52:55]\n\t"
        "ds_read_b128 v[108:111], v32 offset:11264\n\t"
        "s_waitcnt lgkmcnt(9)\n\t"
        "v_mfma_f32_16x16x32_bf16 v[40:43], v[72:75], a[8:11], v[40:43]\n\t"
        "ds_read_b128 v[112:115], v32 offset:12288\n\t"
        "v_mfma_f32_16x16x32_bf16 v[44:47], v[72:75], a[72:75], v[44:47]\n\t"
        "ds_read_b128 v[116:119], v32 offset:13312\n\t"
        "v_mfma_f32_16x16x32_bf16 v[48:51], v[72:75], a[136:139], v[48:51]\n\t"
        "ds_read_b128 v[120:123], v32 offset:14336\n\t"
        "v_mfma_f32_16x16x32_bf16 v[52:55], v[72:75], a[200:203], v[52:55]\n\t"
        "ds_read_b128 v[124:127], v32 offset:15360\n\t"
        "s_waitcnt lgkmcnt(12)\n\t"
        "v_mfma_f32_16x16x32_bf16 v[40:43], v[76:79], a[12:15], v[40:43]\n\t"
        "s_add_u32 s58, s54, 32768\n\t"
        "v_mfma_f32_16x16x32_bf16 v[44:47], v[76:79], a[76:79], v[44:47]\n\t"
        "s_add_u32 s59, s54, 262144\n\t"
        "v_mfma_f32_16x16x32_bf16 v[48:51], v[76:79], a[140:143], v[48:51]\n\t"
        "s_mov_b32 vcc_lo, 0x00ff00ff\n\t"
        "v_mfma_f32_16x16x32_bf16 v[52:55], v[76:79], a[204:207], v[52:55]\n\t"
        "s_mov_b32 vcc_hi, 0x00ff00ff\n\t"
        "s_waitcnt lgkmcnt(11)\n\t"
        "v_mfma_f32_16x16x32_bf16 v[40:43], v[80:83], a[16:19], v[40:43]\n\t"
        "v_mfma_f32_16x16x32_bf16 v[44:47], v[80:83], a[80:83], v[44:47]\n\t"
        "v_mfma_f32_16x16x32_bf16 v[48:51], v[80:83], a[144:147], v[48:51]\n\t"
        "v_mfma_f32_16x16x32_bf16 v[52:55], v[80:83], a[208:211], v[52:55]\n\t"
        "s_waitcnt lgkmcnt(10)\n\t"
        "v_mfma_f32_16x16x32_bf16 v[40:43], v[84:87], a[20:23], v[40:43]\n\t"
        "v_mfma_f32_16x16x32_bf16 v[44:47], v[84:87], a[84:87], v[44:47]\n\t"
        "v_mfma_f32_16x16x32_bf16 v[48:51], v[84:87], a[148:151], v[48:51]\n\t"
        "v_mfma_f32_16x16x32_bf16 v[52:55], v[84:87], a[212:215], v[52:55]\n\t"
        "s_waitcnt lgkmcnt(9)\n\t"
        "v_mfma_f32_16x16x32_bf16 v[40:43], v[88:91], a[24:27], v[40:43]\n\t"
        "v_mfma_f32_16x16x32_bf16 v[44:47], v[88:91], a[88:91], v[44:47]\n\t"
        "v_mfma_f32_16x16x32_bf16 v[48:51], v[88:91], a[152:155], v[48:51]\n\t"
        "v_mfma_f32_16x16x32_bf16 v[52:55], v[88:91], a[216:219], v[52:55]\n\t"
        "s_waitcnt lgkmcnt(8)\n\t"
        "v_mfma_f32_16x16x32_bf16 v[40:43], v[92:95], a[28:31], v[40:43]\n\t"
        "v_mfma_f32_16x16x32_bf16 v[44:47], v[92:95], a[92:95], v[44:47]\n\t"
        "v_mfma_f32_16x16x32_bf16 v[48:51], v[92:95], a[156:159], v[48:51]\n\t"
        "v_mfma_f32_16x16x32_bf16 v[52:55], v[92:95], a[220:223], v[52:55]\n\t"
        "s_waitcnt lgkmcnt(7)\n\t"
        "v_mfma_f32_16x16x32_bf16 v[40:43], v[96:99], a[32:35], v[40:43]\n\t"
        "v_mfma_f32_16x16x32_bf16 v[44:47], v[96:99], a[96:99], v[44:47]\n\t"
        "v_mfma_f32_16x16x32_bf16 v[48:51], v[96:99], a[160:163], v[48:51]\n\t"
        "v_mfma_f32_16x16x32_bf16 v[52:55], v[96:99], a[224:227], v[52:55]\n\t"
        "s_waitcnt lgkmcnt(6)\n\t"
        "v_mfma_f32_16x16x32_bf16 v[40:43], v[100:103], a[36:39], v[40:43]\n\t"
        "v_mfma_f32_16x16x32_bf16 v[44:47], v[100:103], a[100:103], v[44:47]\n\t"
        "v_mfma_f32_16x16x32_bf16 v[48:51], v[100:103], a[164:167], v[48:51]\n\t"
        "v_mfma_f32_16x16x32_bf16 v[52:55], v[100:103], a[228:231], v[52:55]\n\t"
        "s_waitcnt vmcnt(0)\n\t"
        "s_barrier\n\t"
        "s_waitcnt lgkmcnt(5)\n\t"
        "v_mfma_f32_16x16x32_bf16 v[40:43], v[104:107], a[40:43], v[40:43]\n\t"
        "ds_read_b128 v[128:131], v32 offset:16384\n\t"
        "v_mfma_f32_16x16x32_bf16 v[44:47], v[104:107], a[104:107], v[44:47]\n\t"
        "ds_read_b128 v[132:135], v32 offset:17408\n\t"
        "v_mfma_f32_16x16x32_bf16 v[48:51], v[104:107], a[168:171], v[48:51]\n\t"
        "ds_read_b128 v[136:139], v32 offset:18432\n\t"
        "v_mfma_f32_16x16x32_bf16 v[52:55], v[104:107], a[232:235], v[52:55]\n\t"
        "ds_read_b128 v[140:143], v32 offset:19456\n\t"
        "s_waitcnt lgkmcnt(8)\n\t"
        "v_mfma_f32_16x16x32_bf16 v[40:43], v[108:111], a[44:47], v[40:43]\n\t"
        "ds_read_b128 v[144:147], v32 offset:20480\n\t"
        "v_mfma_f32_16x16x32_bf16 v[44:47], v[108:111], a[108:111], v[44:47]\n\t"
        "ds_read_b128 v[148:151], v32 offset:21504\n\t"
        "v_mfma_f32_16x16x32_bf16 v[48:51], v[108:111], a[172:175], v[48:51]\n\t"
        "ds_read_b128 v[152:155], v32 offset:22528\n\t"
        "v_mfma_f32_16x16x32_bf16 v[52:55], v[108:111], a[236:239], v[52:55]\n\t"
        "ds_read_b128 v[156:159], v32 offset:23552\n\t"
        "s_waitcnt lgkmcnt(11)\n\t"
        "v_mfma_f32_16x16x32_bf16 v[40:43], v[112:115], a[48:51], v[40:43]\n\t"
        "ds_read_b128 v[160:163], v32 offset:24576\n\t"
        "v_mfma_f32_16x16x32_bf16 v[44:47], v[112:115], a[112:115], v[44:47]\n\t"
        "ds_read_b128 v[164:167], v32 offset:25600\n\t"
        "v_mfma_f32_16x16x32_bf16 v[48:51], v[112:115], a[176:179], v[48:51]\n\t"
        "ds_read_b128 v[168:171], v32 offset:26624\n\t"
        "v_mfma_f32_16x16x32_bf16 v[52:55], v[112:115], a[240:243], v[52:55]\n\t"
        "ds_read_b128 v[172:175], v32 offset:27648\n\t"
        "s_waitcnt lgkmcnt(14)\n\t"
        "v_mfma_f32_16x16x32_bf16 v[40:43], v[116:119], a[52:55], v[40:43]\n\t"
        "ds_read_b128 v[176:179], v32 offset:28672\n\t"
        "v_mfma_f32_16x16x32_bf16 v[44:47], v[116:119], a[116:119], v[44:47]\n\t"
        "ds_read_b128 v[180:183], v32 offset:29696\n\t"
        "v_mfma_f32_16x16x32_bf16 v[48:51], v[116:119], a[180:183], v[48:51]\n\t"
        "ds_read_b128 v[184:187], v32 offset:30720\n\t"
        "v_mfma_f32_16x16x32_bf16 v[52:55], v[116:119], a[244:247], v[52:55]\n\t"
        "ds_read_b128 v[188:191], v32 offset:31744\n\t"
        "s_waitcnt lgkmcnt(15)\n\t"
        "v_mfma_f32_16x16x32_bf16 v[40:43], v[120:123], a[56:59], v[40:43]\n\t"
        "v_mfma_f32_16x16x32_bf16 v[44:47], v[120:123], a[120:123], v[44:47]\n\t"
        "v_mfma_f32_16x16x32_bf16 v[48:51], v[120:123], a[184:187], v[48:51]\n\t"
        "v_mfma_f32_16x16x32_bf16 v[52:55], v[120:123], a[248:251], v[52:55]\n\t"
        "v_mfma_f32_16x16x32_bf16 v[40:43], v[124:127], a[60:63], v[40:43]\n\t"
        "v_mfma_f32_16x16x32_bf16 v[44:47], v[124:127], a[124:127], v[44:47]\n\t"
        "v_mfma_f32_16x16x32_bf16 v[48:51], v[124:127], a[188:191], v[48:51]\n\t"
        "v_mfma_f32_16x16x32_bf16 v[52:55], v[124:127], a[252:255], v[52:55]\n\t"
        "v_mfma_f32_16x16x32_bf16 v[192:195], v[128:131], a[0:3], 0\n\t"
        "v_mfma_f32_16x16x32_bf16 v[196:199], v[128:131], a[64:67], 0\n\t"
        "v_mfma_f32_16x16x32_bf16 v[200:203], v[128:131], a[128:131], 0\n\t"
        "s_nop 2\n\t"
        "v_cvt_pk_bf16_f32 v56, v40, v44\n\t"
        "s_nop 0\n\t"
        "v_cvt_pk_bf16_f32 v57, v48, v52\n\t"
        "v_mfma_f32_16x16x32_bf16 v[204:207], v[128:131], a[192:195], 0\n\t"
        "s_waitcnt lgkmcnt(14)\n\t"
        "v_cvt_pk_bf16_f32 v58, v41, v45\n\t"
        "v_cvt_pk_bf16_f32 v59, v49, v53\n\t"
        "v_mfma_f32_16x16x32_bf16 v[192:195], v[132:135], a[4:7], v[192:195]\n\t"
        "v_cvt_pk_bf16_f32 v60, v42, v46\n\t"
        "v_cvt_pk_bf16_f32 v61, v50, v54\n\t"
        "v_mfma_f32_16x16x32_bf16 v[196:199], v[132:135], a[68:71], v[196:199]\n\t"
        "v_cvt_pk_bf16_f32 v62, v43, v47\n\t"
        "v_cvt_pk_bf16_f32 v63, v51, v55\n\t"
        "v_mfma_f32_16x16x32_bf16 v[200:203], v[132:135], a[132:135], v[200:203]\n\t"
        "v_cndmask_b32_dpp v64, v58, v56, vcc row_ror:8 row_mask:0xf bank_mask:0xf\n\t"
        "v_cndmask_b32_dpp v65, v59, v57, vcc row_ror:8 row_mask:0xf bank_mask:0xf\n\t"
        "v_mfma_f32_16x16x32_bf16 v[204:207], v[132:135], a[196:199], v[204:207]\n\t"
        "s_waitcnt lgkmcnt(13)\n\t"
        "v_cndmask_b32_dpp v68, v62, v60, vcc row_ror:8 row_mask:0xf bank_mask:0xf\n\t"
        "v_cndmask_b32_dpp v69, v63, v61, vcc row_ror:8 row_mask:0xf bank_mask:0xf\n\t"
        "v_mfma_f32_16x16x32_bf16 v[192:195], v[136:139], a[8:11], v[192:195]\n\t"
        "s_not_b64 vcc, vcc\n\t"
        "v_cndmask_b32_dpp v66, v56, v58, vcc row_ror:8 row_mask:0xf bank_mask:0xf\n\t"
        "v_mfma_f32_16x16x32_bf16 v[196:199], v[136:139], a[72:75], v[196:199]\n\t"
        "v_cndmask_b32_dpp v67, v57, v59, vcc row_ror:8 row_mask:0xf bank_mask:0xf\n\t"
        "buffer_store_dwordx4 v[64:67], v35, s[40:43], s54 offen offset:0 sc1\n\t"
        "v_mfma_f32_16x16x32_bf16 v[200:203], v[136:139], a[136:139], v[200:203]\n\t"
        "v_cndmask_b32_dpp v70, v60, v62, vcc row_ror:8 row_mask:0xf bank_mask:0xf\n\t"
        "v_cndmask_b32_dpp v71, v61, v63, vcc row_ror:8 row_mask:0xf bank_mask:0xf\n\t"
        "v_mfma_f32_16x16x32_bf16 v[204:207], v[136:139], a[200:203], v[204:207]\n\t"
        "buffer_store_dwordx4 v[68:71], v35, s[40:43], s58 offen offset:0 sc1\n\t"
        "s_add_u32 s58, s59, 32768\n\t"
        "s_waitcnt lgkmcnt(12)\n\t"
        "v_mfma_f32_16x16x32_bf16 v[192:195], v[140:143], a[12:15], v[192:195]\n\t"
        "s_add_u32 s64, s51, 16777216\n\t"
        "s_mov_b32 vcc_lo, 0x00ff00ff\n\t"
        "v_mfma_f32_16x16x32_bf16 v[196:199], v[140:143], a[76:79], v[196:199]\n\t"
        "s_mov_b32 vcc_hi, 0x00ff00ff\n\t"
        "v_mfma_f32_16x16x32_bf16 v[200:203], v[140:143], a[140:143], v[200:203]\n\t"
        "v_mfma_f32_16x16x32_bf16 v[204:207], v[140:143], a[204:207], v[204:207]\n\t"
        "s_waitcnt lgkmcnt(11)\n\t"
        "v_mfma_f32_16x16x32_bf16 v[192:195], v[144:147], a[16:19], v[192:195]\n\t"
        "v_mfma_f32_16x16x32_bf16 v[196:199], v[144:147], a[80:83], v[196:199]\n\t"
        "v_mfma_f32_16x16x32_bf16 v[200:203], v[144:147], a[144:147], v[200:203]\n\t"
        "v_mfma_f32_16x16x32_bf16 v[204:207], v[144:147], a[208:211], v[204:207]\n\t"
        "s_waitcnt lgkmcnt(10)\n\t"
        "v_mfma_f32_16x16x32_bf16 v[192:195], v[148:151], a[20:23], v[192:195]\n\t"
        "v_mfma_f32_16x16x32_bf16 v[196:199], v[148:151], a[84:87], v[196:199]\n\t"
        "v_mfma_f32_16x16x32_bf16 v[200:203], v[148:151], a[148:151], v[200:203]\n\t"
        "v_mfma_f32_16x16x32_bf16 v[204:207], v[148:151], a[212:215], v[204:207]\n\t"
        "s_waitcnt lgkmcnt(9)\n\t"
        "v_mfma_f32_16x16x32_bf16 v[192:195], v[152:155], a[24:27], v[192:195]\n\t"
        "v_mfma_f32_16x16x32_bf16 v[196:199], v[152:155], a[88:91], v[196:199]\n\t"
        "v_mfma_f32_16x16x32_bf16 v[200:203], v[152:155], a[152:155], v[200:203]\n\t"
        "v_mfma_f32_16x16x32_bf16 v[204:207], v[152:155], a[216:219], v[204:207]\n\t"
        "s_waitcnt lgkmcnt(8)\n\t"
        "v_mfma_f32_16x16x32_bf16 v[192:195], v[156:159], a[28:31], v[192:195]\n\t"
        "v_mfma_f32_16x16x32_bf16 v[196:199], v[156:159], a[92:95], v[196:199]\n\t"
        "v_mfma_f32_16x16x32_bf16 v[200:203], v[156:159], a[156:159], v[200:203]\n\t"
        "s_memrealtime s[62:63]\n\t"
        "v_mfma_f32_16x16x32_bf16 v[204:207], v[156:159], a[220:223], v[204:207]\n\t"
        "s_waitcnt lgkmcnt(7)\n\t"
        "v_mfma_f32_16x16x32_bf16 v[192:195], v[160:163], a[32:35], v[192:195]\n\t"
        "v_mfma_f32_16x16x32_bf16 v[196:199], v[160:163], a[96:99], v[196:199]\n\t"
        "v_mfma_f32_16x16x32_bf16 v[200:203], v[160:163], a[160:163], v[200:203]\n\t"
        "v_mfma_f32_16x16x32_bf16 v[204:207], v[160:163], a[224:227], v[204:207]\n\t"
        "s_waitcnt lgkmcnt(0)\n\t"
        "s_sub_u32 s63, s74, s62\n\t"
        "s_cmp_lt_u32 s63, 600\n\t"
        "s_cselect_b32 s62, 0x7ff00000, s52\n\t"
        "buffer_load_dword v34, v33, s[40:43], s62 offen sc1\n\t"
        "v_mfma_f32_16x16x32_bf16 v[192:195], v[164:167], a[36:39], v[192:195]\n\t"
        "v_mfma_f32_16x16x32_bf16 v[196:199], v[164:167], a[100:103], v[196:199]\n\t"
        "v_mfma_f32_16x16x32_bf16 v[200:203], v[164:167], a[164:167], v[200:203]\n\t"
        "v_mfma_f32_16x16x32_bf16 v[204:207], v[164:167], a[228:231], v[204:207]\n\t"
        "v_mfma_f32_16x16x32_bf16 v[192:195], v[168:171], a[40:43], v[192:195]\n\t"
        "v_mfma_f32_16x16x32_bf16 v[196:199], v[168:171], a[104:107], v[196:199]\n\t"
        "v_mfma_f32_16x16x32_bf16 v[200:203], v[168:171], a[168:171], v[200:203]\n\t"
        "v_mfma_f32_16x16x32_bf16 v[204:207], v[168:171], a[232:235], v[204:207]\n\t"
        "v_mfma_f32_16x16x32_bf16 v[192:195], v[172:175], a[44:47], v[192:195]\n\t"
        "v_mfma_f32_16x16x32_bf16 v[196:199], v[172:175], a[108:111], v[196:199]\n\t"
        "v_mfma_f32_16x16x32_bf16 v[200:203], v[172:175], a[172:175], v[200:203]\n\t"
        "v_mfma_f32_16x16x32_bf16 v[204:207], v[172:175], a[236:239], v[204:207]\n\t"
        "v_mfma_f32_16x16x32_bf16 v[192:195], v[176:179], a[48:51], v[192:195]\n\t"
        "v_mfma_f32_16x16x32_bf16 v[196:199], v[176:179], a[112:115], v[196:199]\n\t"
        "v_mfma_f32_16x16x32_bf16 v[200:203], v[176:179], a[176:179], v[200:203]\n\t"
        "v_mfma_f32_16x16x32_bf16 v[204:207], v[176:179], a[240:243], v[204:207]\n\t"
        "s_waitcnt vmcnt(1)\n\t"
        "s_waitcnt lgkmcnt(0)\n\t"
        "v_mfma_f32_16x16x32_bf16 v[192:195], v[180:183], a[52:55], v[192:195]\n\t"
        "s_memrealtime s[88:89]\n\t"
        "v_mfma_f32_16x16x32_bf16 v[196:199], v[180:183], a[116:119], v[196:199]\n\t"
        "s_barrier\n\t"
        "v_mfma_f32_16x16x32_bf16 v[200:203], v[180:183], a[180:183], v[200:203]\n\t"
        "s_waitcnt lgkmcnt(0)\n\t"
        "s_and_b32 s88, s88, 0xffffff\n\t"
        "v_mfma_f32_16x16x32_bf16 v[204:207], v[180:183], a[244:247], v[204:207]\n\t"
        "s_or_b32 s88, s88, s51\n\t"
        "v_mfma_f32_16x16x32_bf16 v[192:195], v[184:187], a[56:59], v[192:195]\n\t"
        "v_mov_b32 v37, s88\n\t"
        "v_mfma_f32_16x16x32_bf16 v[196:199], v[184:187], a[120:123], v[196:199]\n\t"
        "s_mov_b64 exec, 0xf\n\t"
        "buffer_store_dword v37, v36, s[40:43], s55 offen offset:0 sc1\n\t"
        "s_mov_b64 exec, -1\n\t"
        "s_nop 0\n\t"
        "v_mfma_f32_16x16x32_bf16 v[200:203], v[184:187], a[184:187], v[200:203]\n\t"
        "v_mfma_f32_16x16x32_bf16 v[204:207], v[184:187], a[248:251], v[204:207]\n\t"
        "v_mfma_f32_16x16x32_bf16 v[192:195], v[188:191], a[60:63], v[192:195]\n\t"
        "v_mfma_f32_16x16x32_bf16 v[196:199], v[188:191], a[124:127], v[196:199]\n\t"
        "v_mfma_f32_16x16x32_bf16 v[200:203], v[188:191], a[188:191], v[200:203]\n\t"
        "v_mfma_f32_16x16x32_bf16 v[204:207], v[188:191], a[252:255], v[204:207]\n\t"
        "s_nop 5\n\t"
        "v_cvt_pk_bf16_f32 v56, v192, v196\n\t"
        "s_nop 0\n\t"
        "v_cvt_pk_bf16_f32 v57, v200, v204\n\t"
        "v_cvt_pk_bf16_f32 v58, v193, v197\n\t"
        "v_cvt_pk_bf16_f32 v59, v201, v205\n\t"
        "v_cvt_pk_bf16_f32 v60, v194, v198\n\t"
        "v_cvt_pk_bf16_f32 v61, v202, v206\n\t"
        "v_cvt_pk_bf16_f32 v62, v195, v199\n\t"
        "v_cvt_pk_bf16_f32 v63, v203, v207\n\t"
        "v_cndmask_b32_dpp v128, v58, v56, vcc row_ror:8 row_mask:0xf bank_mask:0xf\n\t"
        "v_cndmask_b32_dpp v129, v59, v57, vcc row_ror:8 row_mask:0xf bank_mask:0xf\n\t"
        "v_cndmask_b32_dpp v132, v62, v60, vcc row_ror:8 row_mask:0xf bank_mask:0xf\n\t"
        "v_cndmask_b32_dpp v133, v63, v61, vcc row_ror:8 row_mask:0xf bank_mask:0xf\n\t"
        "s_not_b64 vcc, vcc\n\t"
        "v_cndmask_b32_dpp v130, v56, v58, vcc row_ror:8 row_mask:0xf bank_mask:0xf\n\t"
        "v_cndmask_b32_dpp v131, v57, v59, vcc row_ror:8 row_mask:0xf bank_mask:0xf\n\t"
        "s_memrealtime s[62:63]\n\t"
        "buffer_store_dwordx4 v[128:131], v35, s[40:43], s59 offen offset:0 sc1\n\t"
        "v_cndmask_b32_dpp v134, v60, v62, vcc row_ror:8 row_mask:0xf bank_mask:0xf\n\t"
        "v_cndmask_b32_dpp v135, v61, v63, vcc row_ror:8 row_mask:0xf bank_mask:0xf\n\t"
        "buffer_store_dwordx4 v[132:135], v35, s[40:43], s58 offen offset:0 sc1\n\t"
        "s_waitcnt vmcnt(3)\n\t"
        "v_cmp_gt_u32 vcc, s64, v34\n\t"
        "s_cbranch_vccnz Lnpre_%=\n\t"
        "s_mov_b32 m0, s60\n\t"
        "s_add_u32 s57, s53, 4096\n\t"
        "buffer_load_dwordx4 v32, s[40:43], s53 offen offset:0 sc1 lds\n\t"
        "buffer_load_dwordx4 v32, s[40:43], s53 offen offset:1024 sc1 lds\n\t"
        "buffer_load_dwordx4 v32, s[40:43], s53 offen offset:2048 sc1 lds\n\t"
        "buffer_load_dwordx4 v32, s[40:43], s53 offen offset:3072 sc1 lds\n\t"
        "s_add_u32 m0, s60, 16384\n\t"
        "s_nop 0\n\t"
        "buffer_load_dwordx4 v32, s[40:43], s57 offen offset:0 sc1 lds\n\t"
        "buffer_load_dwordx4 v32, s[40:43], s57 offen offset:1024 sc1 lds\n\t"
        "buffer_load_dwordx4 v32, s[40:43], s57 offen offset:2048 sc1 lds\n\t"
        "buffer_load_dwordx4 v32, s[40:43], s57 offen offset:3072 sc1 lds\n\t"
        "s_add_u32 s67, s53, 524288\n\t"
        "s_xor_b32 s53, s53, s67\n\t"
        "s_lshr_b32 s53, s53, 8\n\t"
        "s_and_b32 s53, s53, 0x2000\n\t"
        "s_xor_b32 s53, s53, s67\n\t"
        "s_xor_b32 s52, s52, 4\n\t"
        "v_subrev_u32 v38, s72, v34\n\t"
        "v_subrev_u32 v39, s64, v34\n\t"
        "s_lshr_b32 s74, s73, 3\n\t"
        "v_bfe_i32 v38, v38, 0, 24\n\t"
        "v_lshrrev_b32 v39, 24, v39\n\t"
        "v_mul_u32_u24 v39, s74, v39\n\t"
        "v_sub_u32 v38, v38, v39\n\t"
        "s_waitcnt lgkmcnt(0)\n\t"
        "v_readfirstlane_b32 s62, v38\n\t"
        "s_nop 0\n\t"
        "s_add_u32 s72, s72, s62\n\t"
        "s_sub_i32 s62, s62, s74\n\t"
        "s_min_i32 s62, s62, 16\n\t"
        "s_max_i32 s62, s62, -16\n\t"
        "s_add_u32 s73, s73, s62\n\t"
        "s_add_u32 s74, s74, s72\n\t"
        "s_add_u32 s74, s74, 35\n\t"
        "s_add_u32 s67, s54, 524288\n\t"
        "s_xor_b32 s54, s54, s67\n\t"
        "s_lshr_b32 s54, s54, 8\n\t"
        "s_and_b32 s54, s54, 0x2000\n\t"
        "s_xor_b32 s54, s54, s67\n\t"
        "s_waitcnt vmcnt(8)\n\t"
        "s_memrealtime s[88:89]\n\t"
        "s_barrier\n\t"
        "s_waitcnt lgkmcnt(0)\n\t"
        "s_and_b32 s88, s88, 0xffffff\n\t"
        "s_or_b32 s88, s88, s51\n\t"
        "v_mov_b32 v37, s88\n\t"
        "s_mov_b64 exec, 0xf\n\t"
        "buffer_store_dword v37, v36, s[40:43], s55 offen offset:2048 sc1\n\t"
        "s_mov_b64 exec, -1\n\t"
        "s_mov_b32 s51, s64\n\t"
        "s_branch Lrdy_%=\n\t"
        "Lnpre_%=:\n\t"
        "s_waitcnt lgkmcnt(0)\n\t"
        "s_sub_u32 s63, s74, s62\n\t"
        "s_cmp_lt_u32 s63, 600\n\t"
        "s_cselect_b32 s62, 0x7ff00000, s52\n\t"
        "buffer_load_dword v34, v33, s[40:43], s62 offen sc1\n\t"
        "s_add_u32 s67, s54, 524288\n\t"
        "s_xor_b32 s54, s54, s67\n\t"
        "s_lshr_b32 s54, s54, 8\n\t"
        "s_and_b32 s54, s54, 0x2000\n\t"
        "s_xor_b32 s54, s54, s67\n\t"
        "s_waitcnt vmcnt(1)\n\t"
        "s_memrealtime s[88:89]\n\t"
        "s_barrier\n\t"
        "s_waitcnt lgkmcnt(0)\n\t"
        "s_and_b32 s88, s88, 0xffffff\n\t"
        "s_or_b32 s88, s88, s51\n\t"
        "v_mov_b32 v37, s88\n\t"
        "s_mov_b64 exec, 0xf\n\t"
        "buffer_store_dword v37, v36, s[40:43], s55 offen offset:2048 sc1\n\t"
        "s_mov_b64 exec, -1\n\t"
        "s_add_u32 s51, s51, 16777216\n\t"
        "s_cmp_lt_u32 s51, 1090519040\n\t"
        "s_cbranch_scc1 Ltask_%=\n\t"
        "s_endpgm\n\t"
        :
        : [pw] "s"(ws), [w] "s"(w), [fs] "s"(l == 0 ? FS0 << 24 : FS0 << 30), [fi] "s"(O_FO + l * 8192 + (kq >> 2) * 16384 + ((kq & 3) * 4 + w) * 512 + nq * 128), [in] "s"(O_OX + l * 8192 + (kq * 4 + w) * 16384),
          [out] "s"(O_WOP + l * 8192 + kq * 1024 + nq * 512 + w * 128), [fo] "s"(w == 0 ? O_FWO + l * 8192 + (p >> 2) * 128 + (p & 3) * 4 : OOB),
          [db] "s"(DBGA(0, l, kq == 0 && nq == 0 && w == 0, p, w == 0)), [gc] "s"(gc), [gd] "s"(O_HR + id * 4), [pa] "s"(Wo), [wb] "s"(kq * 524288 + (w >> 1) * 512 + (w & 1) * 128 + nq * 8), [ga] "v"(g * 4096 + (cc >> 3) * 256 + (cc & 7) * 16), [fa] "v"(0), [sv] "v"((4 * g + (cc >> 3)) * 16384 + (cc & 7) * 16)
        : "memory", "vcc", "scc", "v32", "v33", "v34", "v35", "v36", "v37", "v38", "v39", "v40", "v41", "v42", "v43", "v44", "v45", "v46", "v47", "v48", "v49", "v50", "v51", "v52", "v53", "v54", "v55", "v56", "v57", "v58", "v59", "v60", "v61", "v62", "v63", "v64", "v65", "v66", "v67", "v68", "v69", "v70", "v71", "v72", "v73", "v74", "v75", "v76", "v77", "v78", "v79", "v80", "v81", "v82", "v83", "v84", "v85", "v86", "v87", "v88", "v89", "v90", "v91", "v92", "v93", "v94", "v95", "v96", "v97", "v98", "v99", "v100", "v101", "v102", "v103", "v104", "v105", "v106", "v107", "v108", "v109", "v110", "v111", "v112", "v113", "v114", "v115", "v116", "v117", "v118", "v119", "v120", "v121", "v122", "v123", "v124", "v125", "v126", "v127", "v128", "v129", "v130", "v131", "v132", "v133", "v134", "v135", "v136", "v137", "v138", "v139", "v140", "v141", "v142", "v143", "v144", "v145", "v146", "v147", "v148", "v149", "v150", "v151", "v152", "v153", "v154", "v155", "v156", "v157", "v158", "v159", "v160", "v161", "v162", "v163", "v164", "v165", "v166", "v167", "v168", "v169", "v170", "v171", "v172", "v173", "v174", "v175", "v176", "v177", "v178", "v179", "v180", "v181", "v182", "v183", "v184", "v185", "v186", "v187", "v188", "v189", "v190", "v191", "v192", "v193", "v194", "v195", "v196", "v197", "v198", "v199", "v200", "v201", "v202", "v203", "v204", "v205", "v206", "v207", "v208", "v209", "v210", "v211", "v212", "v213", "v214", "v215", "v216", "v217", "v218", "v219", "v220", "v221", "v222", "v223", "v224", "v225", "v226", "v227", "v228", "v229", "v230", "v231", "v232", "v233", "v234", "v235", "v236", "v237", "v238", "v239", "v240", "v241", "v242", "v243", "v244", "v245", "v246", "v247", "v248", "v249", "v250", "v251", "v252", "v253", "v254", "v255", "a0", "a1", "a2", "a3", "a4", "a5", "a6", "a7", "a8", "a9", "a10", "a11", "a12", "a13", "a14", "a15", "a16", "a17", "a18", "a19", "a20", "a21", "a22", "a23", "a24", "a25", "a26", "a27", "a28", "a29", "a30", "a31", "a32", "a33", "a34", "a35", "a36", "a37", "a38", "a39", "a40", "a41", "a42", "a43", "a44", "a45", "a46", "a47", "a48", "a49", "a50", "a51", "a52", "a53", "a54", "a55", "a56", "a57", "a58", "a59", "a60", "a61", "a62", "a63", "a64", "a65", "a66", "a67", "a68", "a69", "a70", "a71", "a72", "a73", "a74", "a75", "a76", "a77", "a78", "a79", "a80", "a81", "a82", "a83", "a84", "a85", "a86", "a87", "a88", "a89", "a90", "a91", "a92", "a93", "a94", "a95", "a96", "a97", "a98", "a99", "a100", "a101", "a102", "a103", "a104", "a105", "a106", "a107", "a108", "a109", "a110", "a111", "a112", "a113", "a114", "a115", "a116", "a117", "a118", "a119", "a120", "a121", "a122", "a123", "a124", "a125", "a126", "a127", "a128", "a129", "a130", "a131", "a132", "a133", "a134", "a135", "a136", "a137", "a138", "a139", "a140", "a141", "a142", "a143", "a144", "a145", "a146", "a147", "a148", "a149", "a150", "a151", "a152", "a153", "a154", "a155", "a156", "a157", "a158", "a159", "a160", "a161", "a162", "a163", "a164", "a165", "a166", "a167", "a168", "a169", "a170", "a171", "a172", "a173", "a174", "a175", "a176", "a177", "a178", "a179", "a180", "a181", "a182", "a183", "a184", "a185", "a186", "a187", "a188", "a189", "a190", "a191", "a192", "a193", "a194", "a195", "a196", "a197", "a198", "a199", "a200", "a201", "a202", "a203", "a204", "a205", "a206", "a207", "a208", "a209", "a210", "a211", "a212", "a213", "a214", "a215", "a216", "a217", "a218", "a219", "a220", "a221", "a222", "a223", "a224", "a225", "a226", "a227", "a228", "a229", "a230", "a231", "a232", "a233", "a234", "a235", "a236", "a237", "a238", "a239", "a240", "a241", "a242", "a243", "a244", "a245", "a246", "a247", "a248", "a249", "a250", "a251", "a252", "a253", "a254", "a255", "s40", "s41", "s42", "s43", "s44", "s45", "s46", "s47", "s48", "s49", "s50", "s51", "s52", "s53", "s54", "s55", "s56", "s57", "s58", "s59", "s60", "s61", "s62", "s63", "s64", "s65", "s66", "s67", "s68", "s69", "s70", "s71", "s72", "s73", "s74", "s75", "s76", "s77", "s78", "s79", "s80", "s81", "s82", "s83", "s84", "s85", "s86", "s87", "s88", "s89");
    __builtin_unreachable();
}

DI void mr_role(unsigned char* ws, int l, int u, int lane, int id, unsigned gc) {
    const unsigned one = (lane & 1) ? 0x3F800000u : 0x00003F80u;
    const unsigned db = 0;
    asm volatile(
        "s_mov_b64 s[40:41], %[pw]\n\t"
        "s_mov_b32 s42, 184549376\n\t"
        "s_mov_b32 s43, 0x20000\n\t"
        "s_mov_b32 s48, %[w]\n\t"
        "s_mov_b32 s52, %[fi]\n\t"
        "s_mov_b32 s53, %[in]\n\t"
        "s_mov_b32 s54, %[out]\n\t"
        "s_mov_b32 s55, %[fo]\n\t"
        "s_mov_b32 s56, %[db]\n\t"
        "s_mov_b32 s75, %[fs]\n\t"
        "s_memrealtime s[72:73]\n\t"
        "s_mov_b32 s49, 131072\n\t"
        "s_mov_b32 s51, 16777216\n\t"
        "s_waitcnt lgkmcnt(0)\n\t"
        "s_mov_b32 s74, s72\n\t"
        "s_mov_b32 s73, 1480\n\t"
        "v_mbcnt_lo_u32_b32 v38, -1, 0\n\t"
        "v_mbcnt_hi_u32_b32 v38, -1, v38\n\t"
        "v_lshlrev_b32 v32, 4, v38\n\t"
        "v_mul_u32_u24 v36, 0, v38\n\t"
        "v_mov_b32 v33, %[fa]\n\t"
        "v_mov_b32 v35, %[sv]\n\t"
        "v_mov_b32 v56, %[s0]\n\t"
        "v_mov_b32 v57, %[s1]\n\t"
        "v_mov_b32 v58, %[s0]\n\t"
        "v_mov_b32 v59, %[s1]\n\t"
        "v_mov_b32 v64, %[idx]\n\t"
        "v_lshrrev_b32 v36, 2, v32\n\t"
        "v_and_b32 v36, 0x7c, v36\n\t"
        "v_mul_u32_u24 v36, 4097, v36\n\t"
        "v_and_b32 v36, 0xf007c, v36\n\t"
        "v_mov_b64 v[68:69], -1\n\t"
        "v_mov_b64 v[70:71], -1\n\t"
        "s_mov_b32 s73, 36\n\t"
        "s_mov_b32 s51, 0\n\t"
        "s_mov_b32 s61, 900\n\t"
        "s_mov_b32 s44, 269\n\t"
        "s_getpc_b64 s[46:47]\n\t"
        "Lgoa_%=:\n\t"
        "s_add_u32 s46, s46, Lgo_%=-Lgoa_%=\n\t"
        "s_addc_u32 s47, s47, 0\n\t"
        "s_memrealtime s[76:77]\n\t"
        "s_memtime s[86:87]\n\t"
        "s_waitcnt lgkmcnt(0)\n\t"
        "s_mul_i32 s64, s76, 5\n\t"
        "s_mov_b32 s66, s86\n\t"
        "s_mov_b32 s74, s64\n\t"
        "s_mov_b32 s45, s64\n\t"
        "s_mov_b32 s57, %[gd]\n\t"
        "s_mov_b32 s58, %[gc]\n\t"
        "s_mov_b32 s50, 0\n\t"
        "Lg_%=:\n\t"
        "buffer_load_dword v34, off, s[40:43], s57 sc1\n\t"
        "s_waitcnt vmcnt(0)\n\t"
        "v_readfirstlane_b32 s59, v34\n\t"
        "s_nop 0\n\t"
        "s_xor_b32 s59, s59, s58\n\t"
        "s_cmp_lt_u32 s59, 2\n\t"
        "s_cbranch_scc1 Lgd_%=\n\t"
        "s_sleep 4\n\t"
        "s_add_u32 s50, s50, 1\n\t"
        "s_cmp_lt_u32 s50, s49\n\t"
        "s_cbranch_scc1 Lg_%=\n\t"
        "Lgd_%=:\n\t"
        "s_mov_b32 s50, 0\n\t"
        "s_and_b32 s59, s59, 1\n\t"
        "s_lshl_b32 s59, s59, 13\n\t"
        "s_xor_b32 s52, s52, s59\n\t"
        "s_xor_b32 s53, s53, s59\n\t"
        "s_xor_b32 s54, s54, s59\n\t"
        ".p2align 6\n\t"
        "Ltask_%=:\n\t"
        "v_mov_b64 v[60:61], 0\n\t"
        "v_mov_b64 v[62:63], 0\n\t"
        "s_add_u32 s57, s53, 4096\n\t"
        "s_mov_b32 s50, 0\n\t"
        "s_branch Lw1_%=\n\t"
        "Lw0_%=:\n\t"
        "s_sleep 1\n\t"
        "Lw1_%=:\n\t"
        "s_memrealtime s[62:63]\n\t"
        "s_waitcnt lgkmcnt(0)\n\t"
        "s_mul_i32 s62, s62, 5\n\t"
        "s_sub_u32 s63, s74, s62\n\t"
        "s_cmp_lt_u32 s63, 3000\n\t"
        "s_cbranch_scc1 Lw0_%=\n\t"
        "Lp_%=:\n\t"
        "buffer_load_dword v34, v36, s[40:43], s52 offen sc1\n\t"
        "s_sub_u32 s59, s73, s48\n\t"
        "s_waitcnt vmcnt(0)\n\t"
        "s_waitcnt lgkmcnt(0)\n\t"
        "s_memrealtime s[76:77]\n\t"
        "v_max_u32_dpp v34, v34, v34 row_ror:1 row_mask:0xf bank_mask:0xf\n\t"
        "s_nop 1\n\t"
        "s_memrealtime s[78:79]\n\t"
        "v_max_u32_dpp v34, v34, v34 row_ror:2 row_mask:0xf bank_mask:0xf\n\t"
        "s_nop 1\n\t"
        "s_memrealtime s[80:81]\n\t"
        "v_max_u32_dpp v34, v34, v34 row_ror:4 row_mask:0xf bank_mask:0xf\n\t"
        "s_nop 1\n\t"
        "s_memrealtime s[82:83]\n\t"
        "v_max_u32_dpp v34, v34, v34 row_ror:8 row_mask:0xf bank_mask:0xf\n\t"
        "s_nop 1\n\t"
        "s_memrealtime s[84:85]\n\t"
        "v_readlane_b32 s72, v34, 0\n\t"
        "v_readlane_b32 s58, v34, 16\n\t"
        "s_nop 0\n\t"
        "s_max_u32 s72, s72, s58\n\t"
        "s_add_u32 s58, s72, s59\n\t"
        "s_sub_u32 s59, s58, s45\n\t"
        "s_cmp_eq_u32 s72, -1\n\t"
        "s_cbranch_scc1 Lno_%=\n\t"
        "Lok_%=:\n\t"
        "s_waitcnt lgkmcnt(0)\n\t"
        "s_add_u32 s76, s76, s78\n\t"
        "s_add_u32 s80, s80, s82\n\t"
        "s_add_u32 s76, s76, s84\n\t"
        "s_add_u32 s76, s76, s80\n\t"
        "s_sub_u32 s78, s58, s76\n\t"
        "s_cmp_lt_u32 s78, 6000\n\t"
        "s_cselect_b32 s78, s78, 0\n\t"
        "s_mul_i32 s80, s78, s44\n\t"
        "s_lshr_b32 s80, s80, 9\n\t"
        "s_min_u32 s80, s80, 64\n\t"
        "s_lshl_b32 s80, s80, 2\n\t"
        "s_sub_u32 s80, s46, s80\n\t"
        "s_subb_u32 s81, s47, 0\n\t"
        "s_add_u32 s45, s76, s78\n\t"
        "s_cmp_gt_u32 s78, 96\n\t"
        "s_cbranch_scc1 Lgow_%=\n\t"
        "s_setpc_b64 s[80:81]\n\t"
        ".fill 64, 4, 0xbf800001\n\t"
        "Lgo_%=:\n\t"
        "buffer_load_dwordx4 v[40:43], v33, s[40:43], s53 offen offset:0 sc1\n\t"
        "buffer_load_dwordx4 v[44:47], v33, s[40:43], s53 offen offset:2048 sc1\n\t"
        "buffer_load_dwordx4 v[48:51], v33, s[40:43], s57 offen offset:0 sc1\n\t"
        "buffer_load_dwordx4 v[52:55], v33, s[40:43], s57 offen offset:2048 sc1\n\t"
        "s_memtime s[86:87]\n\t"
        "s_add_u32 s61, s61, 30\n\t"
        "s_min_u32 s61, s61, s59\n\t"
        "s_add_u32 s58, s54, 65536\n\t"
        "s_waitcnt vmcnt(2)\n\t"
        "v_smfmac_f32_16x16x64_bf16 v[60:63], v[56:59], v[40:47], v64\n\t"
        "v_max_u32 v65, v40, v41\n\t"
        "s_waitcnt vmcnt(0)\n\t"
        "v_smfmac_f32_16x16x64_bf16 v[60:63], v[56:59], v[48:55], v64\n\t"
        "v_max3_u32 v65, v65, v42, v43\n\t"
        "v_max3_u32 v65, v65, v44, v45\n\t"
        "v_max3_u32 v65, v65, v46, v47\n\t"
        "v_max3_u32 v65, v65, v48, v49\n\t"
        "v_max3_u32 v65, v65, v50, v51\n\t"
        "v_max3_u32 v65, v65, v52, v53\n\t"
        "v_max3_u32 v65, v65, v54, v55\n\t"
        "v_cmp_eq_u32 vcc, -1, v65\n\t"
        "Lmrok_%=:\n\t"
        "v_add_f32_dpp v60, v60, v60 row_ror:8 row_mask:0xf bank_mask:0xf\n\t"
        "v_add_f32_dpp v61, v61, v61 row_ror:8 row_mask:0xf bank_mask:0xf\n\t"
        "v_add_f32_dpp v62, v62, v62 row_ror:8 row_mask:0xf bank_mask:0xf\n\t"
        "v_add_f32_dpp v63, v63, v63 row_ror:8 row_mask:0xf bank_mask:0xf\n\t"
        "v_cvt_pk_bf16_f32 v40, v60, v61\n\t"
        "v_cvt_pk_bf16_f32 v41, v62, v63\n\t"
        "s_cbranch_vccnz Lmrf_%=\n\t"
        "buffer_store_dwordx2 v[40:41], v35, s[40:43], s54 offen sc1\n\t"
        "buffer_store_dwordx2 v[40:41], v35, s[40:43], s58 offen sc1\n\t"
        "s_sleep 8\n\t"
        "buffer_store_dwordx4 v[68:71], v33, s[40:43], s53 offen offset:0 sc1\n\t"
        "buffer_store_dwordx4 v[68:71], v33, s[40:43], s53 offen offset:2048 sc1\n\t"
        "buffer_store_dwordx4 v[68:71], v33, s[40:43], s57 offen offset:0 sc1\n\t"
        "buffer_store_dwordx4 v[68:71], v33, s[40:43], s57 offen offset:2048 sc1\n\t"
        "s_add_u32 s67, s54, 131072\n\t"
        "s_xor_b32 s54, s54, s67\n\t"
        "s_lshr_b32 s54, s54, 8\n\t"
        "s_and_b32 s54, s54, 0x2000\n\t"
        "s_xor_b32 s54, s54, s67\n\t"
        "s_add_u32 s53, s53, 524288\n\t"
        "s_bitset0_b32 s53, 21\n\t"
        "s_add_u32 s52, s52, 128\n\t"
        "s_sub_u32 s57, s45, s64\n\t"
        "s_waitcnt lgkmcnt(0)\n\t"
        "s_sub_u32 s58, s86, s66\n\t"
        "s_mov_b32 s64, s45\n\t"
        "s_mov_b32 s66, s86\n\t"
        "v_cvt_f32_u32 v72, s57\n\t"
        "v_cvt_f32_u32 v73, s58\n\t"
        "v_rcp_f32 v74, v72\n\t"
        "v_mul_f32 v73, 0x42800000, v73\n\t"
        "v_mul_f32 v74, v74, v73\n\t"
        "v_cvt_u32_f32 v74, v74\n\t"
        "s_nop 0\n\t"
        "v_readfirstlane_b32 s57, v74\n\t"
        "s_nop 0\n\t"
        "s_sub_u32 s59, s57, 224\n\t"
        "s_cmp_lt_u32 s59, 112\n\t"
        "s_cselect_b32 s44, s57, s44\n\t"
        "s_add_u32 s74, s45, s61\n\t"
        "s_sub_u32 s74, s74, 230\n\t"
        "s_add_u32 s51, s51, 1\n\t"
        "s_cmp_lt_u32 s51, 63\n\t"
        "s_cbranch_scc1 Ltask_%=\n\t"
        "s_endpgm\n\t"
        "Lgow_%=:\n\t"
        "s_memrealtime s[76:77]\n\t"
        "s_waitcnt lgkmcnt(0)\n\t"
        "s_mul_i32 s76, s76, 5\n\t"
        "s_sub_u32 s76, s58, s76\n\t"
        "s_sub_u32 s76, s76, 61\n\t"
        "s_cmp_lt_u32 s76, 6000\n\t"
        "s_cbranch_scc1 Lgow_%=\n\t"
        "s_memrealtime s[76:77]\n\t"
        "s_nop 2\n\t"
        "s_memrealtime s[78:79]\n\t"
        "s_nop 2\n\t"
        "s_memrealtime s[80:81]\n\t"
        "s_nop 2\n\t"
        "s_memrealtime s[82:83]\n\t"
        "s_nop 2\n\t"
        "s_memrealtime s[84:85]\n\t"
        "s_waitcnt lgkmcnt(0)\n\t"
        "s_add_u32 s76, s76, s78\n\t"
        "s_add_u32 s80, s80, s82\n\t"
        "s_add_u32 s76, s76, s84\n\t"
        "s_add_u32 s76, s76, s80\n\t"
        "s_sub_u32 s76, s76, 3\n\t"
        "s_sub_u32 s78, s58, s76\n\t"
        "s_cmp_lt_u32 s78, 6000\n\t"
        "s_cselect_b32 s78, s78, 0\n\t"
        "s_mul_i32 s80, s78, s44\n\t"
        "s_lshr_b32 s80, s80, 9\n\t"
        "s_min_u32 s80, s80, 64\n\t"
        "s_lshl_b32 s80, s80, 2\n\t"
        "s_sub_u32 s80, s46, s80\n\t"
        "s_subb_u32 s81, s47, 0\n\t"
        "s_add_u32 s45, s76, s78\n\t"
        "s_setpc_b64 s[80:81]\n\t"
        "Lno_%=:\n\t"
        "s_add_u32 s50, s50, 1\n\t"
        "s_cmp_lt_u32 s50, s49\n\t"
        "s_cbranch_scc1 Lp_%=\n\t"
        "s_mov_b32 s49, 0\n\t"
        "s_branch Lok_%=\n\t"
        "Lmrf_%=:\n\t"
        "s_add_u32 s73, s73, 0\n\t"
        "s_min_i32 s73, s73, 56\n\t"
        "s_mov_b32 s50, 0\n\t"
        "Lmrl_%=:\n\t"
        "s_nop 7\n\t"
        "buffer_load_dwordx4 v[40:43], v33, s[40:43], s53 offen offset:0 sc1\n\t"
        "buffer_load_dwordx4 v[44:47], v33, s[40:43], s53 offen offset:2048 sc1\n\t"
        "buffer_load_dwordx4 v[48:51], v33, s[40:43], s57 offen offset:0 sc1\n\t"
        "buffer_load_dwordx4 v[52:55], v33, s[40:43], s57 offen offset:2048 sc1\n\t"
        "s_waitcnt vmcnt(0)\n\t"
        "v_max3_u32 v65, v40, v41, v42\n\t"
        "v_max3_u32 v65, v65, v43, v44\n\t"
        "v_max3_u32 v65, v65, v45, v46\n\t"
        "v_max3_u32 v65, v65, v47, v48\n\t"
        "v_max3_u32 v65, v65, v49, v50\n\t"
        "v_max3_u32 v65, v65, v51, v52\n\t"
        "v_max3_u32 v65, v65, v53, v54\n\t"
        "v_max_u32 v65, v65, v55\n\t"
        "v_cmp_eq_u32 vcc, -1, v65\n\t"
        "s_cbranch_vccz Lmrd_%=\n\t"
        "s_add_u32 s50, s50, 1\n\t"
        "s_cmp_lt_u32 s50, s49\n\t"
        "s_cbranch_scc1 Lmrl_%=\n\t"
        "s_mov_b32 s49, 0\n\t"
        "Lmrd_%=:\n\t"
        "v_mov_b64 v[60:61], 0\n\t"
        "v_mov_b64 v[62:63], 0\n\t"
        "s_nop 1\n\t"
        "v_smfmac_f32_16x16x64_bf16 v[60:63], v[56:59], v[40:47], v64\n\t"
        "v_smfmac_f32_16x16x64_bf16 v[60:63], v[56:59], v[48:55], v64\n\t"
        "s_mov_b64 vcc, 0\n\t"
        "s_nop 7\n\t"
        "s_branch Lmrok_%=\n\t"
        :
        : [pw] "s"(ws), [w] "s"(((u & 2) ? MED : 0) - l * M1D), [fs] "s"(0), [fi] "s"(O_TS + l * 8192), [in] "s"((MPN ? O_WOP : O_MP) + l * 8192 + (u >> 1) * 16384 + (u & 1) * 128), [out] "s"(O_MEMB + MEMB_STRIDE + l * 8192 + (DMB ? (u >> 4) * 2048 + (u & 1) * 1024 + (u & 14) * 64 : u * 128)), [fo] "s"(0),
          [db] "s"(db), [gc] "s"(gc), [gd] "s"(O_HR + id * 4), [fa] "v"((lane >> 3) * 256 + (lane & 7) * 16), [sv] "v"(MRX ? ((lane >> 4 & 2) + (lane >> 3 & 1)) * 16384 + (lane & 16) * 4 + ((0x9C >> (lane & 6) ^ u >> 2) & 3) * 16 + (lane & 1) * 8 : DMB ? ((lane >> 3 & 4) + (lane >> 2 & 3)) * 16384 + (lane & 16) * 4 + ((0x369C >> (lane & 7) * 2 ^ u >> 2) & 3) * 16 : (lane >> 3) * 16384 + (lane & 7) * 16),
          [s0] "v"((lane & 2 << DMB) ? 0u : one), [s1] "v"((lane & 2 << DMB) ? one : 0u), [idx] "v"((lane & (DMB ? 2 : 8)) ? 0xEEEE : 0x4444)
        : "memory", "vcc", "scc", "v32", "v33", "v34", "v35", "v36", "v37", "v38", "v39", "v40", "v41", "v42", "v43", "v44", "v45", "v46", "v47", "v48", "v49", "v50", "v51", "v52", "v53", "v54", "v55", "v56", "v57", "v58", "v59", "v60", "v61", "v62", "v63", "v64", "v65", "v66", "v67", "v68", "v69", "v70", "v71", "v72", "v73", "v74", "v75", "v76", "v77", "v78", "v79", "v80", "v81", "v82", "v83", "v84", "v85", "v86", "v87", "v88", "v89", "v90", "v91", "v92", "v93", "v94", "v95", "v96", "v97", "v98", "v99", "v100", "v101", "v102", "v103", "v104", "v105", "v106", "v107", "v108", "v109", "v110", "v111", "v112", "v113", "v114", "v115", "v116", "v117", "v118", "v119", "v120", "v121", "v122", "v123", "v124", "v125", "v126", "v127", "v128", "v129", "v130", "v131", "v132", "v133", "v134", "v135", "v136", "v137", "v138", "v139", "v140", "v141", "v142", "v143", "v144", "v145", "v146", "v147", "v148", "v149", "v150", "v151", "v152", "v153", "v154", "v155", "v156", "v157", "v158", "v159", "v160", "v161", "v162", "v163", "v164", "v165", "v166", "v167", "v168", "v169", "v170", "v171", "v172", "v173", "v174", "v175", "v176", "v177", "v178", "v179", "v180", "v181", "v182", "v183", "v184", "v185", "v186", "v187", "v188", "v189", "v190", "v191", "v192", "v193", "v194", "v195", "v196", "v197", "v198", "v199", "v200", "v201", "v202", "v203", "v204", "v205", "v206", "v207", "v208", "v209", "v210", "v211", "v212", "v213", "v214", "v215", "v216", "v217", "v218", "v219", "v220", "v221", "v222", "v223", "v224", "v225", "v226", "v227", "v228", "v229", "v230", "v231", "v232", "v233", "v234", "v235", "v236", "v237", "v238", "v239", "v240", "v241", "v242", "v243", "v244", "v245", "v246", "v247", "v248", "v249", "v250", "v251", "v252", "v253", "v254", "v255", "a0", "a1", "a2", "a3", "a4", "a5", "a6", "a7", "a8", "a9", "a10", "a11", "a12", "a13", "a14", "a15", "a16", "a17", "a18", "a19", "a20", "a21", "a22", "a23", "a24", "a25", "a26", "a27", "a28", "a29", "a30", "a31", "a32", "a33", "a34", "a35", "a36", "a37", "a38", "a39", "a40", "a41", "a42", "a43", "a44", "a45", "a46", "a47", "a48", "a49", "a50", "a51", "a52", "a53", "a54", "a55", "a56", "a57", "a58", "a59", "a60", "a61", "a62", "a63", "a64", "a65", "a66", "a67", "a68", "a69", "a70", "a71", "a72", "a73", "a74", "a75", "a76", "a77", "a78", "a79", "a80", "a81", "a82", "a83", "a84", "a85", "a86", "a87", "a88", "a89", "a90", "a91", "a92", "a93", "a94", "a95", "a96", "a97", "a98", "a99", "a100", "a101", "a102", "a103", "a104", "a105", "a106", "a107", "a108", "a109", "a110", "a111", "a112", "a113", "a114", "a115", "a116", "a117", "a118", "a119", "a120", "a121", "a122", "a123", "a124", "a125", "a126", "a127", "a128", "a129", "a130", "a131", "a132", "a133", "a134", "a135", "a136", "a137", "a138", "a139", "a140", "a141", "a142", "a143", "a144", "a145", "a146", "a147", "a148", "a149", "a150", "a151", "a152", "a153", "a154", "a155", "a156", "a157", "a158", "a159", "a160", "a161", "a162", "a163", "a164", "a165", "a166", "a167", "a168", "a169", "a170", "a171", "a172", "a173", "a174", "a175", "a176", "a177", "a178", "a179", "a180", "a181", "a182", "a183", "a184", "a185", "a186", "a187", "a188", "a189", "a190", "a191", "a192", "a193", "a194", "a195", "a196", "a197", "a198", "a199", "a200", "a201", "a202", "a203", "a204", "a205", "a206", "a207", "a208", "a209", "a210", "a211", "a212", "a213", "a214", "a215", "a216", "a217", "a218", "a219", "a220", "a221", "a222", "a223", "a224", "a225", "a226", "a227", "a228", "a229", "a230", "a231", "a232", "a233", "a234", "a235", "a236", "a237", "a238", "a239", "a240", "a241", "a242", "a243", "a244", "a245", "a246", "a247", "a248", "a249", "a250", "a251", "a252", "a253", "a254", "a255", "s40", "s41", "s42", "s43", "s44", "s45", "s46", "s47", "s48", "s49", "s50", "s51", "s52", "s53", "s54", "s55", "s56", "s57", "s58", "s59", "s60", "s61", "s62", "s63", "s64", "s65", "s66", "s67", "s68", "s69", "s70", "s71", "s72", "s73", "s74", "s75", "s76", "s77", "s78", "s79", "s80", "s81", "s82", "s83", "s84", "s85", "s86", "s87", "s88", "s89");
    __builtin_unreachable();
}

DI void mlp_role(unsigned char* ws, const u16* w1, const u16* w2, const u16* __restrict__ b1, int l, int f, int w, int lane, int id, unsigned gc) {
    const int cc = lane & 15, g = lane >> 4;
    float b[8];
#pragma unroll
    for (int i = 0; i < 8; ++i) { const int fc = 128 * f + 32 * w + 16 * (i >> 2) + 4 * g + (i & 3); b[i] = fc < FDIM ? bf2f(b1[fc]) : 0.f; }
    asm volatile(
        "s_mov_b64 s[40:41], %[pw]\n\t"
        "s_mov_b32 s42, 184549376\n\t"
        "s_mov_b32 s43, 0x20000\n\t"
        "s_mov_b32 s48, %[w]\n\t"
        "s_mov_b32 s52, %[fi]\n\t"
        "s_mov_b32 s53, %[in]\n\t"
        "s_mov_b32 s54, %[out]\n\t"
        "s_mov_b32 s55, %[fo]\n\t"
        "s_mov_b32 s56, %[db]\n\t"
        "s_mov_b32 s75, %[fs]\n\t"
        "s_memrealtime s[72:73]\n\t"
        "s_mov_b32 s49, 131072\n\t"
        "s_mov_b32 s51, 16777216\n\t"
        "s_waitcnt lgkmcnt(0)\n\t"
        "s_mov_b32 s74, s72\n\t"
        "s_mov_b32 s73, 1480\n\t"
        "v_mbcnt_lo_u32_b32 v38, -1, 0\n\t"
        "v_mbcnt_hi_u32_b32 v38, -1, v38\n\t"
        "v_lshlrev_b32 v32, 4, v38\n\t"
        "v_mul_u32_u24 v36, 512, v38\n\t"
        "v_mov_b32 v33, %[fa]\n\t"
        "v_mov_b32 v35, %[sv]\n\t"
        "s_mov_b64 s[44:45], %[pa]\n\t"
        "s_mov_b32 s46, 1409024\n\t"
        "s_mov_b32 s47, 0x20000\n\t"
        "s_mov_b64 s[68:69], %[pb]\n\t"
        "s_mov_b32 s70, 1409024\n\t"
        "s_mov_b32 s71, 0x20000\n\t"
        "s_mov_b32 s57, %[wb]\n\t"
        "s_mov_b32 s59, %[wc]\n\t"
        "v_mov_b32 v40, %[ga]\n\t"
        "s_mov_b32 s62, 0x05040100\n\t"
        "s_mov_b32 s63, 0x07060302\n\t"
        "s_lshl_b32 s60, s48, 15\n\t"
        "v_lshrrev_b32 v41, 2, v38\n\t"
        "v_mul_u32_u24 v41, 2752, v41\n\t"
        "v_lshrrev_b32 v44, 4, v38\n\t"
        "v_and_b32 v44, 2, v44\n\t"
        "v_xor_b32 v44, v44, v38\n\t"
        "v_and_b32 v44, 3, v44\n\t"
        "v_lshl_add_u32 v41, v44, 4, v41\n\t"
        "v_and_b32 v42, 48, v38\n\t"
        "v_lshlrev_b32 v42, 5, v42\n\t"
        "v_and_b32 v44, 12, v38\n\t"
        "v_lshl_add_u32 v42, v44, 4, v42\n\t"
        "v_and_b32 v44, 16, v38\n\t"
        "v_lshl_add_u32 v42, v44, 1, v42\n\t"
        "v_and_b32 v44, 3, v38\n\t"
        "v_lshl_add_u32 v42, v44, 3, v42\n\t"
        "v_add_u32 v42, s60, v42\n\t"
        "v_xor_b32 v43, 32, v42\n\t"
        "s_add_u32 m0, s60, 0\n\t"
        "s_add_u32 s58, s57, 0\n\t"
        "buffer_load_dwordx4 v41, s[44:47], s58 offen lds\n\t"
        "s_add_u32 s58, s57, 43008\n\t"
        "buffer_load_dwordx4 v41, s[44:47], s58 offen offset:1024 lds\n\t"
        "s_add_u32 m0, s60, 2048\n\t"
        "s_add_u32 s58, s57, 88064\n\t"
        "buffer_load_dwordx4 v41, s[44:47], s58 offen lds\n\t"
        "s_add_u32 s58, s57, 131072\n\t"
        "buffer_load_dwordx4 v41, s[44:47], s58 offen offset:1024 lds\n\t"
        "s_add_u32 m0, s60, 4096\n\t"
        "s_add_u32 s58, s57, 176128\n\t"
        "buffer_load_dwordx4 v41, s[44:47], s58 offen lds\n\t"
        "s_add_u32 s58, s57, 219136\n\t"
        "buffer_load_dwordx4 v41, s[44:47], s58 offen offset:1024 lds\n\t"
        "s_add_u32 m0, s60, 6144\n\t"
        "s_add_u32 s58, s57, 264192\n\t"
        "buffer_load_dwordx4 v41, s[44:47], s58 offen lds\n\t"
        "s_add_u32 s58, s57, 307200\n\t"
        "buffer_load_dwordx4 v41, s[44:47], s58 offen offset:1024 lds\n\t"
        "s_add_u32 m0, s60, 8192\n\t"
        "s_add_u32 s58, s57, 352256\n\t"
        "buffer_load_dwordx4 v41, s[44:47], s58 offen lds\n\t"
        "s_add_u32 s58, s57, 395264\n\t"
        "buffer_load_dwordx4 v41, s[44:47], s58 offen offset:1024 lds\n\t"
        "s_add_u32 m0, s60, 10240\n\t"
        "s_add_u32 s58, s57, 440320\n\t"
        "buffer_load_dwordx4 v41, s[44:47], s58 offen lds\n\t"
        "s_add_u32 s58, s57, 483328\n\t"
        "buffer_load_dwordx4 v41, s[44:47], s58 offen offset:1024 lds\n\t"
        "s_add_u32 m0, s60, 12288\n\t"
        "s_add_u32 s58, s57, 528384\n\t"
        "buffer_load_dwordx4 v41, s[44:47], s58 offen lds\n\t"
        "s_add_u32 s58, s57, 571392\n\t"
        "buffer_load_dwordx4 v41, s[44:47], s58 offen offset:1024 lds\n\t"
        "s_add_u32 m0, s60, 14336\n\t"
        "s_add_u32 s58, s57, 616448\n\t"
        "buffer_load_dwordx4 v41, s[44:47], s58 offen lds\n\t"
        "s_add_u32 s58, s57, 659456\n\t"
        "buffer_load_dwordx4 v41, s[44:47], s58 offen offset:1024 lds\n\t"
        "s_add_u32 m0, s60, 16384\n\t"
        "s_add_u32 s58, s57, 704512\n\t"
        "buffer_load_dwordx4 v41, s[44:47], s58 offen lds\n\t"
        "s_add_u32 s58, s57, 747520\n\t"
        "buffer_load_dwordx4 v41, s[44:47], s58 offen offset:1024 lds\n\t"
        "s_add_u32 m0, s60, 18432\n\t"
        "s_add_u32 s58, s57, 792576\n\t"
        "buffer_load_dwordx4 v41, s[44:47], s58 offen lds\n\t"
        "s_add_u32 s58, s57, 835584\n\t"
        "buffer_load_dwordx4 v41, s[44:47], s58 offen offset:1024 lds\n\t"
        "s_add_u32 m0, s60, 20480\n\t"
        "s_add_u32 s58, s57, 880640\n\t"
        "buffer_load_dwordx4 v41, s[44:47], s58 offen lds\n\t"
        "s_add_u32 s58, s57, 923648\n\t"
        "buffer_load_dwordx4 v41, s[44:47], s58 offen offset:1024 lds\n\t"
        "s_add_u32 m0, s60, 22528\n\t"
        "s_add_u32 s58, s57, 968704\n\t"
        "buffer_load_dwordx4 v41, s[44:47], s58 offen lds\n\t"
        "s_add_u32 s58, s57, 1011712\n\t"
        "buffer_load_dwordx4 v41, s[44:47], s58 offen offset:1024 lds\n\t"
        "s_add_u32 m0, s60, 24576\n\t"
        "s_add_u32 s58, s57, 1056768\n\t"
        "buffer_load_dwordx4 v41, s[44:47], s58 offen lds\n\t"
        "s_add_u32 s58, s57, 1099776\n\t"
        "buffer_load_dwordx4 v41, s[44:47], s58 offen offset:1024 lds\n\t"
        "s_add_u32 m0, s60, 26624\n\t"
        "s_add_u32 s58, s57, 1144832\n\t"
        "buffer_load_dwordx4 v41, s[44:47], s58 offen lds\n\t"
        "s_add_u32 s58, s57, 1187840\n\t"
        "buffer_load_dwordx4 v41, s[44:47], s58 offen offset:1024 lds\n\t"
        "s_add_u32 m0, s60, 28672\n\t"
        "s_add_u32 s58, s57, 1232896\n\t"
        "buffer_load_dwordx4 v41, s[44:47], s58 offen lds\n\t"
        "s_add_u32 s58, s57, 1275904\n\t"
        "buffer_load_dwordx4 v41, s[44:47], s58 offen offset:1024 lds\n\t"
        "s_add_u32 m0, s60, 30720\n\t"
        "s_add_u32 s58, s57, 1320960\n\t"
        "buffer_load_dwordx4 v41, s[44:47], s58 offen lds\n\t"
        "s_add_u32 s58, s57, 1363968\n\t"
        "buffer_load_dwordx4 v41, s[44:47], s58 offen offset:1024 lds\n\t"
        "s_add_u32 s58, s59, 0\n\t"
        "buffer_load_dwordx2 v[64:65], v40, s[68:71], s58 offen offset:0\n\t"
        "buffer_load_dwordx2 v[66:67], v40, s[68:71], s58 offen offset:1024\n\t"
        "buffer_load_dwordx2 v[68:69], v40, s[68:71], s58 offen offset:2048\n\t"
        "buffer_load_dwordx2 v[70:71], v40, s[68:71], s58 offen offset:3072\n\t"
        "s_add_u32 s58, s59, 16384\n\t"
        "buffer_load_dwordx2 v[72:73], v40, s[68:71], s58 offen offset:0\n\t"
        "buffer_load_dwordx2 v[74:75], v40, s[68:71], s58 offen offset:1024\n\t"
        "buffer_load_dwordx2 v[76:77], v40, s[68:71], s58 offen offset:2048\n\t"
        "buffer_load_dwordx2 v[78:79], v40, s[68:71], s58 offen offset:3072\n\t"
        "s_add_u32 s58, s59, 256\n\t"
        "buffer_load_dwordx2 v[80:81], v40, s[68:71], s58 offen offset:0\n\t"
        "buffer_load_dwordx2 v[82:83], v40, s[68:71], s58 offen offset:1024\n\t"
        "buffer_load_dwordx2 v[84:85], v40, s[68:71], s58 offen offset:2048\n\t"
        "buffer_load_dwordx2 v[86:87], v40, s[68:71], s58 offen offset:3072\n\t"
        "s_add_u32 s58, s59, 16640\n\t"
        "buffer_load_dwordx2 v[88:89], v40, s[68:71], s58 offen offset:0\n\t"
        "buffer_load_dwordx2 v[90:91], v40, s[68:71], s58 offen offset:1024\n\t"
        "buffer_load_dwordx2 v[92:93], v40, s[68:71], s58 offen offset:2048\n\t"
        "buffer_load_dwordx2 v[94:95], v40, s[68:71], s58 offen offset:3072\n\t"
        "s_add_u32 s58, s59, 32768\n\t"
        "buffer_load_dwordx2 v[96:97], v40, s[68:71], s58 offen offset:0\n\t"
        "buffer_load_dwordx2 v[98:99], v40, s[68:71], s58 offen offset:1024\n\t"
        "buffer_load_dwordx2 v[100:101], v40, s[68:71], s58 offen offset:2048\n\t"
        "buffer_load_dwordx2 v[102:103], v40, s[68:71], s58 offen offset:3072\n\t"
        "s_add_u32 s58, s59, 49152\n\t"
        "buffer_load_dwordx2 v[104:105], v40, s[68:71], s58 offen offset:0\n\t"
        "buffer_load_dwordx2 v[106:107], v40, s[68:71], s58 offen offset:1024\n\t"
        "buffer_load_dwordx2 v[108:109], v40, s[68:71], s58 offen offset:2048\n\t"
        "buffer_load_dwordx2 v[110:111], v40, s[68:71], s58 offen offset:3072\n\t"
        "s_waitcnt vmcnt(54)\n\t"
        "ds_read_b64_tr_b16 a[0:1], v42 offset:0\n\t"
        "ds_read_b64_tr_b16 a[2:3], v42 offset:256\n\t"
        "ds_read_b64_tr_b16 a[64:65], v43 offset:0\n\t"
        "ds_read_b64_tr_b16 a[66:67], v43 offset:256\n\t"
        "s_waitcnt vmcnt(52)\n\t"
        "ds_read_b64_tr_b16 a[4:5], v42 offset:2048\n\t"
        "ds_read_b64_tr_b16 a[6:7], v42 offset:2304\n\t"
        "ds_read_b64_tr_b16 a[68:69], v43 offset:2048\n\t"
        "ds_read_b64_tr_b16 a[70:71], v43 offset:2304\n\t"
        "s_waitcnt vmcnt(50)\n\t"
        "ds_read_b64_tr_b16 a[8:9], v42 offset:4096\n\t"
        "ds_read_b64_tr_b16 a[10:11], v42 offset:4352\n\t"
        "ds_read_b64_tr_b16 a[72:73], v43 offset:4096\n\t"
        "ds_read_b64_tr_b16 a[74:75], v43 offset:4352\n\t"
        "s_waitcnt vmcnt(48)\n\t"
        "ds_read_b64_tr_b16 a[12:13], v42 offset:6144\n\t"
        "ds_read_b64_tr_b16 a[14:15], v42 offset:6400\n\t"
        "ds_read_b64_tr_b16 a[76:77], v43 offset:6144\n\t"
        "ds_read_b64_tr_b16 a[78:79], v43 offset:6400\n\t"
        "s_add_u32 s58, s59, 33024\n\t"
        "buffer_load_dwordx2 v[112:113], v40, s[68:71], s58 offen offset:0\n\t"
        "buffer_load_dwordx2 v[114:115], v40, s[68:71], s58 offen offset:1024\n\t"
        "buffer_load_dwordx2 v[116:117], v40, s[68:71], s58 offen offset:2048\n\t"
        "buffer_load_dwordx2 v[118:119], v40, s[68:71], s58 offen offset:3072\n\t"
        "s_add_u32 s58, s59, 49408\n\t"
        "buffer_load_dwordx2 v[120:121], v40, s[68:71], s58 offen offset:0\n\t"
        "buffer_load_dwordx2 v[122:123], v40, s[68:71], s58 offen offset:1024\n\t"
        "buffer_load_dwordx2 v[124:125], v40, s[68:71], s58 offen offset:2048\n\t"
        "buffer_load_dwordx2 v[126:127], v40, s[68:71], s58 offen offset:3072\n\t"
        "s_waitcnt vmcnt(54)\n\t"
        "ds_read_b64_tr_b16 a[16:17], v42 offset:8192\n\t"
        "ds_read_b64_tr_b16 a[18:19], v42 offset:8448\n\t"
        "ds_read_b64_tr_b16 a[80:81], v43 offset:8192\n\t"
        "ds_read_b64_tr_b16 a[82:83], v43 offset:8448\n\t"
        "s_waitcnt vmcnt(52)\n\t"
        "ds_read_b64_tr_b16 a[20:21], v42 offset:10240\n\t"
        "ds_read_b64_tr_b16 a[22:23], v42 offset:10496\n\t"
        "ds_read_b64_tr_b16 a[84:85], v43 offset:10240\n\t"
        "ds_read_b64_tr_b16 a[86:87], v43 offset:10496\n\t"
        "s_waitcnt vmcnt(50)\n\t"
        "ds_read_b64_tr_b16 a[24:25], v42 offset:12288\n\t"
        "ds_read_b64_tr_b16 a[26:27], v42 offset:12544\n\t"
        "ds_read_b64_tr_b16 a[88:89], v43 offset:12288\n\t"
        "ds_read_b64_tr_b16 a[90:91], v43 offset:12544\n\t"
        "s_waitcnt vmcnt(48)\n\t"
        "ds_read_b64_tr_b16 a[28:29], v42 offset:14336\n\t"
        "ds_read_b64_tr_b16 a[30:31], v42 offset:14592\n\t"
        "ds_read_b64_tr_b16 a[92:93], v43 offset:14336\n\t"
        "ds_read_b64_tr_b16 a[94:95], v43 offset:14592\n\t"
        "s_add_u32 s58, s59, 65536\n\t"
        "buffer_load_dwordx2 v[128:129], v40, s[68:71], s58 offen offset:0\n\t"
        "buffer_load_dwordx2 v[130:131], v40, s[68:71], s58 offen offset:1024\n\t"
        "buffer_load_dwordx2 v[132:133], v40, s[68:71], s58 offen offset:2048\n\t"
        "buffer_load_dwordx2 v[134:135], v40, s[68:71], s58 offen offset:3072\n\t"
        "s_add_u32 s58, s59, 81920\n\t"
        "buffer_load_dwordx2 v[136:137], v40, s[68:71], s58 offen offset:0\n\t"
        "buffer_load_dwordx2 v[138:139], v40, s[68:71], s58 offen offset:1024\n\t"
        "buffer_load_dwordx2 v[140:141], v40, s[68:71], s58 offen offset:2048\n\t"
        "buffer_load_dwordx2 v[142:143], v40, s[68:71], s58 offen offset:3072\n\t"
        "s_waitcnt vmcnt(54)\n\t"
        "ds_read_b64_tr_b16 a[32:33], v42 offset:16384\n\t"
        "ds_read_b64_tr_b16 a[34:35], v42 offset:16640\n\t"
        "ds_read_b64_tr_b16 a[96:97], v43 offset:16384\n\t"
        "ds_read_b64_tr_b16 a[98:99], v43 offset:16640\n\t"
        "s_waitcnt vmcnt(52)\n\t"
        "ds_read_b64_tr_b16 a[36:37], v42 offset:18432\n\t"
        "ds_read_b64_tr_b16 a[38:39], v42 offset:18688\n\t"
        "ds_read_b64_tr_b16 a[100:101], v43 offset:18432\n\t"
        "ds_read_b64_tr_b16 a[102:103], v43 offset:18688\n\t"
        "s_waitcnt vmcnt(50)\n\t"
        "ds_read_b64_tr_b16 a[40:41], v42 offset:20480\n\t"
        "ds_read_b64_tr_b16 a[42:43], v42 offset:20736\n\t"
        "ds_read_b64_tr_b16 a[104:105], v43 offset:20480\n\t"
        "ds_read_b64_tr_b16 a[106:107], v43 offset:20736\n\t"
        "s_waitcnt vmcnt(48)\n\t"
        "ds_read_b64_tr_b16 a[44:45], v42 offset:22528\n\t"
        "ds_read_b64_tr_b16 a[46:47], v42 offset:22784\n\t"
        "ds_read_b64_tr_b16 a[108:109], v43 offset:22528\n\t"
        "ds_read_b64_tr_b16 a[110:111], v43 offset:22784\n\t"
        "s_add_u32 s58, s59, 65792\n\t"
        "buffer_load_dwordx2 v[144:145], v40, s[68:71], s58 offen offset:0\n\t"
        "buffer_load_dwordx2 v[146:147], v40, s[68:71], s58 offen offset:1024\n\t"
        "buffer_load_dwordx2 v[148:149], v40, s[68:71], s58 offen offset:2048\n\t"
        "buffer_load_dwordx2 v[150:151], v40, s[68:71], s58 offen offset:3072\n\t"
        "s_add_u32 s58, s59, 82176\n\t"
        "buffer_load_dwordx2 v[152:153], v40, s[68:71], s58 offen offset:0\n\t"
        "buffer_load_dwordx2 v[154:155], v40, s[68:71], s58 offen offset:1024\n\t"
        "buffer_load_dwordx2 v[156:157], v40, s[68:71], s58 offen offset:2048\n\t"
        "buffer_load_dwordx2 v[158:159], v40, s[68:71], s58 offen offset:3072\n\t"
        "s_waitcnt vmcnt(54)\n\t"
        "ds_read_b64_tr_b16 a[48:49], v42 offset:24576\n\t"
        "ds_read_b64_tr_b16 a[50:51], v42 offset:24832\n\t"
        "ds_read_b64_tr_b16 a[112:113], v43 offset:24576\n\t"
        "ds_read_b64_tr_b16 a[114:115], v43 offset:24832\n\t"
        "s_waitcnt vmcnt(52)\n\t"
        "ds_read_b64_tr_b16 a[52:53], v42 offset:26624\n\t"
        "ds_read_b64_tr_b16 a[54:55], v42 offset:26880\n\t"
        "ds_read_b64_tr_b16 a[116:117], v43 offset:26624\n\t"
        "ds_read_b64_tr_b16 a[118:119], v43 offset:26880\n\t"
        "s_waitcnt vmcnt(50)\n\t"
        "ds_read_b64_tr_b16 a[56:57], v42 offset:28672\n\t"
        "ds_read_b64_tr_b16 a[58:59], v42 offset:28928\n\t"
        "ds_read_b64_tr_b16 a[120:121], v43 offset:28672\n\t"
        "ds_read_b64_tr_b16 a[122:123], v43 offset:28928\n\t"
        "s_waitcnt vmcnt(48)\n\t"
        "ds_read_b64_tr_b16 a[60:61], v42 offset:30720\n\t"
        "ds_read_b64_tr_b16 a[62:63], v42 offset:30976\n\t"
        "ds_read_b64_tr_b16 a[124:125], v43 offset:30720\n\t"
        "ds_read_b64_tr_b16 a[126:127], v43 offset:30976\n\t"
        "s_add_u32 s58, s59, 98304\n\t"
        "buffer_load_dwordx2 v[160:161], v40, s[68:71], s58 offen offset:0\n\t"
        "buffer_load_dwordx2 v[162:163], v40, s[68:71], s58 offen offset:1024\n\t"
        "buffer_load_dwordx2 v[164:165], v40, s[68:71], s58 offen offset:2048\n\t"
        "buffer_load_dwordx2 v[166:167], v40, s[68:71], s58 offen offset:3072\n\t"
        "s_add_u32 s58, s59, 114688\n\t"
        "buffer_load_dwordx2 v[168:169], v40, s[68:71], s58 offen offset:0\n\t"
        "buffer_load_dwordx2 v[170:171], v40, s[68:71], s58 offen offset:1024\n\t"
        "buffer_load_dwordx2 v[172:173], v40, s[68:71], s58 offen offset:2048\n\t"
        "buffer_load_dwordx2 v[174:175], v40, s[68:71], s58 offen offset:3072\n\t"
        "s_add_u32 s58, s59, 98560\n\t"
        "buffer_load_dwordx2 v[176:177], v40, s[68:71], s58 offen offset:0\n\t"
        "buffer_load_dwordx2 v[178:179], v40, s[68:71], s58 offen offset:1024\n\t"
        "buffer_load_dwordx2 v[180:181], v40, s[68:71], s58 offen offset:2048\n\t"
        "buffer_load_dwordx2 v[182:183], v40, s[68:71], s58 offen offset:3072\n\t"
        "s_add_u32 s58, s59, 114944\n\t"
        "buffer_load_dwordx2 v[184:185], v40, s[68:71], s58 offen offset:0\n\t"
        "buffer_load_dwordx2 v[186:187], v40, s[68:71], s58 offen offset:1024\n\t"
        "buffer_load_dwordx2 v[188:189], v40, s[68:71], s58 offen offset:2048\n\t"
        "buffer_load_dwordx2 v[190:191], v40, s[68:71], s58 offen offset:3072\n\t"
        "s_waitcnt vmcnt(62)\n\t"
        "s_waitcnt vmcnt(60)\n\t"
        "s_waitcnt vmcnt(58)\n\t"
        "v_perm_b32 v48, v66, v64, s62\n\t"
        "v_perm_b32 v49, v70, v68, s62\n\t"
        "v_perm_b32 v50, v74, v72, s62\n\t"
        "s_waitcnt vmcnt(56)\n\t"
        "v_perm_b32 v51, v78, v76, s62\n\t"
        "v_accvgpr_write_b32 a128, v48\n\t"
        "v_accvgpr_write_b32 a129, v49\n\t"
        "v_accvgpr_write_b32 a130, v50\n\t"
        "v_accvgpr_write_b32 a131, v51\n\t"
        "v_perm_b32 v52, v66, v64, s63\n\t"
        "v_perm_b32 v53, v70, v68, s63\n\t"
        "v_perm_b32 v54, v74, v72, s63\n\t"
        "v_perm_b32 v55, v78, v76, s63\n\t"
        "v_accvgpr_write_b32 a144, v52\n\t"
        "v_accvgpr_write_b32 a145, v53\n\t"
        "v_accvgpr_write_b32 a146, v54\n\t"
        "v_accvgpr_write_b32 a147, v55\n\t"
        "v_perm_b32 v48, v67, v65, s62\n\t"
        "v_perm_b32 v49, v71, v69, s62\n\t"
        "v_perm_b32 v50, v75, v73, s62\n\t"
        "v_perm_b32 v51, v79, v77, s62\n\t"
        "v_accvgpr_write_b32 a160, v48\n\t"
        "v_accvgpr_write_b32 a161, v49\n\t"
        "v_accvgpr_write_b32 a162, v50\n\t"
        "v_accvgpr_write_b32 a163, v51\n\t"
        "v_perm_b32 v52, v67, v65, s63\n\t"
        "v_perm_b32 v53, v71, v69, s63\n\t"
        "v_perm_b32 v54, v75, v73, s63\n\t"
        "v_perm_b32 v55, v79, v77, s63\n\t"
        "v_accvgpr_write_b32 a176, v52\n\t"
        "v_accvgpr_write_b32 a177, v53\n\t"
        "v_accvgpr_write_b32 a178, v54\n\t"
        "v_accvgpr_write_b32 a179, v55\n\t"
        "s_waitcnt vmcnt(54)\n\t"
        "s_waitcnt vmcnt(52)\n\t"
        "s_waitcnt vmcnt(50)\n\t"
        "v_perm_b32 v48, v82, v80, s62\n\t"
        "v_perm_b32 v49, v86, v84, s62\n\t"
        "v_perm_b32 v50, v90, v88, s62\n\t"
        "s_waitcnt vmcnt(48)\n\t"
        "v_perm_b32 v51, v94, v92, s62\n\t"
        "v_accvgpr_write_b32 a192, v48\n\t"
        "v_accvgpr_write_b32 a193, v49\n\t"
        "v_accvgpr_write_b32 a194, v50\n\t"
        "v_accvgpr_write_b32 a195, v51\n\t"
        "v_perm_b32 v52, v82, v80, s63\n\t"
        "v_perm_b32 v53, v86, v84, s63\n\t"
        "v_perm_b32 v54, v90, v88, s63\n\t"
        "v_perm_b32 v55, v94, v92, s63\n\t"
        "v_accvgpr_write_b32 a208, v52\n\t"
        "v_accvgpr_write_b32 a209, v53\n\t"
        "v_accvgpr_write_b32 a210, v54\n\t"
        "v_accvgpr_write_b32 a211, v55\n\t"
        "v_perm_b32 v48, v83, v81, s62\n\t"
        "v_perm_b32 v49, v87, v85, s62\n\t"
        "v_perm_b32 v50, v91, v89, s62\n\t"
        "v_perm_b32 v51, v95, v93, s62\n\t"
        "v_accvgpr_write_b32 a224, v48\n\t"
        "v_accvgpr_write_b32 a225, v49\n\t"
        "v_accvgpr_write_b32 a226, v50\n\t"
        "v_accvgpr_write_b32 a227, v51\n\t"
        "v_perm_b32 v52, v83, v81, s63\n\t"
        "v_perm_b32 v53, v87, v85, s63\n\t"
        "v_perm_b32 v54, v91, v89, s63\n\t"
        "v_perm_b32 v55, v95, v93, s63\n\t"
        "v_accvgpr_write_b32 a240, v52\n\t"
        "v_accvgpr_write_b32 a241, v53\n\t"
        "v_accvgpr_write_b32 a242, v54\n\t"
        "v_accvgpr_write_b32 a243, v55\n\t"
        "s_waitcnt vmcnt(46)\n\t"
        "s_waitcnt vmcnt(44)\n\t"
        "s_waitcnt vmcnt(42)\n\t"
        "v_perm_b32 v48, v98, v96, s62\n\t"
        "v_perm_b32 v49, v102, v100, s62\n\t"
        "v_perm_b32 v50, v106, v104, s62\n\t"
        "s_waitcnt vmcnt(40)\n\t"
        "v_perm_b32 v51, v110, v108, s62\n\t"
        "v_accvgpr_write_b32 a132, v48\n\t"
        "v_accvgpr_write_b32 a133, v49\n\t"
        "v_accvgpr_write_b32 a134, v50\n\t"
        "v_accvgpr_write_b32 a135, v51\n\t"
        "v_perm_b32 v52, v98, v96, s63\n\t"
        "v_perm_b32 v53, v102, v100, s63\n\t"
        "v_perm_b32 v54, v106, v104, s63\n\t"
        "v_perm_b32 v55, v110, v108, s63\n\t"
        "v_accvgpr_write_b32 a148, v52\n\t"
        "v_accvgpr_write_b32 a149, v53\n\t"
        "v_accvgpr_write_b32 a150, v54\n\t"
        "v_accvgpr_write_b32 a151, v55\n\t"
        "v_perm_b32 v48, v99, v97, s62\n\t"
        "v_perm_b32 v49, v103, v101, s62\n\t"
        "v_perm_b32 v50, v107, v105, s62\n\t"
        "v_perm_b32 v51, v111, v109, s62\n\t"
        "v_accvgpr_write_b32 a164, v48\n\t"
        "v_accvgpr_write_b32 a165, v49\n\t"
        "v_accvgpr_write_b32 a166, v50\n\t"
        "v_accvgpr_write_b32 a167, v51\n\t"
        "v_perm_b32 v52, v99, v97, s63\n\t"
        "v_perm_b32 v53, v103, v101, s63\n\t"
        "v_perm_b32 v54, v107, v105, s63\n\t"
        "v_perm_b32 v55, v111, v109, s63\n\t"
        "v_accvgpr_write_b32 a180, v52\n\t"
        "v_accvgpr_write_b32 a181, v53\n\t"
        "v_accvgpr_write_b32 a182, v54\n\t"
        "v_accvgpr_write_b32 a183, v55\n\t"
        "s_waitcnt vmcnt(38)\n\t"
        "s_waitcnt vmcnt(36)\n\t"
        "s_waitcnt vmcnt(34)\n\t"
        "v_perm_b32 v48, v114, v112, s62\n\t"
        "v_perm_b32 v49, v118, v116, s62\n\t"
        "v_perm_b32 v50, v122, v120, s62\n\t"
        "s_waitcnt vmcnt(32)\n\t"
        "v_perm_b32 v51, v126, v124, s62\n\t"
        "v_accvgpr_write_b32 a196, v48\n\t"
        "v_accvgpr_write_b32 a197, v49\n\t"
        "v_accvgpr_write_b32 a198, v50\n\t"
        "v_accvgpr_write_b32 a199, v51\n\t"
        "v_perm_b32 v52, v114, v112, s63\n\t"
        "v_perm_b32 v53, v118, v116, s63\n\t"
        "v_perm_b32 v54, v122, v120, s63\n\t"
        "v_perm_b32 v55, v126, v124, s63\n\t"
        "v_accvgpr_write_b32 a212, v52\n\t"
        "v_accvgpr_write_b32 a213, v53\n\t"
        "v_accvgpr_write_b32 a214, v54\n\t"
        "v_accvgpr_write_b32 a215, v55\n\t"
        "v_perm_b32 v48, v115, v113, s62\n\t"
        "v_perm_b32 v49, v119, v117, s62\n\t"
        "v_perm_b32 v50, v123, v121, s62\n\t"
        "v_perm_b32 v51, v127, v125, s62\n\t"
        "v_accvgpr_write_b32 a228, v48\n\t"
        "v_accvgpr_write_b32 a229, v49\n\t"
        "v_accvgpr_write_b32 a230, v50\n\t"
        "v_accvgpr_write_b32 a231, v51\n\t"
        "v_perm_b32 v52, v115, v113, s63\n\t"
        "v_perm_b32 v53, v119, v117, s63\n\t"
        "v_perm_b32 v54, v123, v121, s63\n\t"
        "v_perm_b32 v55, v127, v125, s63\n\t"
        "v_accvgpr_write_b32 a244, v52\n\t"
        "v_accvgpr_write_b32 a245, v53\n\t"
        "v_accvgpr_write_b32 a246, v54\n\t"
        "v_accvgpr_write_b32 a247, v55\n\t"
        "s_waitcnt vmcnt(30)\n\t"
        "s_waitcnt vmcnt(28)\n\t"
        "s_waitcnt vmcnt(26)\n\t"
        "v_perm_b32 v48, v130, v128, s62\n\t"
        "v_perm_b32 v49, v134, v132, s62\n\t"
        "v_perm_b32 v50, v138, v136, s62\n\t"
        "s_waitcnt vmcnt(24)\n\t"
        "v_perm_b32 v51, v142, v140, s62\n\t"
        "v_accvgpr_write_b32 a136, v48\n\t"
        "v_accvgpr_write_b32 a137, v49\n\t"
        "v_accvgpr_write_b32 a138, v50\n\t"
        "v_accvgpr_write_b32 a139, v51\n\t"
        "v_perm_b32 v52, v130, v128, s63\n\t"
        "v_perm_b32 v53, v134, v132, s63\n\t"
        "v_perm_b32 v54, v138, v136, s63\n\t"
        "v_perm_b32 v55, v142, v140, s63\n\t"
        "v_accvgpr_write_b32 a152, v52\n\t"
        "v_accvgpr_write_b32 a153, v53\n\t"
        "v_accvgpr_write_b32 a154, v54\n\t"
        "v_accvgpr_write_b32 a155, v55\n\t"
        "v_perm_b32 v48, v131, v129, s62\n\t"
        "v_perm_b32 v49, v135, v133, s62\n\t"
        "v_perm_b32 v50, v139, v137, s62\n\t"
        "v_perm_b32 v51, v143, v141, s62\n\t"
        "v_accvgpr_write_b32 a168, v48\n\t"
        "v_accvgpr_write_b32 a169, v49\n\t"
        "v_accvgpr_write_b32 a170, v50\n\t"
        "v_accvgpr_write_b32 a171, v51\n\t"
        "v_perm_b32 v52, v131, v129, s63\n\t"
        "v_perm_b32 v53, v135, v133, s63\n\t"
        "v_perm_b32 v54, v139, v137, s63\n\t"
        "v_perm_b32 v55, v143, v141, s63\n\t"
        "v_accvgpr_write_b32 a184, v52\n\t"
        "v_accvgpr_write_b32 a185, v53\n\t"
        "v_accvgpr_write_b32 a186, v54\n\t"
        "v_accvgpr_write_b32 a187, v55\n\t"
        "s_waitcnt vmcnt(22)\n\t"
        "s_waitcnt vmcnt(20)\n\t"
        "s_waitcnt vmcnt(18)\n\t"
        "v_perm_b32 v48, v146, v144, s62\n\t"
        "v_perm_b32 v49, v150, v148, s62\n\t"
        "v_perm_b32 v50, v154, v152, s62\n\t"
        "s_waitcnt vmcnt(16)\n\t"
        "v_perm_b32 v51, v158, v156, s62\n\t"
        "v_accvgpr_write_b32 a200, v48\n\t"
        "v_accvgpr_write_b32 a201, v49\n\t"
        "v_accvgpr_write_b32 a202, v50\n\t"
        "v_accvgpr_write_b32 a203, v51\n\t"
        "v_perm_b32 v52, v146, v144, s63\n\t"
        "v_perm_b32 v53, v150, v148, s63\n\t"
        "v_perm_b32 v54, v154, v152, s63\n\t"
        "v_perm_b32 v55, v158, v156, s63\n\t"
        "v_accvgpr_write_b32 a216, v52\n\t"
        "v_accvgpr_write_b32 a217, v53\n\t"
        "v_accvgpr_write_b32 a218, v54\n\t"
        "v_accvgpr_write_b32 a219, v55\n\t"
        "v_perm_b32 v48, v147, v145, s62\n\t"
        "v_perm_b32 v49, v151, v149, s62\n\t"
        "v_perm_b32 v50, v155, v153, s62\n\t"
        "v_perm_b32 v51, v159, v157, s62\n\t"
        "v_accvgpr_write_b32 a232, v48\n\t"
        "v_accvgpr_write_b32 a233, v49\n\t"
        "v_accvgpr_write_b32 a234, v50\n\t"
        "v_accvgpr_write_b32 a235, v51\n\t"
        "v_perm_b32 v52, v147, v145, s63\n\t"
        "v_perm_b32 v53, v151, v149, s63\n\t"
        "v_perm_b32 v54, v155, v153, s63\n\t"
        "v_perm_b32 v55, v159, v157, s63\n\t"
        "v_accvgpr_write_b32 a248, v52\n\t"
        "v_accvgpr_write_b32 a249, v53\n\t"
        "v_accvgpr_write_b32 a250, v54\n\t"
        "v_accvgpr_write_b32 a251, v55\n\t"
        "s_waitcnt vmcnt(14)\n\t"
        "s_waitcnt vmcnt(12)\n\t"
        "s_waitcnt vmcnt(10)\n\t"
        "v_perm_b32 v48, v162, v160, s62\n\t"
        "v_perm_b32 v49, v166, v164, s62\n\t"
        "v_perm_b32 v50, v170, v168, s62\n\t"
        "s_waitcnt vmcnt(8)\n\t"
        "v_perm_b32 v51, v174, v172, s62\n\t"
        "v_accvgpr_write_b32 a140, v48\n\t"
        "v_accvgpr_write_b32 a141, v49\n\t"
        "v_accvgpr_write_b32 a142, v50\n\t"
        "v_accvgpr_write_b32 a143, v51\n\t"
        "v_perm_b32 v52, v162, v160, s63\n\t"
        "v_perm_b32 v53, v166, v164, s63\n\t"
        "v_perm_b32 v54, v170, v168, s63\n\t"
        "v_perm_b32 v55, v174, v172, s63\n\t"
        "v_accvgpr_write_b32 a156, v52\n\t"
        "v_accvgpr_write_b32 a157, v53\n\t"
        "v_accvgpr_write_b32 a158, v54\n\t"
        "v_accvgpr_write_b32 a159, v55\n\t"
        "v_perm_b32 v48, v163, v161, s62\n\t"
        "v_perm_b32 v49, v167, v165, s62\n\t"
        "v_perm_b32 v50, v171, v169, s62\n\t"
        "v_perm_b32 v51, v175, v173, s62\n\t"
        "v_accvgpr_write_b32 a172, v48\n\t"
        "v_accvgpr_write_b32 a173, v49\n\t"
        "v_accvgpr_write_b32 a174, v50\n\t"
        "v_accvgpr_write_b32 a175, v51\n\t"
        "v_perm_b32 v52, v163, v161, s63\n\t"
        "v_perm_b32 v53, v167, v165, s63\n\t"
        "v_perm_b32 v54, v171, v169, s63\n\t"
        "v_perm_b32 v55, v175, v173, s63\n\t"
        "v_accvgpr_write_b32 a188, v52\n\t"
        "v_accvgpr_write_b32 a189, v53\n\t"
        "v_accvgpr_write_b32 a190, v54\n\t"
        "v_accvgpr_write_b32 a191, v55\n\t"
        "s_waitcnt vmcnt(6)\n\t"
        "s_waitcnt vmcnt(4)\n\t"
        "s_waitcnt vmcnt(2)\n\t"
        "v_perm_b32 v48, v178, v176, s62\n\t"
        "v_perm_b32 v49, v182, v180, s62\n\t"
        "v_perm_b32 v50, v186, v184, s62\n\t"
        "s_waitcnt vmcnt(0)\n\t"
        "v_perm_b32 v51, v190, v188, s62\n\t"
        "v_accvgpr_write_b32 a204, v48\n\t"
        "v_accvgpr_write_b32 a205, v49\n\t"
        "v_accvgpr_write_b32 a206, v50\n\t"
        "v_accvgpr_write_b32 a207, v51\n\t"
        "v_perm_b32 v52, v178, v176, s63\n\t"
        "v_perm_b32 v53, v182, v180, s63\n\t"
        "v_perm_b32 v54, v186, v184, s63\n\t"
        "v_perm_b32 v55, v190, v188, s63\n\t"
        "v_accvgpr_write_b32 a220, v52\n\t"
        "v_accvgpr_write_b32 a221, v53\n\t"
        "v_accvgpr_write_b32 a222, v54\n\t"
        "v_accvgpr_write_b32 a223, v55\n\t"
        "v_perm_b32 v48, v179, v177, s62\n\t"
        "v_perm_b32 v49, v183, v181, s62\n\t"
        "v_perm_b32 v50, v187, v185, s62\n\t"
        "v_perm_b32 v51, v191, v189, s62\n\t"
        "v_accvgpr_write_b32 a236, v48\n\t"
        "v_accvgpr_write_b32 a237, v49\n\t"
        "v_accvgpr_write_b32 a238, v50\n\t"
        "v_accvgpr_write_b32 a239, v51\n\t"
        "v_perm_b32 v52, v179, v177, s63\n\t"
        "v_perm_b32 v53, v183, v181, s63\n\t"
        "v_perm_b32 v54, v187, v185, s63\n\t"
        "v_perm_b32 v55, v191, v189, s63\n\t"
        "v_accvgpr_write_b32 a252, v52\n\t"
        "v_accvgpr_write_b32 a253, v53\n\t"
        "v_accvgpr_write_b32 a254, v54\n\t"
        "v_accvgpr_write_b32 a255, v55\n\t"
        "s_waitcnt lgkmcnt(0)\n\t"
        "s_barrier\n\t"
        "v_mov_b32 v48, %[b0]\n\t"
        "v_mov_b32 v49, %[b1]\n\t"
        "v_mov_b32 v50, %[b2]\n\t"
        "v_mov_b32 v51, %[b3]\n\t"
        "v_mov_b32 v52, %[b4]\n\t"
        "v_mov_b32 v53, %[b5]\n\t"
        "v_mov_b32 v54, %[b6]\n\t"
        "v_mov_b32 v55, %[b7]\n\t"
        "v_mov_b32 v56, 0xc0135761\n\t"
        "v_mov_b32 v57, 0xc0135761\n\t"
        "v_mov_b32 v58, 0xbdd2d3e8\n\t"
        "v_mov_b32 v59, 0xbdd2d3e8\n\t"
        "v_mov_b32 v60, 0x3f800000\n\t"
        "v_mov_b32 v61, 0x3f800000\n\t"
        "v_and_b32 v40, 0xf0, v32\n\t"
        "v_lshrrev_b32 v41, 4, v32\n\t"
        "v_and_b32 v41, 0x30, v41\n\t"
        "v_xor_b32 v41, v41, v40\n\t"
        "v_lshl_add_u32 v40, v40, 6, v41\n\t"
        "v_xor_b32 v41, 64, v40\n\t"
        "v_xor_b32 v42, 128, v40\n\t"
        "v_xor_b32 v43, 192, v40\n\t"
        "s_lshl_b32 s60, s48, 13\n\t"
        "v_add_u32 v46, 65536, v32\n\t"
        "s_lshl_b32 s58, s48, 10\n\t"
        "v_add_u32 v47, s58, v46\n\t"
        "s_mov_b32 s57, %[gd]\n\t"
        "s_mov_b32 s58, %[gc]\n\t"
        "s_mov_b32 s50, 0\n\t"
        "Lg_%=:\n\t"
        "buffer_load_dword v34, off, s[40:43], s57 sc1\n\t"
        "s_waitcnt vmcnt(0)\n\t"
        "v_readfirstlane_b32 s59, v34\n\t"
        "s_nop 0\n\t"
        "s_xor_b32 s59, s59, s58\n\t"
        "s_cmp_lt_u32 s59, 2\n\t"
        "s_cbranch_scc1 Lgd_%=\n\t"
        "s_sleep 4\n\t"
        "s_add_u32 s50, s50, 1\n\t"
        "s_cmp_lt_u32 s50, s49\n\t"
        "s_cbranch_scc1 Lg_%=\n\t"
        "Lgd_%=:\n\t"
        "s_mov_b32 s50, 0\n\t"
        "s_and_b32 s59, s59, 1\n\t"
        "s_lshl_b32 s59, s59, 13\n\t"
        "s_xor_b32 s52, s52, s59\n\t"
        "s_xor_b32 s53, s53, s59\n\t"
        "s_xor_b32 s54, s54, s59\n\t"
        "s_xor_b32 s55, s55, s59\n\t"
        "v_mov_b32 v57, 73728\n\t"
        "v_mov_b32 v61, 0\n\t"
        "ds_write_b32 v57, v61\n\t"
        "s_waitcnt lgkmcnt(0)\n\t"
        "s_mov_b32 s61, 0\n\t"
        "v_mov_b32 v34, 0\n\t"
        "Ltask_%=:\n\t"
        "s_mov_b32 s50, 0\n\t"
        "s_waitcnt vmcnt(1)\n\t"
        "v_cmp_gt_u32 vcc, s51, v34\n\t"
        "s_cbranch_vccz Lok_%=\n\t"
        "s_cmp_eq_u32 s51, s75\n\t"
        "s_cbranch_scc1 Lp_%=\n\t"
        "s_branch Lw1_%=\n\t"
        "Lw0_%=:\n\t"
        "s_sleep 1\n\t"
        "Lw1_%=:\n\t"
        "s_memrealtime s[62:63]\n\t"
        "s_waitcnt lgkmcnt(0)\n\t"
        "s_sub_u32 s63, s74, s62\n\t"
        "s_cmp_lt_u32 s63, 600\n\t"
        "s_cbranch_scc1 Lw0_%=\n\t"
        "Lp_%=:\n\t"
        "buffer_load_dword v34, v33, s[40:43], s52 offen sc1\n\t"
        "s_waitcnt vmcnt(0)\n\t"
        "v_cmp_gt_u32 vcc, s51, v34\n\t"
        "s_cbranch_vccz Lok_%=\n\t"
        "s_add_u32 s50, s50, 1\n\t"
        "s_cmp_eq_u32 s51, s75\n\t"
        "s_cbranch_scc1 Lns_%=\n\t"
        "s_sleep 3\n\t"
        "s_cmp_lt_u32 s50, 3\n\t"
        "s_cbranch_scc1 Lp_%=\n\t"
        "s_sleep 12\n\t"
        "Lns_%=:\n\t"
        "s_cmp_lt_u32 s50, s49\n\t"
        "s_cbranch_scc1 Lp_%=\n\t"
        "s_mov_b32 s49, 0\n\t"
        "Lok_%=:\n\t"
        "s_mov_b32 m0, s60\n\t"
        "s_add_u32 s58, s53, 4096\n\t"
        "buffer_load_dwordx4 v32, s[40:43], s53 offen offset:0 sc1 lds\n\t"
        "buffer_load_dwordx4 v32, s[40:43], s53 offen offset:1024 sc1 lds\n\t"
        "buffer_load_dwordx4 v32, s[40:43], s53 offen offset:2048 sc1 lds\n\t"
        "buffer_load_dwordx4 v32, s[40:43], s53 offen offset:3072 sc1 lds\n\t"
        "s_add_u32 m0, s60, 4096\n\t"
        "s_nop 0\n\t"
        "buffer_load_dwordx4 v32, s[40:43], s58 offen offset:0 sc1 lds\n\t"
        "buffer_load_dwordx4 v32, s[40:43], s58 offen offset:1024 sc1 lds\n\t"
        "buffer_load_dwordx4 v32, s[40:43], s58 offen offset:2048 sc1 lds\n\t"
        "buffer_load_dwordx4 v32, s[40:43], s58 offen offset:3072 sc1 lds\n\t"
        "v_subrev_u32 v38, s72, v34\n\t"
        "v_subrev_u32 v39, s51, v34\n\t"
        "s_lshr_b32 s74, s73, 3\n\t"
        "v_bfe_i32 v38, v38, 0, 24\n\t"
        "v_lshrrev_b32 v39, 24, v39\n\t"
        "v_mul_u32_u24 v39, s74, v39\n\t"
        "v_sub_u32 v38, v38, v39\n\t"
        "s_nop 1\n\t"
        "v_max_i32_dpp v38, v38, v38 row_ror:1 row_mask:0xf bank_mask:0xf\n\t"
        "s_nop 1\n\t"
        "v_max_i32_dpp v38, v38, v38 row_ror:2 row_mask:0xf bank_mask:0xf\n\t"
        "s_nop 1\n\t"
        "v_max_i32_dpp v38, v38, v38 row_ror:4 row_mask:0xf bank_mask:0xf\n\t"
        "s_nop 0\n\t"
        "v_readfirstlane_b32 s62, v38\n\t"
        "s_nop 0\n\t"
        "s_add_u32 s72, s72, s62\n\t"
        "s_sub_i32 s62, s62, s74\n\t"
        "s_min_i32 s62, s62, 16\n\t"
        "s_max_i32 s62, s62, -16\n\t"
        "s_add_u32 s73, s73, s62\n\t"
        "s_add_u32 s74, s74, s72\n\t"
        "s_add_u32 s74, s74, 20\n\t"
        "s_waitcnt vmcnt(0)\n\t"
        "Lrdy_%=:\n\t"
        "s_barrier\n\t"
        "s_memrealtime s[88:89]\n\t"
        "ds_read_b128 v[192:195], v40 offset:0\n\t"
        "ds_read_b128 v[248:251], v42 offset:768\n\t"
        "ds_read_b128 v[196:199], v41 offset:0\n\t"
        "ds_read_b128 v[208:211], v40 offset:256\n\t"
        "ds_read_b128 v[244:247], v41 offset:768\n\t"
        "s_waitcnt lgkmcnt(4)\n\t"
        "v_mfma_f32_16x16x32_bf16 v[64:67], a[0:3], v[192:195], v[48:51]\n\t"
        "ds_read_b128 v[224:227], v40 offset:512\n\t"
        "s_waitcnt lgkmcnt(4)\n\t"
        "v_mfma_f32_16x16x32_bf16 v[64:67], a[56:59], v[248:251], v[64:67]\n\t"
        "s_sub_u32 s64, s51, 16777216\n\t"
        "ds_read_b128 v[228:231], v41 offset:512\n\t"
        "v_mfma_f32_16x16x32_bf16 v[68:71], a[64:67], v[192:195], v[52:55]\n\t"
        "ds_read_b128 v[236:239], v43 offset:512\n\t"
        "s_waitcnt lgkmcnt(4)\n\t"
        "v_mfma_f32_16x16x32_bf16 v[64:67], a[16:19], v[208:211], v[64:67]\n\t"
        "ds_read_b128 v[216:219], v42 offset:256\n\t"
        "v_mfma_f32_16x16x32_bf16 v[68:71], a[120:123], v[248:251], v[68:71]\n\t"
        "s_waitcnt lgkmcnt(0)\n\t"
        "s_add_u32 s88, s88, 22\n\t"
        "s_and_b32 s88, s88, 0xffffff\n\t"
        "v_mfma_f32_16x16x32_bf16 v[68:71], a[116:119], v[244:247], v[68:71]\n\t"
        "ds_read_b128 v[232:235], v42 offset:512\n\t"
        "v_mfma_f32_16x16x32_bf16 v[64:67], a[4:7], v[196:199], v[64:67]\n\t"
        "ds_read_b128 v[252:255], v43 offset:768\n\t"
        "v_mfma_f32_16x16x32_bf16 v[64:67], a[32:35], v[224:227], v[64:67]\n\t"
        "ds_read_b128 v[240:243], v40 offset:768\n\t"
        "v_mfma_f32_16x16x32_bf16 v[68:71], a[88:91], v[216:219], v[68:71]\n\t"
        "ds_read_b128 v[220:223], v43 offset:256\n\t"
        "s_waitcnt lgkmcnt(3)\n\t"
        "v_mfma_f32_16x16x32_bf16 v[64:67], a[40:43], v[232:235], v[64:67]\n\t"
        "ds_read_b128 v[212:215], v41 offset:256\n\t"
        "s_waitcnt lgkmcnt(3)\n\t"
        "v_mfma_f32_16x16x32_bf16 v[64:67], a[60:63], v[252:255], v[64:67]\n\t"
        "s_or_b32 s88, s88, s64\n\t"
        "ds_read_b128 v[128:131], v40 offset:16384\n\t"
        "ds_read_b128 v[152:155], v42 offset:16640\n\t"
        "v_mfma_f32_16x16x32_bf16 v[64:67], a[52:55], v[244:247], v[64:67]\n\t"
        "ds_read_b128 v[136:139], v42 offset:16384\n\t"
        "v_mfma_f32_16x16x32_bf16 v[64:67], a[24:27], v[216:219], v[64:67]\n\t"
        "ds_read_b128 v[204:207], v43 offset:0\n\t"
        "ds_read_b128 v[180:183], v41 offset:17152\n\t"
        "v_mfma_f32_16x16x32_bf16 v[64:67], a[44:47], v[236:239], v[64:67]\n\t"
        "ds_read_b128 v[172:175], v43 offset:16896\n\t"
        "s_waitcnt lgkmcnt(5)\n\t"
        "v_mfma_f32_16x16x32_bf16 v[72:75], a[0:3], v[128:131], v[48:51]\n\t"
        "s_add_u32 s57, s51, 16777216\n\t"
        "ds_read_b128 v[176:179], v40 offset:17152\n\t"
        "v_mfma_f32_16x16x32_bf16 v[64:67], a[36:39], v[228:231], v[64:67]\n\t"
        "ds_read_b128 v[200:203], v42 offset:0\n\t"
        "v_mov_b32 v37, s88\n\t"
        "v_mfma_f32_16x16x32_bf16 v[68:71], a[80:83], v[208:211], v[68:71]\n\t"
        "ds_read_b128 v[132:135], v41 offset:16384\n\t"
        "v_mfma_f32_16x16x32_bf16 v[64:67], a[48:51], v[240:243], v[64:67]\n\t"
        "s_waitcnt vmcnt(0)\n\t"
        "s_mov_b64 exec, 1\n\t"
        "ds_add_rtn_u32 v59, v57, v61\n\t"
        "s_mov_b64 exec, -1\n\t"
        "v_mov_b32 v61, 0\n\t"
        "v_mfma_f32_16x16x32_bf16 v[64:67], a[28:31], v[220:223], v[64:67]\n\t"
        "ds_read_b128 v[184:187], v42 offset:17152\n\t"
        "ds_read_b128 v[144:147], v40 offset:16640\n\t"
        "v_mfma_f32_16x16x32_bf16 v[64:67], a[20:23], v[212:215], v[64:67]\n\t"
        "ds_read_b128 v[164:167], v41 offset:16896\n\t"
        "s_waitcnt lgkmcnt(5)\n\t"
        "v_mfma_f32_16x16x32_bf16 v[64:67], a[8:11], v[200:203], v[64:67]\n\t"
        "s_add_u32 s67, s53, 65536\n\t"
        "ds_read_b128 v[140:143], v43 offset:16384\n\t"
        "v_mfma_f32_16x16x32_bf16 v[64:67], a[12:15], v[204:207], v[64:67]\n\t"
        "s_waitcnt lgkmcnt(4)\n\t"
        "v_readfirstlane_b32 s89, v59\n\t"
        "v_mov_b32 v61, 1\n\t"
        "v_mfma_f32_16x16x32_bf16 v[68:71], a[68:71], v[196:199], v[68:71]\n\t"
        "s_and_b32 s89, s89, s61\n\t"
        "s_cmp_eq_u32 s89, 3\n\t"
        "s_cselect_b32 s89, s55, 0x7ff00000\n\t"
        "v_mfma_f32_16x16x32_bf16 v[68:71], a[92:95], v[220:223], v[68:71]\n\t"
        "v_mul_f32 v83, v67, v67\n\t"
        "v_fma_f32 v83, v83, v58, v56\n\t"
        "v_mfma_f32_16x16x32_bf16 v[68:71], a[84:87], v[212:215], v[68:71]\n\t"
        "s_mov_b64 exec, 0xff\n\t"
        "buffer_store_dword v37, v36, s[40:43], s89 offen sc1\n\t"
        "s_mov_b64 exec, -1\n\t"
        "v_mul_f32 v80, v64, v64\n\t"
        "v_mfma_f32_16x16x32_bf16 v[68:71], a[104:107], v[232:235], v[68:71]\n\t"
        "v_mul_f32 v83, v83, v67\n\t"
        "v_mul_f32 v81, v65, v65\n\t"
        "v_fma_f32 v81, v81, v58, v56\n\t"
        "v_mfma_f32_16x16x32_bf16 v[68:71], a[96:99], v[224:227], v[68:71]\n\t"
        "v_mul_f32 v82, v66, v66\n\t"
        "v_fma_f32 v82, v82, v58, v56\n\t"
        "v_mfma_f32_16x16x32_bf16 v[72:75], a[44:47], v[172:175], v[72:75]\n\t"
        "ds_read_b128 v[148:151], v41 offset:16640\n\t"
        "v_mul_f32 v81, v81, v65\n\t"
        "v_mfma_f32_16x16x32_bf16 v[68:71], a[112:115], v[240:243], v[68:71]\n\t"
        "ds_read_b128 v[156:159], v43 offset:16640\n\t"
        "v_exp_f32 v81, v81\n\t"
        "v_mfma_f32_16x16x32_bf16 v[68:71], a[100:103], v[228:231], v[68:71]\n\t"
        "v_exp_f32 v83, v83\n\t"
        "v_mfma_f32_16x16x32_bf16 v[68:71], a[108:111], v[236:239], v[68:71]\n\t"
        "v_add_f32 v83, 1.0, v83\n\t"
        "v_rcp_f32 v83, v83\n\t"
        "v_mfma_f32_16x16x32_bf16 v[68:71], a[72:75], v[200:203], v[68:71]\n\t"
        "ds_read_b128 v[168:171], v42 offset:16896\n\t"
        "v_mul_f32 v82, v82, v66\n\t"
        "v_mfma_f32_16x16x32_bf16 v[68:71], a[76:79], v[204:207], v[68:71]\n\t"
        "ds_read_b128 v[188:191], v43 offset:17152\n\t"
        "v_xor_b32 v41, 32768, v41\n\t"
        "v_mfma_f32_16x16x32_bf16 v[76:79], a[64:67], v[128:131], v[52:55]\n\t"
        "v_xor_b32 v42, 32768, v42\n\t"
        "v_add_f32 v81, 1.0, v81\n\t"
        "v_mfma_f32_16x16x32_bf16 v[68:71], a[124:127], v[252:255], v[68:71]\n\t"
        "ds_read_b128 v[160:163], v40 offset:16896\n\t"
        "v_xor_b32 v40, 32768, v40\n\t"
        "v_mfma_f32_16x16x32_bf16 v[72:75], a[8:11], v[136:139], v[72:75]\n\t"
        "s_waitcnt lgkmcnt(3)\n\t"
        "v_fma_f32 v80, v80, v58, v56\n\t"
        "v_mul_f32 v80, v80, v64\n\t"
        "v_mfma_f32_16x16x32_bf16 v[72:75], a[28:31], v[156:159], v[72:75]\n\t"
        "s_waitcnt lgkmcnt(1)\n\t"
        "v_exp_f32 v82, v82\n\t"
        "v_mfma_f32_16x16x32_bf16 v[72:75], a[60:63], v[188:191], v[72:75]\n\t"
        "v_mul_f32 v87, v71, v71\n\t"
        "v_fma_f32 v87, v87, v58, v56\n\t"
        "v_mfma_f32_16x16x32_bf16 v[72:75], a[36:39], v[164:167], v[72:75]\n\t"
        "v_mul_f32 v85, v69, v69\n\t"
        "v_mul_f32 v87, v87, v71\n\t"
        "v_mfma_f32_16x16x32_bf16 v[72:75], a[48:51], v[176:179], v[72:75]\n\t"
        "v_rcp_f32 v81, v81\n\t"
        "v_mfma_f32_16x16x32_bf16 v[72:75], a[16:19], v[144:147], v[72:75]\n\t"
        "v_fma_f32 v85, v85, v58, v56\n\t"
        "v_exp_f32 v87, v87\n\t"
        "v_mfma_f32_16x16x32_bf16 v[72:75], a[12:15], v[140:143], v[72:75]\n\t"
        "v_mul_f32 v85, v85, v69\n\t"
        "v_mul_f32 v84, v68, v68\n\t"
        "v_exp_f32 v80, v80\n\t"
        "v_mfma_f32_16x16x32_bf16 v[72:75], a[4:7], v[132:135], v[72:75]\n\t"
        "v_mul_f32 v86, v70, v70\n\t"
        "v_add_f32 v82, 1.0, v82\n\t"
        "v_fma_f32 v84, v84, v58, v56\n\t"
        "v_mul_f32 v84, v84, v68\n\t"
        "v_mfma_f32_16x16x32_bf16 v[72:75], a[52:55], v[180:183], v[72:75]\n\t"
        "v_exp_f32 v84, v84\n\t"
        "v_mfma_f32_16x16x32_bf16 v[72:75], a[24:27], v[152:155], v[72:75]\n\t"
        "v_rcp_f32 v82, v82\n\t"
        "v_mfma_f32_16x16x32_bf16 v[72:75], a[40:43], v[168:171], v[72:75]\n\t"
        "s_waitcnt lgkmcnt(0)\n\t"
        "v_exp_f32 v85, v85\n\t"
        "v_mfma_f32_16x16x32_bf16 v[76:79], a[96:99], v[160:163], v[76:79]\n\t"
        "v_mul_f32 v81, v81, v65\n\t"
        "v_add_f32 v80, 1.0, v80\n\t"
        "v_mfma_f32_16x16x32_bf16 v[76:79], a[108:111], v[172:175], v[76:79]\n\t"
        "v_rcp_f32 v80, v80\n\t"
        "v_mfma_f32_16x16x32_bf16 v[72:75], a[20:23], v[148:151], v[72:75]\n\t"
        "v_mul_f32 v80, v80, v64\n\t"
        "v_add_f32 v85, 1.0, v85\n\t"
        "v_mfma_f32_16x16x32_bf16 v[76:79], a[120:123], v[184:187], v[76:79]\n\t"
        "v_rcp_f32 v85, v85\n\t"
        "v_add_f32 v84, 1.0, v84\n\t"
        "v_fma_f32 v86, v86, v58, v56\n\t"
        "v_rcp_f32 v84, v84\n\t"
        "v_mfma_f32_16x16x32_bf16 v[76:79], a[68:71], v[132:135], v[76:79]\n\t"
        "v_mul_f32 v84, v84, v68\n\t"
        "v_add_f32 v87, 1.0, v87\n\t"
        "v_mul_f32 v86, v86, v70\n\t"
        "v_rcp_f32 v87, v87\n\t"
        "v_mfma_f32_16x16x32_bf16 v[76:79], a[100:103], v[164:167], v[76:79]\n\t"
        "v_exp_f32 v86, v86\n\t"
        "v_mfma_f32_16x16x32_bf16 v[76:79], a[72:75], v[136:139], v[76:79]\n\t"
        "s_add_u32 s59, s54, 0\n\t"
        "v_add_f32 v86, 1.0, v86\n\t"
        "v_rcp_f32 v86, v86\n\t"
        "v_mul_f32 v87, v87, v71\n\t"
        "v_mfma_f32_16x16x32_bf16 v[72:75], a[56:59], v[184:187], v[72:75]\n\t"
        "v_mul_f32 v85, v85, v69\n\t"
        "v_mul_f32 v82, v82, v66\n\t"
        "v_mfma_f32_16x16x32_bf16 v[72:75], a[32:35], v[160:163], v[72:75]\n\t"
        "v_mul_f32 v86, v86, v70\n\t"
        "v_cvt_pk_bf16_f32 v91, v86, v87\n\t"
        "v_mul_f32 v83, v83, v67\n\t"
        "v_mfma_f32_16x16x32_bf16 v[76:79], a[104:107], v[168:171], v[76:79]\n\t"
        "v_cvt_pk_bf16_f32 v90, v84, v85\n\t"
        "v_cvt_pk_bf16_f32 v88, v80, v81\n\t"
        "v_cvt_pk_bf16_f32 v89, v82, v83\n\t"
        "v_mfma_f32_16x16x32_bf16 v[76:79], a[116:119], v[180:183], v[76:79]\n\t"
        "ds_write_b128 v47, v[88:91] offset:0\n\t"
        "v_mfma_f32_16x16x32_bf16 v[76:79], a[80:83], v[144:147], v[76:79]\n\t"
        "v_mul_f32 v82, v74, v74\n\t"
        "v_mul_f32 v80, v72, v72\n\t"
        "v_mfma_f32_16x16x32_bf16 v[76:79], a[76:79], v[140:143], v[76:79]\n\t"
        "s_xor_b32 s53, s53, s67\n\t"
        "v_mul_f32 v83, v75, v75\n\t"
        "v_fma_f32 v82, v82, v58, v56\n\t"
        "v_fma_f32 v83, v83, v58, v56\n\t"
        "v_fma_f32 v80, v80, v58, v56\n\t"
        "v_mul_f32 v80, v80, v72\n\t"
        "v_mul_f32 v82, v82, v74\n\t"
        "v_exp_f32 v82, v82\n\t"
        "v_mfma_f32_16x16x32_bf16 v[76:79], a[88:91], v[152:155], v[76:79]\n\t"
        "s_waitcnt lgkmcnt(0)\n\t"
        "s_barrier\n\t"
        "v_mfma_f32_16x16x32_bf16 v[76:79], a[92:95], v[156:159], v[76:79]\n\t"
        "ds_read_b128 v[96:99], v46 offset:0\n\t"
        "v_exp_f32 v80, v80\n\t"
        "v_mfma_f32_16x16x32_bf16 v[76:79], a[112:115], v[176:179], v[76:79]\n\t"
        "ds_read_b128 v[104:107], v46 offset:2048\n\t"
        "v_mul_f32 v81, v73, v73\n\t"
        "v_mfma_f32_16x16x32_bf16 v[76:79], a[124:127], v[188:191], v[76:79]\n\t"
        "v_mul_f32 v83, v83, v75\n\t"
        "v_exp_f32 v83, v83\n\t"
        "v_fma_f32 v81, v81, v58, v56\n\t"
        "v_mfma_f32_16x16x32_bf16 v[76:79], a[84:87], v[148:151], v[76:79]\n\t"
        "s_waitcnt lgkmcnt(1)\n\t"
        "v_mul_f32 v81, v81, v73\n\t"
        "v_add_f32 v82, 1.0, v82\n\t"
        "v_mfma_f32_16x16x32_bf16 v[156:159], v[96:99], a[240:243], 0\n\t"
        "v_exp_f32 v81, v81\n\t"
        "v_mfma_f32_16x16x32_bf16 v[140:143], v[96:99], a[176:179], 0\n\t"
        "ds_read_b128 v[108:111], v46 offset:3072\n\t"
        "v_add_f32 v81, 1.0, v81\n\t"
        "v_mfma_f32_16x16x32_bf16 v[136:139], v[96:99], a[160:163], 0\n\t"
        "v_mul_f32 v85, v77, v77\n\t"
        "v_fma_f32 v85, v85, v58, v56\n\t"
        "v_mfma_f32_16x16x32_bf16 v[152:155], v[96:99], a[224:227], 0\n\t"
        "ds_read_b128 v[100:103], v46 offset:1024\n\t"
        "v_add_f32 v83, 1.0, v83\n\t"
        "v_mfma_f32_16x16x32_bf16 v[148:151], v[96:99], a[208:211], 0\n\t"
        "s_waitcnt lgkmcnt(2)\n\t"
        "v_mul_f32 v84, v76, v76\n\t"
        "v_fma_f32 v84, v84, v58, v56\n\t"
        "v_mfma_f32_16x16x32_bf16 v[148:151], v[104:107], a[216:219], v[148:151]\n\t"
        "s_waitcnt lgkmcnt(1)\n\t"
        "v_mul_f32 v84, v84, v76\n\t"
        "v_mul_f32 v87, v79, v79\n\t"
        "v_mfma_f32_16x16x32_bf16 v[152:155], v[108:111], a[236:239], v[152:155]\n\t"
        "v_fma_f32 v87, v87, v58, v56\n\t"
        "v_mul_f32 v86, v78, v78\n\t"
        "v_mfma_f32_16x16x32_bf16 v[144:147], v[96:99], a[192:195], 0\n\t"
        "s_waitcnt lgkmcnt(0)\n\t"
        "v_rcp_f32 v82, v82\n\t"
        "v_fma_f32 v86, v86, v58, v56\n\t"
        "v_mfma_f32_16x16x32_bf16 v[136:139], v[100:103], a[164:167], v[136:139]\n\t"
        "v_add_f32 v80, 1.0, v80\n\t"
        "v_mul_f32 v85, v85, v77\n\t"
        "v_exp_f32 v84, v84\n\t"
        "v_mfma_f32_16x16x32_bf16 v[136:139], v[104:107], a[168:171], v[136:139]\n\t"
        "v_rcp_f32 v81, v81\n\t"
        "v_mfma_f32_16x16x32_bf16 v[148:151], v[100:103], a[212:215], v[148:151]\n\t"
        "v_exp_f32 v85, v85\n\t"
        "v_mfma_f32_16x16x32_bf16 v[140:143], v[104:107], a[184:187], v[140:143]\n\t"
        "v_add_f32 v85, 1.0, v85\n\t"
        "v_rcp_f32 v85, v85\n\t"
        "v_rcp_f32 v80, v80\n\t"
        "v_mul_f32 v87, v87, v79\n\t"
        "v_mfma_f32_16x16x32_bf16 v[156:159], v[100:103], a[244:247], v[156:159]\n\t"
        "v_exp_f32 v87, v87\n\t"
        "v_mfma_f32_16x16x32_bf16 v[128:131], v[96:99], a[128:131], 0\n\t"
        "v_add_f32 v87, 1.0, v87\n\t"
        "v_mul_f32 v86, v86, v78\n\t"
        "v_mfma_f32_16x16x32_bf16 v[140:143], v[100:103], a[180:183], v[140:143]\n\t"
        "v_rcp_f32 v87, v87\n\t"
        "v_exp_f32 v86, v86\n\t"
        "v_mfma_f32_16x16x32_bf16 v[132:135], v[96:99], a[144:147], 0\n\t"
        "v_add_f32 v86, 1.0, v86\n\t"
        "v_rcp_f32 v86, v86\n\t"
        "v_mfma_f32_16x16x32_bf16 v[132:135], v[104:107], a[152:155], v[132:135]\n\t"
        "v_rcp_f32 v83, v83\n\t"
        "v_mul_f32 v81, v81, v73\n\t"
        "v_mfma_f32_16x16x32_bf16 v[140:143], v[108:111], a[188:191], v[140:143]\n\t"
        "v_mul_f32 v80, v80, v72\n\t"
        "v_cvt_pk_bf16_f32 v92, v80, v81\n\t"
        "v_mfma_f32_16x16x32_bf16 v[148:151], v[108:111], a[220:223], v[148:151]\n\t"
        "v_mul_f32 v82, v82, v74\n\t"
        "v_add_f32 v84, 1.0, v84\n\t"
        "v_rcp_f32 v84, v84\n\t"
        "v_mfma_f32_16x16x32_bf16 v[136:139], v[108:111], a[172:175], v[136:139]\n\t"
        "s_memrealtime s[62:63]\n\t"
        "v_mul_f32 v85, v85, v77\n\t"
        "v_mul_f32 v86, v86, v78\n\t"
        "v_mfma_f32_16x16x32_bf16 v[128:131], v[100:103], a[132:135], v[128:131]\n\t"
        "v_mul_f32 v87, v87, v79\n\t"
        "v_mul_f32 v83, v83, v75\n\t"
        "v_cvt_pk_bf16_f32 v93, v82, v83\n\t"
        "v_mfma_f32_16x16x32_bf16 v[132:135], v[100:103], a[148:151], v[132:135]\n\t"
        "v_cvt_pk_bf16_f32 v95, v86, v87\n\t"
        "v_mul_f32 v84, v84, v76\n\t"
        "v_mfma_f32_16x16x32_bf16 v[156:159], v[104:107], a[248:251], v[156:159]\n\t"
        "s_xor_b32 s60, s60, 32768\n\t"
        "v_cvt_pk_bf16_f32 v94, v84, v85\n\t"
        "v_mfma_f32_16x16x32_bf16 v[152:155], v[104:107], a[232:235], v[152:155]\n\t"
        "s_waitcnt lgkmcnt(0)\n\t"
        "s_sub_u32 s63, s74, s62\n\t"
        "s_cmp_lt_u32 s63, 600\n\t"
        "s_cselect_b32 s62, 0x7ff00000, s52\n\t"
        "v_mfma_f32_16x16x32_bf16 v[152:155], v[100:103], a[228:231], v[152:155]\n\t"
        "ds_write_b128 v47, v[92:95] offset:4096\n\t"
        "v_mfma_f32_16x16x32_bf16 v[132:135], v[108:111], a[156:159], v[132:135]\n\t"
        "buffer_load_dword v34, v33, s[40:43], s62 offen sc1\n\t"
        "v_mfma_f32_16x16x32_bf16 v[144:147], v[108:111], a[204:207], v[144:147]\n\t"
        "s_lshr_b32 s53, s53, 8\n\t"
        "v_cvt_pk_bf16_f32 v233, v138, v142\n\t"
        "s_and_b32 s53, s53, 0x2000\n\t"
        "v_mfma_f32_16x16x32_bf16 v[144:147], v[100:103], a[196:199], v[144:147]\n\t"
        "s_waitcnt lgkmcnt(0)\n\t"
        "s_barrier\n\t"
        "v_mfma_f32_16x16x32_bf16 v[156:159], v[108:111], a[252:255], v[156:159]\n\t"
        "ds_read_b128 v[112:115], v46 offset:4096\n\t"
        "v_cvt_pk_bf16_f32 v237, v139, v143\n\t"
        "v_mfma_f32_16x16x32_bf16 v[144:147], v[104:107], a[200:203], v[144:147]\n\t"
        "v_cvt_pk_bf16_f32 v225, v136, v140\n\t"
        "s_mov_b32 s61, 0\n\t"
        "v_mfma_f32_16x16x32_bf16 v[128:131], v[104:107], a[136:139], v[128:131]\n\t"
        "ds_read_b128 v[120:123], v46 offset:6144\n\t"
        "s_xor_b32 s53, s53, s67\n\t"
        "v_mfma_f32_16x16x32_bf16 v[128:131], v[108:111], a[140:143], v[128:131]\n\t"
        "ds_read_b128 v[124:127], v46 offset:7168\n\t"
        "s_waitcnt lgkmcnt(2)\n\t"
        "v_mfma_f32_16x16x32_bf16 v[164:167], v[112:115], a[144:147], 0\n\t"
        "v_cvt_pk_bf16_f32 v226, v144, v148\n\t"
        "v_cvt_pk_bf16_f32 v227, v152, v156\n\t"
        "v_mfma_f32_16x16x32_bf16 v[172:175], v[112:115], a[176:179], 0\n\t"
        "v_cvt_pk_bf16_f32 v238, v147, v151\n\t"
        "v_cvt_pk_bf16_f32 v229, v137, v141\n\t"
        "v_mfma_f32_16x16x32_bf16 v[168:171], v[112:115], a[160:163], 0\n\t"
        "s_mov_b32 s61, 3\n\t"
        "s_waitcnt lgkmcnt(1)\n\t"
        "v_cvt_pk_bf16_f32 v236, v131, v135\n\t"
        "v_mfma_f32_16x16x32_bf16 v[172:175], v[120:123], a[184:187], v[172:175]\n\t"
        "v_cvt_pk_bf16_f32 v224, v128, v132\n\t"
        "v_cvt_pk_bf16_f32 v228, v129, v133\n\t"
        "v_mfma_f32_16x16x32_bf16 v[168:171], v[120:123], a[168:171], v[168:171]\n\t"
        "ds_read_b128 v[116:119], v46 offset:5120\n\t"
        "v_cvt_pk_bf16_f32 v235, v154, v158\n\t"
        "v_mfma_f32_16x16x32_bf16 v[184:187], v[112:115], a[224:227], 0\n\t"
        "buffer_store_dwordx4 v[224:227], v35, s[40:43], s59 offen offset:0 sc1\n\t"
        "v_mfma_f32_16x16x32_bf16 v[160:163], v[112:115], a[128:131], 0\n\t"
        "s_waitcnt lgkmcnt(0)\n\t"
        "v_cvt_pk_bf16_f32 v239, v155, v159\n\t"
        "v_mfma_f32_16x16x32_bf16 v[168:171], v[116:119], a[164:167], v[168:171]\n\t"
        "s_add_u32 s59, s54, 32768\n\t"
        "v_cvt_pk_bf16_f32 v231, v153, v157\n\t"
        "v_cvt_pk_bf16_f32 v230, v145, v149\n\t"
        "v_mfma_f32_16x16x32_bf16 v[188:191], v[112:115], a[240:243], 0\n\t"
        "buffer_store_dwordx4 v[228:231], v35, s[40:43], s59 offen offset:0 sc1\n\t"
        "v_mfma_f32_16x16x32_bf16 v[164:167], v[120:123], a[152:155], v[164:167]\n\t"
        "v_xor_b32 v43, 32768, v43\n\t"
        "s_add_u32 s59, s54, 65536\n\t"
        "v_mfma_f32_16x16x32_bf16 v[176:179], v[112:115], a[192:195], 0\n\t"
        "v_cvt_pk_bf16_f32 v232, v130, v134\n\t"
        "v_cvt_pk_bf16_f32 v234, v146, v150\n\t"
        "v_mfma_f32_16x16x32_bf16 v[176:179], v[120:123], a[200:203], v[176:179]\n\t"
        "buffer_store_dwordx4 v[232:235], v35, s[40:43], s59 offen offset:0 sc1\n\t"
        "v_mfma_f32_16x16x32_bf16 v[172:175], v[116:119], a[180:183], v[172:175]\n\t"
        "s_add_u32 s59, s54, 98304\n\t"
        "v_mfma_f32_16x16x32_bf16 v[180:183], v[112:115], a[208:211], 0\n\t"
        "buffer_store_dwordx4 v[236:239], v35, s[40:43], s59 offen offset:0 sc1\n\t"
        "v_mfma_f32_16x16x32_bf16 v[160:163], v[116:119], a[132:135], v[160:163]\n\t"
        "s_waitcnt vmcnt(4)\n\t"
        "v_cmp_gt_u32 vcc, s57, v34\n\t"
        "s_cbranch_vccnz Lna_%=\n\t"
        "v_mfma_f32_16x16x32_bf16 v[184:187], v[120:123], a[232:235], v[184:187]\n\t"
        "s_add_u32 s59, s54, 524288\n\t"
        "v_subrev_u32 v38, s72, v34\n\t"
        "v_bfe_i32 v38, v38, 0, 24\n\t"
        "v_mfma_f32_16x16x32_bf16 v[188:191], v[116:119], a[244:247], v[188:191]\n\t"
        "s_mov_b32 m0, s60\n\t"
        "v_subrev_u32 v39, s57, v34\n\t"
        "v_lshrrev_b32 v39, 24, v39\n\t"
        "v_mfma_f32_16x16x32_bf16 v[180:183], v[124:127], a[220:223], v[180:183]\n\t"
        "s_lshr_b32 s74, s73, 3\n\t"
        "v_mul_u32_u24 v39, s74, v39\n\t"
        "v_sub_u32 v38, v38, v39\n\t"
        "v_mfma_f32_16x16x32_bf16 v[164:167], v[124:127], a[156:159], v[164:167]\n\t"
        "buffer_load_dwordx4 v32, s[40:43], s53 offen offset:0 sc1 lds\n\t"
        "v_mfma_f32_16x16x32_bf16 v[160:163], v[124:127], a[140:143], v[160:163]\n\t"
        "buffer_load_dwordx4 v32, s[40:43], s53 offen offset:1024 sc1 lds\n\t"
        "v_mfma_f32_16x16x32_bf16 v[180:183], v[116:119], a[212:215], v[180:183]\n\t"
        "buffer_load_dwordx4 v32, s[40:43], s53 offen offset:2048 sc1 lds\n\t"
        "v_mfma_f32_16x16x32_bf16 v[188:191], v[124:127], a[252:255], v[188:191]\n\t"
        "buffer_load_dwordx4 v32, s[40:43], s53 offen offset:3072 sc1 lds\n\t"
        "v_mfma_f32_16x16x32_bf16 v[188:191], v[120:123], a[248:251], v[188:191]\n\t"
        "s_add_u32 s58, s53, 4096\n\t"
        "s_add_u32 m0, s60, 4096\n\t"
        "v_max_i32_dpp v38, v38, v38 row_ror:1 row_mask:0xf bank_mask:0xf\n\t"
        "v_mfma_f32_16x16x32_bf16 v[184:187], v[124:127], a[236:239], v[184:187]\n\t"
        "buffer_load_dwordx4 v32, s[40:43], s58 offen offset:0 sc1 lds\n\t"
        "v_mfma_f32_16x16x32_bf16 v[184:187], v[116:119], a[228:231], v[184:187]\n\t"
        "buffer_load_dwordx4 v32, s[40:43], s58 offen offset:1024 sc1 lds\n\t"
        "v_max_i32_dpp v38, v38, v38 row_ror:2 row_mask:0xf bank_mask:0xf\n\t"
        "v_mfma_f32_16x16x32_bf16 v[160:163], v[120:123], a[136:139], v[160:163]\n\t"
        "buffer_load_dwordx4 v32, s[40:43], s58 offen offset:2048 sc1 lds\n\t"
        "v_mfma_f32_16x16x32_bf16 v[164:167], v[116:119], a[148:151], v[164:167]\n\t"
        "buffer_load_dwordx4 v32, s[40:43], s58 offen offset:3072 sc1 lds\n\t"
        "v_max_i32_dpp v38, v38, v38 row_ror:4 row_mask:0xf bank_mask:0xf\n\t"
        "v_mfma_f32_16x16x32_bf16 v[172:175], v[124:127], a[188:191], v[172:175]\n\t"
        "v_cvt_pk_bf16_f32 v255, v187, v191\n\t"
        "v_cvt_pk_bf16_f32 v243, v184, v188\n\t"
        "v_mfma_f32_16x16x32_bf16 v[168:171], v[124:127], a[172:175], v[168:171]\n\t"
        "v_cvt_pk_bf16_f32 v251, v186, v190\n\t"
        "v_readfirstlane_b32 s62, v38\n\t"
        "v_mfma_f32_16x16x32_bf16 v[176:179], v[124:127], a[204:207], v[176:179]\n\t"
        "s_add_u32 s72, s72, s62\n\t"
        "v_cvt_pk_bf16_f32 v248, v162, v166\n\t"
        "s_sub_i32 s62, s62, s74\n\t"
        "v_mfma_f32_16x16x32_bf16 v[180:183], v[120:123], a[216:219], v[180:183]\n\t"
        "s_min_i32 s62, s62, 16\n\t"
        "v_cvt_pk_bf16_f32 v245, v169, v173\n\t"
        "v_cvt_pk_bf16_f32 v253, v171, v175\n\t"
        "v_mfma_f32_16x16x32_bf16 v[176:179], v[116:119], a[196:199], v[176:179]\n\t"
        "s_add_u32 s74, s74, s72\n\t"
        "v_cvt_pk_bf16_f32 v252, v163, v167\n\t"
        "v_cvt_pk_bf16_f32 v240, v160, v164\n\t"
        "v_cvt_pk_bf16_f32 v247, v185, v189\n\t"
        "v_cvt_pk_bf16_f32 v241, v168, v172\n\t"
        "s_max_i32 s62, s62, -16\n\t"
        "v_cvt_pk_bf16_f32 v249, v170, v174\n\t"
        "v_cvt_pk_bf16_f32 v244, v161, v165\n\t"
        "v_cvt_pk_bf16_f32 v250, v178, v182\n\t"
        "v_cvt_pk_bf16_f32 v246, v177, v181\n\t"
        "v_cvt_pk_bf16_f32 v242, v176, v180\n\t"
        "s_add_u32 s74, s74, 20\n\t"
        "buffer_store_dwordx4 v[240:243], v35, s[40:43], s59 offen offset:0 sc1\n\t"
        "s_add_u32 s59, s54, 557056\n\t"
        "buffer_store_dwordx4 v[244:247], v35, s[40:43], s59 offen offset:0 sc1\n\t"
        "s_add_u32 s59, s54, 589824\n\t"
        "v_cvt_pk_bf16_f32 v254, v179, v183\n\t"
        "s_add_u32 s73, s73, s62\n\t"
        "buffer_store_dwordx4 v[248:251], v35, s[40:43], s59 offen offset:0 sc1\n\t"
        "s_add_u32 s59, s54, 622592\n\t"
        "buffer_store_dwordx4 v[252:255], v35, s[40:43], s59 offen offset:0 sc1\n\t"
        "s_add_u32 s67, s54, 1048576\n\t"
        "s_xor_b32 s54, s54, s67\n\t"
        "s_lshr_b32 s54, s54, 8\n\t"
        "s_and_b32 s54, s54, 0x2000\n\t"
        "s_xor_b32 s54, s54, s67\n\t"
        "s_mov_b32 s51, s57\n\t"
        "s_waitcnt vmcnt(4)\n\t"
        "s_branch Lrdy_%=\n\t"
        "Lna_%=:\n\t"
        "v_mfma_f32_16x16x32_bf16 v[164:167], v[124:127], a[156:159], v[164:167]\n\t"
        "v_mfma_f32_16x16x32_bf16 v[172:175], v[124:127], a[188:191], v[172:175]\n\t"
        "v_mfma_f32_16x16x32_bf16 v[168:171], v[124:127], a[172:175], v[168:171]\n\t"
        "s_memrealtime s[62:63]\n\t"
        "v_mfma_f32_16x16x32_bf16 v[180:183], v[124:127], a[220:223], v[180:183]\n\t"
        "v_mfma_f32_16x16x32_bf16 v[180:183], v[116:119], a[212:215], v[180:183]\n\t"
        "v_mfma_f32_16x16x32_bf16 v[160:163], v[124:127], a[140:143], v[160:163]\n\t"
        "v_mfma_f32_16x16x32_bf16 v[164:167], v[116:119], a[148:151], v[164:167]\n\t"
        "v_mfma_f32_16x16x32_bf16 v[188:191], v[116:119], a[244:247], v[188:191]\n\t"
        "s_nop 1\n\t"
        "v_cvt_pk_bf16_f32 v249, v170, v174\n\t"
        "v_mfma_f32_16x16x32_bf16 v[160:163], v[120:123], a[136:139], v[160:163]\n\t"
        "s_waitcnt lgkmcnt(0)\n\t"
        "s_sub_u32 s63, s74, s62\n\t"
        "s_cmp_lt_u32 s63, 600\n\t"
        "s_cselect_b32 s62, 0x7ff00000, s52\n\t"
        "v_mfma_f32_16x16x32_bf16 v[188:191], v[124:127], a[252:255], v[188:191]\n\t"
        "buffer_load_dword v34, v33, s[40:43], s62 offen sc1\n\t"
        "v_mfma_f32_16x16x32_bf16 v[188:191], v[120:123], a[248:251], v[188:191]\n\t"
        "v_cvt_pk_bf16_f32 v245, v169, v173\n\t"
        "v_mfma_f32_16x16x32_bf16 v[180:183], v[120:123], a[216:219], v[180:183]\n\t"
        "v_cvt_pk_bf16_f32 v240, v160, v164\n\t"
        "v_mfma_f32_16x16x32_bf16 v[176:179], v[124:127], a[204:207], v[176:179]\n\t"
        "v_mfma_f32_16x16x32_bf16 v[176:179], v[116:119], a[196:199], v[176:179]\n\t"
        "s_add_u32 s59, s54, 524288\n\t"
        "v_mfma_f32_16x16x32_bf16 v[184:187], v[120:123], a[232:235], v[184:187]\n\t"
        "v_cvt_pk_bf16_f32 v253, v171, v175\n\t"
        "v_mfma_f32_16x16x32_bf16 v[184:187], v[124:127], a[236:239], v[184:187]\n\t"
        "v_cvt_pk_bf16_f32 v252, v163, v167\n\t"
        "v_cvt_pk_bf16_f32 v241, v168, v172\n\t"
        "v_mfma_f32_16x16x32_bf16 v[184:187], v[116:119], a[228:231], v[184:187]\n\t"
        "s_nop 0\n\t"
        "v_cvt_pk_bf16_f32 v242, v176, v180\n\t"
        "v_cvt_pk_bf16_f32 v254, v179, v183\n\t"
        "v_cvt_pk_bf16_f32 v250, v178, v182\n\t"
        "v_cvt_pk_bf16_f32 v244, v161, v165\n\t"
        "v_cvt_pk_bf16_f32 v246, v177, v181\n\t"
        "v_cvt_pk_bf16_f32 v248, v162, v166\n\t"
        "s_nop 0\n\t"
        "v_cvt_pk_bf16_f32 v243, v184, v188\n\t"
        "v_cvt_pk_bf16_f32 v247, v185, v189\n\t"
        "v_cvt_pk_bf16_f32 v251, v186, v190\n\t"
        "buffer_store_dwordx4 v[240:243], v35, s[40:43], s59 offen offset:0 sc1\n\t"
        "s_add_u32 s59, s54, 557056\n\t"
        "buffer_store_dwordx4 v[244:247], v35, s[40:43], s59 offen offset:0 sc1\n\t"
        "s_add_u32 s59, s54, 589824\n\t"
        "buffer_store_dwordx4 v[248:251], v35, s[40:43], s59 offen offset:0 sc1\n\t"
        "s_memrealtime s[62:63]\n\t"
        "v_cvt_pk_bf16_f32 v255, v187, v191\n\t"
        "s_add_u32 s59, s54, 622592\n\t"
        "buffer_store_dwordx4 v[252:255], v35, s[40:43], s59 offen offset:0 sc1\n\t"
        "s_waitcnt vmcnt(4)\n\t"
        "v_cmp_gt_u32 vcc, s57, v34\n\t"
        "s_cbranch_vccnz Lnpre_%=\n\t"
        "s_mov_b32 m0, s60\n\t"
        "s_add_u32 s58, s53, 4096\n\t"
        "buffer_load_dwordx4 v32, s[40:43], s53 offen offset:0 sc1 lds\n\t"
        "buffer_load_dwordx4 v32, s[40:43], s53 offen offset:1024 sc1 lds\n\t"
        "buffer_load_dwordx4 v32, s[40:43], s53 offen offset:2048 sc1 lds\n\t"
        "buffer_load_dwordx4 v32, s[40:43], s53 offen offset:3072 sc1 lds\n\t"
        "s_add_u32 m0, s60, 4096\n\t"
        "s_nop 0\n\t"
        "buffer_load_dwordx4 v32, s[40:43], s58 offen offset:0 sc1 lds\n\t"
        "buffer_load_dwordx4 v32, s[40:43], s58 offen offset:1024 sc1 lds\n\t"
        "buffer_load_dwordx4 v32, s[40:43], s58 offen offset:2048 sc1 lds\n\t"
        "buffer_load_dwordx4 v32, s[40:43], s58 offen offset:3072 sc1 lds\n\t"
        "v_subrev_u32 v38, s72, v34\n\t"
        "v_subrev_u32 v39, s57, v34\n\t"
        "s_lshr_b32 s74, s73, 3\n\t"
        "v_bfe_i32 v38, v38, 0, 24\n\t"
        "v_lshrrev_b32 v39, 24, v39\n\t"
        "v_mul_u32_u24 v39, s74, v39\n\t"
        "v_sub_u32 v38, v38, v39\n\t"
        "s_nop 1\n\t"
        "v_max_i32_dpp v38, v38, v38 row_ror:1 row_mask:0xf bank_mask:0xf\n\t"
        "s_nop 1\n\t"
        "v_max_i32_dpp v38, v38, v38 row_ror:2 row_mask:0xf bank_mask:0xf\n\t"
        "s_nop 1\n\t"
        "s_waitcnt lgkmcnt(0)\n\t"
        "v_max_i32_dpp v38, v38, v38 row_ror:4 row_mask:0xf bank_mask:0xf\n\t"
        "s_nop 0\n\t"
        "v_readfirstlane_b32 s62, v38\n\t"
        "s_nop 0\n\t"
        "s_add_u32 s72, s72, s62\n\t"
        "s_sub_i32 s62, s62, s74\n\t"
        "s_min_i32 s62, s62, 16\n\t"
        "s_max_i32 s62, s62, -16\n\t"
        "s_add_u32 s73, s73, s62\n\t"
        "s_add_u32 s74, s74, s72\n\t"
        "s_add_u32 s74, s74, 20\n\t"
        "s_add_u32 s67, s54, 1048576\n\t"
        "s_xor_b32 s54, s54, s67\n\t"
        "s_lshr_b32 s54, s54, 8\n\t"
        "s_and_b32 s54, s54, 0x2000\n\t"
        "s_xor_b32 s54, s54, s67\n\t"
        "s_waitcnt vmcnt(8)\n\t"
        "s_mov_b64 exec, 1\n\t"
        "ds_add_rtn_u32 v59, v57, v61\n\t"
        "s_mov_b64 exec, -1\n\t"
        "s_memrealtime s[88:89]\n\t"
        "s_waitcnt lgkmcnt(0)\n\t"
        "s_and_b32 s88, s88, 0xffffff\n\t"
        "s_or_b32 s88, s88, s51\n\t"
        "v_mov_b32 v37, s88\n\t"
        "v_readfirstlane_b32 s89, v59\n\t"
        "s_nop 0\n\t"
        "s_and_b32 s89, s89, s61\n\t"
        "s_cmp_eq_u32 s89, 3\n\t"
        "s_cselect_b32 s89, s55, 0x7ff00000\n\t"
        "s_mov_b64 exec, 0xff\n\t"
        "buffer_store_dword v37, v36, s[40:43], s89 offen sc1\n\t"
        "s_mov_b64 exec, -1\n\t"
        "s_mov_b32 s61, 0\n\t"
        "v_mov_b32 v61, 0\n\t"
        "s_mov_b32 s51, s57\n\t"
        "s_waitcnt vmcnt(1)\n\t"
        "s_branch Lrdy_%=\n\t"
        "Lnpre_%=:\n\t"
        "s_waitcnt lgkmcnt(0)\n\t"
        "s_sub_u32 s63, s74, s62\n\t"
        "s_cmp_lt_u32 s63, 600\n\t"
        "s_cselect_b32 s62, 0x7ff00000, s52\n\t"
        "buffer_load_dword v34, v33, s[40:43], s62 offen sc1\n\t"
        "s_add_u32 s67, s54, 1048576\n\t"
        "s_xor_b32 s54, s54, s67\n\t"
        "s_lshr_b32 s54, s54, 8\n\t"
        "s_and_b32 s54, s54, 0x2000\n\t"
        "s_xor_b32 s54, s54, s67\n\t"
        "s_waitcnt vmcnt(1)\n\t"
        "s_mov_b64 exec, 1\n\t"
        "ds_add_rtn_u32 v59, v57, v61\n\t"
        "s_mov_b64 exec, -1\n\t"
        "s_memrealtime s[88:89]\n\t"
        "s_waitcnt lgkmcnt(0)\n\t"
        "s_and_b32 s88, s88, 0xffffff\n\t"
        "s_or_b32 s88, s88, s51\n\t"
        "v_mov_b32 v37, s88\n\t"
        "v_readfirstlane_b32 s89, v59\n\t"
        "s_nop 0\n\t"
        "s_and_b32 s89, s89, s61\n\t"
        "s_cmp_eq_u32 s89, 3\n\t"
        "s_cselect_b32 s89, s55, 0x7ff00000\n\t"
        "s_mov_b64 exec, 0xff\n\t"
        "buffer_store_dword v37, v36, s[40:43], s89 offen sc1\n\t"
        "s_mov_b64 exec, -1\n\t"
        "s_mov_b32 s61, 0\n\t"
        "v_mov_b32 v61, 0\n\t"
        "s_mov_b32 s51, s57\n\t"
        "s_cmp_lt_u32 s51, 1090519040\n\t"
        "s_cbranch_scc1 Ltask_%=\n\t"
        "s_endpgm\n\t"
        :
        : [pw] "s"(ws), [w] "s"(w), [fs] "s"(l == 0 ? FS0 << 24 : FS0 << 30), [fi] "s"(O_FXM + l * 8192 + ((MUNI ? 0 : w * 16) + f) * 128), [in] "s"(O_XNM + l * 8192 + w * 16384),
          [out] "s"(O_MLPP + l * 8192 + (f + (f & 8)) * 1024 + w * 256), [fo] "s"(O_FMP + l * 8192 + (f >> 2) * 128 + (f & 3) * 4),
          [db] "s"(DBGA(2, l, f == 0 && w == 0, f, true)), [gc] "s"(gc), [gd] "s"(O_HR + id * 4), [pa] "s"(w1), [pb] "s"(w2), [wb] "s"(128 * f + 32 * w < FDIM ? f * 256 + w * 64 : OOB), [wc] "s"(f * 131072 + (w & 1) * 512 + (w >> 1) * 8),
          [ga] "v"(g * 4096 + cc * 16), [fa] "v"((lane & 1) * 4 + (MUNI ? (lane & 6) * 1024 : 0)), [sv] "v"(g * 131072 + cc * 16),
          [b0] "v"(b[0]), [b1] "v"(b[1]), [b2] "v"(b[2]), [b3] "v"(b[3]), [b4] "v"(b[4]), [b5] "v"(b[5]), [b6] "v"(b[6]), [b7] "v"(b[7])
        : "memory", "vcc", "scc", "v32", "v33", "v34", "v35", "v36", "v37", "v38", "v39", "v40", "v41", "v42", "v43", "v44", "v45", "v46", "v47", "v48", "v49", "v50", "v51", "v52", "v53", "v54", "v55", "v56", "v57", "v58", "v59", "v60", "v61", "v62", "v63", "v64", "v65", "v66", "v67", "v68", "v69", "v70", "v71", "v72", "v73", "v74", "v75", "v76", "v77", "v78", "v79", "v80", "v81", "v82", "v83", "v84", "v85", "v86", "v87", "v88", "v89", "v90", "v91", "v92", "v93", "v94", "v95", "v96", "v97", "v98", "v99", "v100", "v101", "v102", "v103", "v104", "v105", "v106", "v107", "v108", "v109", "v110", "v111", "v112", "v113", "v114", "v115", "v116", "v117", "v118", "v119", "v120", "v121", "v122", "v123", "v124", "v125", "v126", "v127", "v128", "v129", "v130", "v131", "v132", "v133", "v134", "v135", "v136", "v137", "v138", "v139", "v140", "v141", "v142", "v143", "v144", "v145", "v146", "v147", "v148", "v149", "v150", "v151", "v152", "v153", "v154", "v155", "v156", "v157", "v158", "v159", "v160", "v161", "v162", "v163", "v164", "v165", "v166", "v167", "v168", "v169", "v170", "v171", "v172", "v173", "v174", "v175", "v176", "v177", "v178", "v179", "v180", "v181", "v182", "v183", "v184", "v185", "v186", "v187", "v188", "v189", "v190", "v191", "v192", "v193", "v194", "v195", "v196", "v197", "v198", "v199", "v200", "v201", "v202", "v203", "v204", "v205", "v206", "v207", "v208", "v209", "v210", "v211", "v212", "v213", "v214", "v215", "v216", "v217", "v218", "v219", "v220", "v221", "v222", "v223", "v224", "v225", "v226", "v227", "v228", "v229", "v230", "v231", "v232", "v233", "v234", "v235", "v236", "v237", "v238", "v239", "v240", "v241", "v242", "v243", "v244", "v245", "v246", "v247", "v248", "v249", "v250", "v251", "v252", "v253", "v254", "v255", "a0", "a1", "a2", "a3", "a4", "a5", "a6", "a7", "a8", "a9", "a10", "a11", "a12", "a13", "a14", "a15", "a16", "a17", "a18", "a19", "a20", "a21", "a22", "a23", "a24", "a25", "a26", "a27", "a28", "a29", "a30", "a31", "a32", "a33", "a34", "a35", "a36", "a37", "a38", "a39", "a40", "a41", "a42", "a43", "a44", "a45", "a46", "a47", "a48", "a49", "a50", "a51", "a52", "a53", "a54", "a55", "a56", "a57", "a58", "a59", "a60", "a61", "a62", "a63", "a64", "a65", "a66", "a67", "a68", "a69", "a70", "a71", "a72", "a73", "a74", "a75", "a76", "a77", "a78", "a79", "a80", "a81", "a82", "a83", "a84", "a85", "a86", "a87", "a88", "a89", "a90", "a91", "a92", "a93", "a94", "a95", "a96", "a97", "a98", "a99", "a100", "a101", "a102", "a103", "a104", "a105", "a106", "a107", "a108", "a109", "a110", "a111", "a112", "a113", "a114", "a115", "a116", "a117", "a118", "a119", "a120", "a121", "a122", "a123", "a124", "a125", "a126", "a127", "a128", "a129", "a130", "a131", "a132", "a133", "a134", "a135", "a136", "a137", "a138", "a139", "a140", "a141", "a142", "a143", "a144", "a145", "a146", "a147", "a148", "a149", "a150", "a151", "a152", "a153", "a154", "a155", "a156", "a157", "a158", "a159", "a160", "a161", "a162", "a163", "a164", "a165", "a166", "a167", "a168", "a169", "a170", "a171", "a172", "a173", "a174", "a175", "a176", "a177", "a178", "a179", "a180", "a181", "a182", "a183", "a184", "a185", "a186", "a187", "a188", "a189", "a190", "a191", "a192", "a193", "a194", "a195", "a196", "a197", "a198", "a199", "a200", "a201", "a202", "a203", "a204", "a205", "a206", "a207", "a208", "a209", "a210", "a211", "a212", "a213", "a214", "a215", "a216", "a217", "a218", "a219", "a220", "a221", "a222", "a223", "a224", "a225", "a226", "a227", "a228", "a229", "a230", "a231", "a232", "a233", "a234", "a235", "a236", "a237", "a238", "a239", "a240", "a241", "a242", "a243", "a244", "a245", "a246", "a247", "a248", "a249", "a250", "a251", "a252", "a253", "a254", "a255", "s40", "s41", "s42", "s43", "s44", "s45", "s46", "s47", "s48", "s49", "s50", "s51", "s52", "s53", "s54", "s55", "s56", "s57", "s58", "s59", "s60", "s61", "s62", "s63", "s64", "s65", "s66", "s67", "s68", "s69", "s70", "s71", "s72", "s73", "s74", "s75", "s76", "s77", "s78", "s79", "s80", "s81", "s82", "s83", "s84", "s85", "s86", "s87", "s88", "s89");
    __builtin_unreachable();
}

#define RR_ARGS(STAGE) \
        : \
        : [pw] "s"(ws), [px] "s"(px), [w] "s"(w), [fs] "s"(l == 0 ? FS0 << 24 : FS0 << 30), [fi] "s"((STAGE == 1 ? O_FWO : O_FMP) + lp + cu * 512), [in] "s"(pin), [out] "s"(pout), [fo] "s"(pfo), \
          [db] "s"(DBGA(STAGE == 1 ? 1 : 3, l, row == 0, cu, w == 0)), [res] "s"(pres), [xo] "s"(pxo), [fa] "v"(fa), [sv] "v"((STAGE == 1 ? lane ^ (row & 15) : lane) * 16), \
          [s0] "v"((gp & 1) ? 0u : one), [s1] "v"((gp & 1) ? one : 0u), [idx] "v"((r >> 1) ? 0xEEEE : 0x4444), [lo] "v"(lo), [lol] "v"((g & 1) ? OOB : lo), \
          [b0] "v"(b[0]), [b1] "v"(b[1]), [b2] "v"(b[2]), [b3] "v"(b[3]), [b4] "v"(b[4]), [b5] "v"(b[5]), [b6] "v"(b[6]), [b7] "v"(b[7]) \
        : "memory", "vcc", "scc", "v32", "v33", "v34", "v35", "v36", "v37", "v38", "v39", "v40", "v41", "v42", "v43", "v44", "v45", "v46", "v47", "v48", "v49", "v50", "v51", "v52", "v53", "v54", "v55", "v56", "v57", "v58", "v59", "v60", "v61", "v62", "v63", "v64", "v65", "v66", "v67", "v68", "v69", "v70", "v71", "v72", "v73", "v74", "v75", "v76", "v77", "v78", "v79", "v80", "v81", "v82", "v83", "v84", "v85", "v86", "v87", "v88", "v89", "v90", "v91", "v92", "v93", "v94", "v95", "v96", "v97", "v98", "v99", "v100", "v101", "v102", "v103", "v104", "v105", "v106", "v107", "v108", "v109", "v110", "v111", "v112", "v113", "v114", "v115", "v116", "v117", "v118", "v119", "v120", "v121", "v122", "v123", "v124", "v125", "v126", "v127", "v128", "v129", "v130", "v131", "v132", "v133", "v134", "v135", "v136", "v137", "v138", "v139", "v140", "v141", "v142", "v143", "v144", "v145", "v146", "v147", "v148", "v149", "v150", "v151", "v152", "v153", "v154", "v155", "v156", "v157", "v158", "v159", "v160", "v161", "v162", "v163", "v164", "v165", "v166", "v167", "v168", "v169", "v170", "v171", "v172", "v173", "v174", "v175", "v176", "v177", "v178", "v179", "v180", "v181", "v182", "v183", "v184", "v185", "v186", "v187", "v188", "v189", "v190", "v191", "v192", "v193", "v194", "v195", "v196", "v197", "v198", "v199", "v200", "v201", "v202", "v203", "v204", "v205", "v206", "v207", "v208", "v209", "v210", "v211", "v212", "v213", "v214", "v215", "v216", "v217", "v218", "v219", "v220", "v221", "v222", "v223", "v224", "v225", "v226", "v227", "v228", "v229", "v230", "v231", "v232", "v233", "v234", "v235", "v236", "v237", "v238", "v239", "v240", "v241", "v242", "v243", "v244", "v245", "v246", "v247", "v248", "v249", "v250", "v251", "v252", "v253", "v254", "v255", "a0", "a1", "a2", "a3", "a4", "a5", "a6", "a7", "a8", "a9", "a10", "a11", "a12", "a13", "a14", "a15", "a16", "a17", "a18", "a19", "a20", "a21", "a22", "a23", "a24", "a25", "a26", "a27", "a28", "a29", "a30", "a31", "a32", "a33", "a34", "a35", "a36", "a37", "a38", "a39", "a40", "a41", "a42", "a43", "a44", "a45", "a46", "a47", "a48", "a49", "a50", "a51", "a52", "a53", "a54", "a55", "a56", "a57", "a58", "a59", "a60", "a61", "a62", "a63", "a64", "a65", "a66", "a67", "a68", "a69", "a70", "a71", "a72", "a73", "a74", "a75", "a76", "a77", "a78", "a79", "a80", "a81", "a82", "a83", "a84", "a85", "a86", "a87", "a88", "a89", "a90", "a91", "a92", "a93", "a94", "a95", "a96", "a97", "a98", "a99", "a100", "a101", "a102", "a103", "a104", "a105", "a106", "a107", "a108", "a109", "a110", "a111", "a112", "a113", "a114", "a115", "a116", "a117", "a118", "a119", "a120", "a121", "a122", "a123", "a124", "a125", "a126", "a127", "a128", "a129", "a130", "a131", "a132", "a133", "a134", "a135", "a136", "a137", "a138", "a139", "a140", "a141", "a142", "a143", "a144", "a145", "a146", "a147", "a148", "a149", "a150", "a151", "a152", "a153", "a154", "a155", "a156", "a157", "a158", "a159", "a160", "a161", "a162", "a163", "a164", "a165", "a166", "a167", "a168", "a169", "a170", "a171", "a172", "a173", "a174", "a175", "a176", "a177", "a178", "a179", "a180", "a181", "a182", "a183", "a184", "a185", "a186", "a187", "a188", "a189", "a190", "a191", "a192", "a193", "a194", "a195", "a196", "a197", "a198", "a199", "a200", "a201", "a202", "a203", "a204", "a205", "a206", "a207", "a208", "a209", "a210", "a211", "a212", "a213", "a214", "a215", "a216", "a217", "a218", "a219", "a220", "a221", "a222", "a223", "a224", "a225", "a226", "a227", "a228", "a229", "a230", "a231", "a232", "a233", "a234", "a235", "a236", "a237", "a238", "a239", "a240", "a241", "a242", "a243", "a244", "a245", "a246", "a247", "a248", "a249", "a250", "a251", "a252", "a253", "a254", "a255", "s40", "s41", "s42", "s43", "s44", "s45", "s46", "s47", "s48", "s49", "s50", "s51", "s52", "s53", "s54", "s55", "s56", "s57", "s58", "s59", "s60", "s61", "s62", "s63", "s64", "s65", "s66", "s67", "s68", "s69", "s70", "s71", "s72", "s73", "s74", "s75", "s76", "s77", "s78", "s79", "s80", "s81", "s82", "s83", "s84", "s85", "s86", "s87", "s88", "s89"

DI void rr_role(unsigned char* ws, const u16* __restrict__ x, const u16* __restrict__ b2, u16* __restrict__ out, int stage, int l, unsigned lp, int cu, int w, int lane) {
    const int cc = lane & 15, g = lane >> 4, gp = cc >> 2, r = cc & 3, row = cu * 4 + w;
    const unsigned r1 = (row >> 3) * 16384 + (row & 7) * 1024, r2 = cu * 16384 + w * 2048;
    const unsigned one = ((g & 1) == (gp >> 1)) ? ((r & 1) ? 0x3F800000u : 0x00003F80u) : 0u;
    const unsigned lo = (g & 1) * 1024 + ((g >> 1) * 16 + cc) * 16;
    float b[8];
#pragma unroll
    for (int i = 0; i < 8; ++i) b[i] = bf2f(b2[lane * 8 + i]);
    if (stage == 1) {
        const void* px = x;
        const unsigned fa = (cc >> 2) * 128 + (cc & 3) * 4, pin = O_WOP + lp + row * 16384, pout = O_XNM + lp + r1;
        const unsigned pfo = w == 0 ? O_FXM + lp + (cu >> 1) * 2048 + (cu & 1) * 4 : OOB, pxo = O_X1 + lp + r2;
        if (l == 0) {
            const unsigned pres = row * 1024;
            asm volatile(
        "s_mov_b64 s[40:41], %[pw]\n\t"
        "s_mov_b32 s42, 184549376\n\t"
        "s_mov_b32 s43, 0x20000\n\t"
        "s_mov_b32 s48, %[w]\n\t"
        "s_mov_b32 s52, %[fi]\n\t"
        "s_mov_b32 s53, %[in]\n\t"
        "s_mov_b32 s54, %[out]\n\t"
        "s_mov_b32 s55, %[fo]\n\t"
        "s_mov_b32 s56, %[db]\n\t"
        "s_mov_b32 s75, %[fs]\n\t"
        "s_memrealtime s[72:73]\n\t"
        "s_mov_b32 s49, 131072\n\t"
        "s_mov_b32 s51, 16777216\n\t"
        "s_waitcnt lgkmcnt(0)\n\t"
        "s_mov_b32 s74, s72\n\t"
        "s_mov_b32 s73, 1480\n\t"
        "v_mbcnt_lo_u32_b32 v38, -1, 0\n\t"
        "v_mbcnt_hi_u32_b32 v38, -1, v38\n\t"
        "v_lshlrev_b32 v32, 4, v38\n\t"
        "v_mul_u32_u24 v36, 128, v38\n\t"
        "v_mov_b32 v33, %[fa]\n\t"
        "v_mov_b32 v35, %[sv]\n\t"
        "s_mov_b64 s[44:45], %[px]\n\t"
        "s_mov_b32 s46, 2097152\n\t"
        "s_mov_b32 s47, 0x20000\n\t"
        "s_mov_b32 s64, %[res]\n\t"
        "s_mov_b32 s65, %[xo]\n\t"
        "v_mov_b32 v40, %[s0]\n\t"
        "v_mov_b32 v41, %[s1]\n\t"
        "v_mov_b32 v42, %[s0]\n\t"
        "v_mov_b32 v43, %[s1]\n\t"
        "v_mov_b32 v44, %[idx]\n\t"
        "v_mov_b32 v45, %[lo]\n\t"
        "v_mov_b32 v46, %[lol]\n\t"
        "v_lshrrev_b32 v47, 1, v32\n\t"
        "v_mov_b32 v34, -1\n\t"
        "s_cmp_lg_u32 s48, 0\n\t"
        "s_cbranch_scc1 Ltask_%=\n\t"
        "v_mov_b32 v34, 0\n\t"
        "Ltask_%=:\n\t"
        "buffer_load_dwordx4 v[48:51], v32, s[44:47], s64 offen\n\t"
        "s_mov_b32 s50, 0\n\t"
        "v_cmp_gt_u32 vcc, s51, v34\n\t"
        "s_cbranch_vccz Lok_%=\n\t"
        "s_cmp_eq_u32 s51, s75\n\t"
        "s_cbranch_scc1 Lp_%=\n\t"
        "s_branch Lw1_%=\n\t"
        "Lw0_%=:\n\t"
        "s_sleep 1\n\t"
        "Lw1_%=:\n\t"
        "s_memrealtime s[62:63]\n\t"
        "s_waitcnt lgkmcnt(0)\n\t"
        "s_sub_u32 s63, s74, s62\n\t"
        "s_cmp_lt_u32 s63, 600\n\t"
        "s_cbranch_scc1 Lw0_%=\n\t"
        "Lp_%=:\n\t"
        "buffer_load_dword v34, v33, s[40:43], s52 offen sc1\n\t"
        "s_waitcnt vmcnt(0)\n\t"
        "v_cmp_gt_u32 vcc, s51, v34\n\t"
        "s_cbranch_vccz Lok_%=\n\t"
        "s_add_u32 s50, s50, 1\n\t"
        "s_cmp_eq_u32 s51, s75\n\t"
        "s_cbranch_scc1 Lns_%=\n\t"
        "s_sleep 3\n\t"
        "s_cmp_lt_u32 s50, 3\n\t"
        "s_cbranch_scc1 Lp_%=\n\t"
        "s_sleep 12\n\t"
        "Lns_%=:\n\t"
        "s_cmp_lt_u32 s50, s49\n\t"
        "s_cbranch_scc1 Lp_%=\n\t"
        "s_mov_b32 s49, 0\n\t"
        "Lok_%=:\n\t"
        "s_barrier\n\t"
        "s_add_u32 s57, s53, 4096\n\t"
        "s_add_u32 s58, s53, 16384\n\t"
        "buffer_load_dwordx4 v[80:83], v45, s[40:43], s53 offen offset:0 sc1\n\t"
        "buffer_load_dwordx4 v[84:87], v45, s[40:43], s53 offen offset:2048 sc1\n\t"
        "buffer_load_dwordx4 v[88:91], v45, s[40:43], s53 offen offset:512 sc1\n\t"
        "buffer_load_dwordx4 v[92:95], v45, s[40:43], s53 offen offset:2560 sc1\n\t"
        "buffer_load_dwordx4 v[96:99], v45, s[40:43], s57 offen offset:0 sc1\n\t"
        "buffer_load_dwordx4 v[100:103], v45, s[40:43], s57 offen offset:2048 sc1\n\t"
        "buffer_load_dwordx4 v[104:107], v45, s[40:43], s57 offen offset:512 sc1\n\t"
        "buffer_load_dwordx4 v[108:111], v45, s[40:43], s57 offen offset:2560 sc1\n\t"
        "s_add_u32 s67, s53, 524288\n\t"
        "s_xor_b32 s53, s53, s67\n\t"
        "s_lshr_b32 s53, s53, 8\n\t"
        "s_and_b32 s53, s53, 0x2000\n\t"
        "s_xor_b32 s53, s53, s67\n\t"
        "v_subrev_u32 v38, s72, v34\n\t"
        "v_subrev_u32 v39, s51, v34\n\t"
        "s_lshr_b32 s74, s73, 3\n\t"
        "v_bfe_i32 v38, v38, 0, 24\n\t"
        "v_lshrrev_b32 v39, 24, v39\n\t"
        "v_mul_u32_u24 v39, s74, v39\n\t"
        "v_sub_u32 v38, v38, v39\n\t"
        "s_nop 1\n\t"
        "v_max_i32_dpp v38, v38, v38 row_ror:1 row_mask:0xf bank_mask:0xf\n\t"
        "s_nop 1\n\t"
        "v_max_i32_dpp v38, v38, v38 row_ror:2 row_mask:0xf bank_mask:0xf\n\t"
        "s_nop 1\n\t"
        "v_max_i32_dpp v38, v38, v38 row_ror:4 row_mask:0xf bank_mask:0xf\n\t"
        "s_nop 1\n\t"
        "v_max_i32_dpp v38, v38, v38 row_ror:8 row_mask:0xf bank_mask:0xf\n\t"
        "s_nop 0\n\t"
        "v_readfirstlane_b32 s62, v38\n\t"
        "s_nop 0\n\t"
        "s_add_u32 s72, s72, s62\n\t"
        "s_sub_i32 s62, s62, s74\n\t"
        "s_min_i32 s62, s62, 16\n\t"
        "s_max_i32 s62, s62, -16\n\t"
        "s_add_u32 s73, s73, s62\n\t"
        "s_add_u32 s74, s74, s72\n\t"
        "s_add_u32 s74, s74, 20\n\t"
        "s_waitcnt vmcnt(8)\n\t"
        "v_lshlrev_b32 v56, 16, v48\n\t"
        "v_and_b32 v57, 0xffff0000, v48\n\t"
        "v_lshlrev_b32 v58, 16, v49\n\t"
        "v_and_b32 v59, 0xffff0000, v49\n\t"
        "v_lshlrev_b32 v60, 16, v50\n\t"
        "v_and_b32 v61, 0xffff0000, v50\n\t"
        "v_lshlrev_b32 v62, 16, v51\n\t"
        "v_and_b32 v63, 0xffff0000, v51\n\t"
        "s_add_u32 s64, s64, 32768\n\t"
        "s_waitcnt vmcnt(6)\n\t"
        "v_smfmac_f32_16x16x64_bf16 v[56:59], v[40:43], v[80:87], v44\n\t"
        "s_waitcnt vmcnt(4)\n\t"
        "v_smfmac_f32_16x16x64_bf16 v[60:63], v[40:43], v[88:95], v44\n\t"
        "s_waitcnt vmcnt(2)\n\t"
        "v_smfmac_f32_16x16x64_bf16 v[56:59], v[40:43], v[96:103], v44\n\t"
        "s_waitcnt vmcnt(0)\n\t"
        "v_smfmac_f32_16x16x64_bf16 v[60:63], v[40:43], v[104:111], v44\n\t"
        "s_nop 5\n\t"
        "v_pk_add_f32 v[64:65], v[56:57], v[58:59]\n\t"
        "s_nop 0\n\t"
        "v_pk_add_f32 v[66:67], v[60:61], v[62:63]\n\t"
        "v_pk_add_f32 v[64:65], v[64:65], v[66:67]\n\t"
        "v_add_f32 v64, v64, v65\n\t"
        "s_nop 1\n\t"
        "v_add_f32_dpp v64, v64, v64 quad_perm:[1,0,3,2] row_mask:0xf bank_mask:0xf\n\t"
        "buffer_store_dwordx4 v[56:59], v32, s[40:43], s65 offen offset:0 sc1\n\t"
        "s_nop 0\n\t"
        "v_add_f32_dpp v64, v64, v64 quad_perm:[2,3,0,1] row_mask:0xf bank_mask:0xf\n\t"
        "buffer_store_dwordx4 v[60:63], v32, s[40:43], s65 offen offset:1024 sc1\n\t"
        "s_nop 0\n\t"
        "v_add_f32_dpp v64, v64, v64 row_half_mirror row_mask:0xf bank_mask:0xf\n\t"
        "s_add_u32 s67, s65, 131072\n\t"
        "s_nop 0\n\t"
        "v_add_f32_dpp v64, v64, v64 row_mirror row_mask:0xf bank_mask:0xf\n\t"
        "s_xor_b32 s65, s65, s67\n\t"
        "v_mov_b32 v65, v64\n\t"
        "s_lshr_b32 s65, s65, 8\n\t"
        "s_nop 0\n\t"
        "v_permlane16_swap_b32 v64, v65\n\t"
        "s_and_b32 s65, s65, 0x2000\n\t"
        "v_add_f32 v64, v64, v65\n\t"
        "s_xor_b32 s65, s65, s67\n\t"
        "v_mov_b32 v65, v64\n\t"
        "s_nop 1\n\t"
        "v_permlane32_swap_b32 v64, v65\n\t"
        "v_add_f32 v64, v64, v65\n\t"
        "v_mul_f32 v64, 0xbb000000, v64\n\t"
        "v_mov_b32 v65, v64\n\t"
        "v_pk_add_f32 v[56:57], v[56:57], v[64:65]\n\t"
        "v_pk_add_f32 v[58:59], v[58:59], v[64:65]\n\t"
        "v_pk_add_f32 v[60:61], v[60:61], v[64:65]\n\t"
        "v_pk_add_f32 v[62:63], v[62:63], v[64:65]\n\t"
        "v_pk_mul_f32 v[64:65], v[56:57], v[56:57]\n\t"
        "v_pk_fma_f32 v[64:65], v[58:59], v[58:59], v[64:65]\n\t"
        "v_pk_fma_f32 v[64:65], v[60:61], v[60:61], v[64:65]\n\t"
        "v_pk_fma_f32 v[64:65], v[62:63], v[62:63], v[64:65]\n\t"
        "v_add_f32 v64, v64, v65\n\t"
        "s_nop 1\n\t"
        "v_add_f32_dpp v64, v64, v64 quad_perm:[1,0,3,2] row_mask:0xf bank_mask:0xf\n\t"
        "v_mov_b32 v66, 0x3727c5ac\n\t"
        "s_nop 0\n\t"
        "v_add_f32_dpp v64, v64, v64 quad_perm:[2,3,0,1] row_mask:0xf bank_mask:0xf\n\t"
        "s_nop 1\n\t"
        "v_add_f32_dpp v64, v64, v64 row_half_mirror row_mask:0xf bank_mask:0xf\n\t"
        "s_nop 1\n\t"
        "v_add_f32_dpp v64, v64, v64 row_mirror row_mask:0xf bank_mask:0xf\n\t"
        "v_mov_b32 v65, v64\n\t"
        "s_nop 1\n\t"
        "v_permlane16_swap_b32 v64, v65\n\t"
        "v_add_f32 v64, v64, v65\n\t"
        "v_mov_b32 v65, v64\n\t"
        "s_nop 1\n\t"
        "v_permlane32_swap_b32 v64, v65\n\t"
        "v_add_f32 v64, v64, v65\n\t"
        "v_fmac_f32 v66, 0x3b000000, v64\n\t"
        "v_rsq_f32 v64, v66\n\t"
        "s_nop 0\n\t"
        "v_mov_b32 v65, v64\n\t"
        "v_pk_mul_f32 v[56:57], v[56:57], v[64:65]\n\t"
        "v_pk_mul_f32 v[58:59], v[58:59], v[64:65]\n\t"
        "v_pk_mul_f32 v[60:61], v[60:61], v[64:65]\n\t"
        "v_pk_mul_f32 v[62:63], v[62:63], v[64:65]\n\t"
        "v_cvt_pk_bf16_f32 v72, v56, v57\n\t"
        "v_cvt_pk_bf16_f32 v73, v58, v59\n\t"
        "v_cvt_pk_bf16_f32 v74, v60, v61\n\t"
        "v_cvt_pk_bf16_f32 v75, v62, v63\n\t"
        "buffer_store_dwordx4 v[72:75], v35, s[40:43], s54 offen offset:0 sc1\n\t"
        "s_add_u32 s67, s54, 65536\n\t"
        "s_xor_b32 s54, s54, s67\n\t"
        "s_lshr_b32 s54, s54, 8\n\t"
        "s_and_b32 s54, s54, 0x2000\n\t"
        "s_xor_b32 s54, s54, s67\n\t"
        "s_waitcnt vmcnt(0)\n\t"
        "s_memrealtime s[88:89]\n\t"
        "s_barrier\n\t"
        "s_waitcnt lgkmcnt(0)\n\t"
        "s_and_b32 s88, s88, 0xffffff\n\t"
        "s_or_b32 s88, s88, s51\n\t"
        "v_mov_b32 v37, s88\n\t"
        "s_mov_b64 exec, 0x7ff\n\t"
        "buffer_store_dword v37, v36, s[40:43], s55 offen offset:0 sc1\n\t"
        "s_mov_b64 exec, -1\n\t"
        "s_add_u32 s51, s51, 16777216\n\t"
        "s_cmp_lt_u32 s51, 1090519040\n\t"
        "s_cbranch_scc1 Ltask_%=\n\t"
        "s_endpgm\n\t"
            RR_ARGS(1));
            __builtin_unreachable();
        } else {
            const unsigned pres = O_X2 + lp + r2;
            asm volatile(
        "s_mov_b64 s[40:41], %[pw]\n\t"
        "s_mov_b32 s42, 184549376\n\t"
        "s_mov_b32 s43, 0x20000\n\t"
        "s_mov_b32 s48, %[w]\n\t"
        "s_mov_b32 s52, %[fi]\n\t"
        "s_mov_b32 s53, %[in]\n\t"
        "s_mov_b32 s54, %[out]\n\t"
        "s_mov_b32 s55, %[fo]\n\t"
        "s_mov_b32 s56, %[db]\n\t"
        "s_mov_b32 s75, %[fs]\n\t"
        "s_memrealtime s[72:73]\n\t"
        "s_mov_b32 s49, 131072\n\t"
        "s_mov_b32 s51, 16777216\n\t"
        "s_waitcnt lgkmcnt(0)\n\t"
        "s_mov_b32 s74, s72\n\t"
        "s_mov_b32 s73, 1480\n\t"
        "v_mbcnt_lo_u32_b32 v38, -1, 0\n\t"
        "v_mbcnt_hi_u32_b32 v38, -1, v38\n\t"
        "v_lshlrev_b32 v32, 4, v38\n\t"
        "v_mul_u32_u24 v36, 128, v38\n\t"
        "v_mov_b32 v33, %[fa]\n\t"
        "v_mov_b32 v35, %[sv]\n\t"
        "s_mov_b64 s[44:45], %[px]\n\t"
        "s_mov_b32 s46, 2097152\n\t"
        "s_mov_b32 s47, 0x20000\n\t"
        "s_mov_b32 s64, %[res]\n\t"
        "s_mov_b32 s65, %[xo]\n\t"
        "v_mov_b32 v40, %[s0]\n\t"
        "v_mov_b32 v41, %[s1]\n\t"
        "v_mov_b32 v42, %[s0]\n\t"
        "v_mov_b32 v43, %[s1]\n\t"
        "v_mov_b32 v44, %[idx]\n\t"
        "v_mov_b32 v45, %[lo]\n\t"
        "v_mov_b32 v46, %[lol]\n\t"
        "v_lshrrev_b32 v47, 1, v32\n\t"
        "v_mov_b32 v34, -1\n\t"
        "s_cmp_lg_u32 s48, 0\n\t"
        "s_cbranch_scc1 Ltask_%=\n\t"
        "v_mov_b32 v34, 0\n\t"
        "Ltask_%=:\n\t"
        "s_mov_b32 s50, 0\n\t"
        "v_cmp_gt_u32 vcc, s51, v34\n\t"
        "s_cbranch_vccz Lok_%=\n\t"
        "s_cmp_eq_u32 s51, s75\n\t"
        "s_cbranch_scc1 Lp_%=\n\t"
        "s_branch Lw1_%=\n\t"
        "Lw0_%=:\n\t"
        "s_sleep 1\n\t"
        "Lw1_%=:\n\t"
        "s_memrealtime s[62:63]\n\t"
        "s_waitcnt lgkmcnt(0)\n\t"
        "s_sub_u32 s63, s74, s62\n\t"
        "s_cmp_lt_u32 s63, 600\n\t"
        "s_cbranch_scc1 Lw0_%=\n\t"
        "Lp_%=:\n\t"
        "buffer_load_dword v34, v33, s[40:43], s52 offen sc1\n\t"
        "s_waitcnt vmcnt(0)\n\t"
        "v_cmp_gt_u32 vcc, s51, v34\n\t"
        "s_cbranch_vccz Lok_%=\n\t"
        "s_add_u32 s50, s50, 1\n\t"
        "s_cmp_eq_u32 s51, s75\n\t"
        "s_cbranch_scc1 Lns_%=\n\t"
        "s_sleep 3\n\t"
        "s_cmp_lt_u32 s50, 3\n\t"
        "s_cbranch_scc1 Lp_%=\n\t"
        "s_sleep 12\n\t"
        "Lns_%=:\n\t"
        "s_cmp_lt_u32 s50, s49\n\t"
        "s_cbranch_scc1 Lp_%=\n\t"
        "s_mov_b32 s49, 0\n\t"
        "Lok_%=:\n\t"
        "s_barrier\n\t"
        "buffer_load_dwordx4 v[56:59], v32, s[40:43], s64 offen offset:0 sc1\n\t"
        "buffer_load_dwordx4 v[60:63], v32, s[40:43], s64 offen offset:1024 sc1\n\t"
        "s_add_u32 s57, s53, 4096\n\t"
        "s_add_u32 s58, s53, 16384\n\t"
        "buffer_load_dwordx4 v[80:83], v45, s[40:43], s53 offen offset:0 sc1\n\t"
        "buffer_load_dwordx4 v[84:87], v45, s[40:43], s53 offen offset:2048 sc1\n\t"
        "buffer_load_dwordx4 v[88:91], v45, s[40:43], s53 offen offset:512 sc1\n\t"
        "buffer_load_dwordx4 v[92:95], v45, s[40:43], s53 offen offset:2560 sc1\n\t"
        "buffer_load_dwordx4 v[96:99], v45, s[40:43], s57 offen offset:0 sc1\n\t"
        "buffer_load_dwordx4 v[100:103], v45, s[40:43], s57 offen offset:2048 sc1\n\t"
        "buffer_load_dwordx4 v[104:107], v45, s[40:43], s57 offen offset:512 sc1\n\t"
        "buffer_load_dwordx4 v[108:111], v45, s[40:43], s57 offen offset:2560 sc1\n\t"
        "s_add_u32 s67, s53, 524288\n\t"
        "s_xor_b32 s53, s53, s67\n\t"
        "s_lshr_b32 s53, s53, 8\n\t"
        "s_and_b32 s53, s53, 0x2000\n\t"
        "s_xor_b32 s53, s53, s67\n\t"
        "v_subrev_u32 v38, s72, v34\n\t"
        "v_subrev_u32 v39, s51, v34\n\t"
        "s_lshr_b32 s74, s73, 3\n\t"
        "v_bfe_i32 v38, v38, 0, 24\n\t"
        "v_lshrrev_b32 v39, 24, v39\n\t"
        "v_mul_u32_u24 v39, s74, v39\n\t"
        "v_sub_u32 v38, v38, v39\n\t"
        "s_nop 1\n\t"
        "v_max_i32_dpp v38, v38, v38 row_ror:1 row_mask:0xf bank_mask:0xf\n\t"
        "s_nop 1\n\t"
        "v_max_i32_dpp v38, v38, v38 row_ror:2 row_mask:0xf bank_mask:0xf\n\t"
        "s_nop 1\n\t"
        "v_max_i32_dpp v38, v38, v38 row_ror:4 row_mask:0xf bank_mask:0xf\n\t"
        "s_nop 1\n\t"
        "v_max_i32_dpp v38, v38, v38 row_ror:8 row_mask:0xf bank_mask:0xf\n\t"
        "s_nop 0\n\t"
        "v_readfirstlane_b32 s62, v38\n\t"
        "s_nop 0\n\t"
        "s_add_u32 s72, s72, s62\n\t"
        "s_sub_i32 s62, s62, s74\n\t"
        "s_min_i32 s62, s62, 16\n\t"
        "s_max_i32 s62, s62, -16\n\t"
        "s_add_u32 s73, s73, s62\n\t"
        "s_add_u32 s74, s74, s72\n\t"
        "s_add_u32 s74, s74, 20\n\t"
        "s_add_u32 s67, s64, 131072\n\t"
        "s_xor_b32 s64, s64, s67\n\t"
        "s_lshr_b32 s64, s64, 8\n\t"
        "s_and_b32 s64, s64, 0x2000\n\t"
        "s_xor_b32 s64, s64, s67\n\t"
        "s_waitcnt vmcnt(6)\n\t"
        "v_smfmac_f32_16x16x64_bf16 v[56:59], v[40:43], v[80:87], v44\n\t"
        "s_waitcnt vmcnt(4)\n\t"
        "v_smfmac_f32_16x16x64_bf16 v[60:63], v[40:43], v[88:95], v44\n\t"
        "s_waitcnt vmcnt(2)\n\t"
        "v_smfmac_f32_16x16x64_bf16 v[56:59], v[40:43], v[96:103], v44\n\t"
        "s_waitcnt vmcnt(0)\n\t"
        "v_smfmac_f32_16x16x64_bf16 v[60:63], v[40:43], v[104:111], v44\n\t"
        "s_nop 5\n\t"
        "v_pk_add_f32 v[64:65], v[56:57], v[58:59]\n\t"
        "s_nop 0\n\t"
        "v_pk_add_f32 v[66:67], v[60:61], v[62:63]\n\t"
        "v_pk_add_f32 v[64:65], v[64:65], v[66:67]\n\t"
        "v_add_f32 v64, v64, v65\n\t"
        "s_nop 1\n\t"
        "v_add_f32_dpp v64, v64, v64 quad_perm:[1,0,3,2] row_mask:0xf bank_mask:0xf\n\t"
        "buffer_store_dwordx4 v[56:59], v32, s[40:43], s65 offen offset:0 sc1\n\t"
        "s_nop 0\n\t"
        "v_add_f32_dpp v64, v64, v64 quad_perm:[2,3,0,1] row_mask:0xf bank_mask:0xf\n\t"
        "buffer_store_dwordx4 v[60:63], v32, s[40:43], s65 offen offset:1024 sc1\n\t"
        "s_nop 0\n\t"
        "v_add_f32_dpp v64, v64, v64 row_half_mirror row_mask:0xf bank_mask:0xf\n\t"
        "s_add_u32 s67, s65, 131072\n\t"
        "s_nop 0\n\t"
        "v_add_f32_dpp v64, v64, v64 row_mirror row_mask:0xf bank_mask:0xf\n\t"
        "s_xor_b32 s65, s65, s67\n\t"
        "v_mov_b32 v65, v64\n\t"
        "s_lshr_b32 s65, s65, 8\n\t"
        "s_nop 0\n\t"
        "v_permlane16_swap_b32 v64, v65\n\t"
        "s_and_b32 s65, s65, 0x2000\n\t"
        "v_add_f32 v64, v64, v65\n\t"
        "s_xor_b32 s65, s65, s67\n\t"
        "v_mov_b32 v65, v64\n\t"
        "s_nop 1\n\t"
        "v_permlane32_swap_b32 v64, v65\n\t"
        "v_add_f32 v64, v64, v65\n\t"
        "v_mul_f32 v64, 0xbb000000, v64\n\t"
        "v_mov_b32 v65, v64\n\t"
        "v_pk_add_f32 v[56:57], v[56:57], v[64:65]\n\t"
        "v_pk_add_f32 v[58:59], v[58:59], v[64:65]\n\t"
        "v_pk_add_f32 v[60:61], v[60:61], v[64:65]\n\t"
        "v_pk_add_f32 v[62:63], v[62:63], v[64:65]\n\t"
        "v_pk_mul_f32 v[64:65], v[56:57], v[56:57]\n\t"
        "v_pk_fma_f32 v[64:65], v[58:59], v[58:59], v[64:65]\n\t"
        "v_pk_fma_f32 v[64:65], v[60:61], v[60:61], v[64:65]\n\t"
        "v_pk_fma_f32 v[64:65], v[62:63], v[62:63], v[64:65]\n\t"
        "v_add_f32 v64, v64, v65\n\t"
        "s_nop 1\n\t"
        "v_add_f32_dpp v64, v64, v64 quad_perm:[1,0,3,2] row_mask:0xf bank_mask:0xf\n\t"
        "v_mov_b32 v66, 0x3727c5ac\n\t"
        "s_nop 0\n\t"
        "v_add_f32_dpp v64, v64, v64 quad_perm:[2,3,0,1] row_mask:0xf bank_mask:0xf\n\t"
        "s_nop 1\n\t"
        "v_add_f32_dpp v64, v64, v64 row_half_mirror row_mask:0xf bank_mask:0xf\n\t"
        "s_nop 1\n\t"
        "v_add_f32_dpp v64, v64, v64 row_mirror row_mask:0xf bank_mask:0xf\n\t"
        "v_mov_b32 v65, v64\n\t"
        "s_nop 1\n\t"
        "v_permlane16_swap_b32 v64, v65\n\t"
        "v_add_f32 v64, v64, v65\n\t"
        "v_mov_b32 v65, v64\n\t"
        "s_nop 1\n\t"
        "v_permlane32_swap_b32 v64, v65\n\t"
        "v_add_f32 v64, v64, v65\n\t"
        "v_fmac_f32 v66, 0x3b000000, v64\n\t"
        "v_rsq_f32 v64, v66\n\t"
        "s_nop 0\n\t"
        "v_mov_b32 v65, v64\n\t"
        "v_pk_mul_f32 v[56:57], v[56:57], v[64:65]\n\t"
        "v_pk_mul_f32 v[58:59], v[58:59], v[64:65]\n\t"
        "v_pk_mul_f32 v[60:61], v[60:61], v[64:65]\n\t"
        "v_pk_mul_f32 v[62:63], v[62:63], v[64:65]\n\t"
        "v_cvt_pk_bf16_f32 v72, v56, v57\n\t"
        "v_cvt_pk_bf16_f32 v73, v58, v59\n\t"
        "v_cvt_pk_bf16_f32 v74, v60, v61\n\t"
        "v_cvt_pk_bf16_f32 v75, v62, v63\n\t"
        "buffer_store_dwordx4 v[72:75], v35, s[40:43], s54 offen offset:0 sc1\n\t"
        "s_add_u32 s67, s54, 65536\n\t"
        "s_xor_b32 s54, s54, s67\n\t"
        "s_lshr_b32 s54, s54, 8\n\t"
        "s_and_b32 s54, s54, 0x2000\n\t"
        "s_xor_b32 s54, s54, s67\n\t"
        "s_waitcnt vmcnt(0)\n\t"
        "s_memrealtime s[88:89]\n\t"
        "s_barrier\n\t"
        "s_waitcnt lgkmcnt(0)\n\t"
        "s_and_b32 s88, s88, 0xffffff\n\t"
        "s_or_b32 s88, s88, s51\n\t"
        "v_mov_b32 v37, s88\n\t"
        "s_mov_b64 exec, 0x7ff\n\t"
        "buffer_store_dword v37, v36, s[40:43], s55 offen offset:0 sc1\n\t"
        "s_mov_b64 exec, -1\n\t"
        "s_add_u32 s51, s51, 16777216\n\t"
        "s_cmp_lt_u32 s51, 1090519040\n\t"
        "s_cbranch_scc1 Ltask_%=\n\t"
        "s_endpgm\n\t"
            RR_ARGS(1));
            __builtin_unreachable();
        }
    } else {
        const void* px = out;
        const int fl = cc < NF ? cc : 0;
        const unsigned fa = (fl >> 2) * 128 + (fl & 3) * 4, pin = O_MLPP + lp + row * 32768, pres = O_X1 + lp + r2, pxo = O_X2 + (lp ^ 8192) + r2;
        if (l == 0) {
            const unsigned pout = O_XNA + (lp ^ 8192) + (cu & 3) * 16384 + (w * 2 + (cu >> 2)) * 1024, pfo = w == 0 ? O_FXA + 32768 + (lp ^ 8192) + (cu >> 2) * 16384 + (cu & 2) * 2048 + (cu & 1) * 16 : OOB;
            asm volatile(
        "s_mov_b64 s[40:41], %[pw]\n\t"
        "s_mov_b32 s42, 184549376\n\t"
        "s_mov_b32 s43, 0x20000\n\t"
        "s_mov_b32 s48, %[w]\n\t"
        "s_mov_b32 s52, %[fi]\n\t"
        "s_mov_b32 s53, %[in]\n\t"
        "s_mov_b32 s54, %[out]\n\t"
        "s_mov_b32 s55, %[fo]\n\t"
        "s_mov_b32 s56, %[db]\n\t"
        "s_mov_b32 s75, %[fs]\n\t"
        "s_memrealtime s[72:73]\n\t"
        "s_mov_b32 s49, 131072\n\t"
        "s_mov_b32 s51, 16777216\n\t"
        "s_waitcnt lgkmcnt(0)\n\t"
        "s_mov_b32 s74, s72\n\t"
        "s_mov_b32 s73, 1480\n\t"
        "v_mbcnt_lo_u32_b32 v38, -1, 0\n\t"
        "v_mbcnt_hi_u32_b32 v38, -1, v38\n\t"
        "v_lshlrev_b32 v32, 4, v38\n\t"
        "v_mul_u32_u24 v36, 128, v38\n\t"
        "v_mov_b32 v33, %[fa]\n\t"
        "v_mov_b32 v35, %[sv]\n\t"
        "s_mov_b64 s[44:45], %[px]\n\t"
        "s_mov_b32 s46, 2097152\n\t"
        "s_mov_b32 s47, 0x20000\n\t"
        "s_mov_b32 s64, %[res]\n\t"
        "s_mov_b32 s65, %[xo]\n\t"
        "v_mov_b32 v40, %[s0]\n\t"
        "v_mov_b32 v41, %[s1]\n\t"
        "v_mov_b32 v42, %[s0]\n\t"
        "v_mov_b32 v43, %[s1]\n\t"
        "v_mov_b32 v44, %[idx]\n\t"
        "v_mov_b32 v45, %[lo]\n\t"
        "v_mov_b32 v46, %[lol]\n\t"
        "v_lshrrev_b32 v47, 1, v32\n\t"
        "v_mov_b32 v48, %[b0]\n\t"
        "v_mov_b32 v49, %[b1]\n\t"
        "v_mov_b32 v50, %[b2]\n\t"
        "v_mov_b32 v51, %[b3]\n\t"
        "v_mov_b32 v52, %[b4]\n\t"
        "v_mov_b32 v53, %[b5]\n\t"
        "v_mov_b32 v54, %[b6]\n\t"
        "v_mov_b32 v55, %[b7]\n\t"
        "v_mov_b32 v34, -1\n\t"
        "s_cmp_lg_u32 s48, 0\n\t"
        "s_cbranch_scc1 Ltask_%=\n\t"
        "v_mov_b32 v34, 0\n\t"
        "Ltask_%=:\n\t"
        "s_mov_b32 s50, 0\n\t"
        "v_cmp_gt_u32 vcc, s51, v34\n\t"
        "s_cbranch_vccz Lok_%=\n\t"
        "s_cmp_eq_u32 s51, s75\n\t"
        "s_cbranch_scc1 Lp_%=\n\t"
        "s_branch Lw1_%=\n\t"
        "Lw0_%=:\n\t"
        "s_sleep 1\n\t"
        "Lw1_%=:\n\t"
        "s_memrealtime s[62:63]\n\t"
        "s_waitcnt lgkmcnt(0)\n\t"
        "s_sub_u32 s63, s74, s62\n\t"
        "s_cmp_lt_u32 s63, 600\n\t"
        "s_cbranch_scc1 Lw0_%=\n\t"
        "Lp_%=:\n\t"
        "buffer_load_dword v34, v33, s[40:43], s52 offen sc1\n\t"
        "s_waitcnt vmcnt(0)\n\t"
        "v_cmp_gt_u32 vcc, s51, v34\n\t"
        "s_cbranch_vccz Lok_%=\n\t"
        "s_add_u32 s50, s50, 1\n\t"
        "s_cmp_eq_u32 s51, s75\n\t"
        "s_cbranch_scc1 Lns_%=\n\t"
        "s_sleep 3\n\t"
        "s_cmp_lt_u32 s50, 3\n\t"
        "s_cbranch_scc1 Lp_%=\n\t"
        "s_sleep 12\n\t"
        "Lns_%=:\n\t"
        "s_cmp_lt_u32 s50, s49\n\t"
        "s_cbranch_scc1 Lp_%=\n\t"
        "s_mov_b32 s49, 0\n\t"
        "Lok_%=:\n\t"
        "s_barrier\n\t"
        "buffer_load_dwordx4 v[56:59], v32, s[40:43], s64 offen offset:0 sc1\n\t"
        "buffer_load_dwordx4 v[60:63], v32, s[40:43], s64 offen offset:1024 sc1\n\t"
        "s_add_u32 s57, s53, 4096\n\t"
        "s_add_u32 s58, s53, 16384\n\t"
        "buffer_load_dwordx4 v[80:83], v45, s[40:43], s53 offen offset:0 sc1\n\t"
        "buffer_load_dwordx4 v[84:87], v45, s[40:43], s53 offen offset:2048 sc1\n\t"
        "buffer_load_dwordx4 v[88:91], v45, s[40:43], s53 offen offset:512 sc1\n\t"
        "buffer_load_dwordx4 v[92:95], v45, s[40:43], s53 offen offset:2560 sc1\n\t"
        "buffer_load_dwordx4 v[96:99], v45, s[40:43], s57 offen offset:0 sc1\n\t"
        "buffer_load_dwordx4 v[100:103], v45, s[40:43], s57 offen offset:2048 sc1\n\t"
        "buffer_load_dwordx4 v[104:107], v45, s[40:43], s57 offen offset:512 sc1\n\t"
        "buffer_load_dwordx4 v[108:111], v45, s[40:43], s57 offen offset:2560 sc1\n\t"
        "buffer_load_dwordx4 v[112:115], v45, s[40:43], s58 offen offset:0 sc1\n\t"
        "buffer_load_dwordx4 v[116:119], v46, s[40:43], s58 offen offset:2048 sc1\n\t"
        "buffer_load_dwordx4 v[120:123], v45, s[40:43], s58 offen offset:512 sc1\n\t"
        "buffer_load_dwordx4 v[124:127], v46, s[40:43], s58 offen offset:2560 sc1\n\t"
        "s_add_u32 s67, s53, 1048576\n\t"
        "s_xor_b32 s53, s53, s67\n\t"
        "s_lshr_b32 s53, s53, 8\n\t"
        "s_and_b32 s53, s53, 0x2000\n\t"
        "s_xor_b32 s53, s53, s67\n\t"
        "v_subrev_u32 v38, s72, v34\n\t"
        "v_subrev_u32 v39, s51, v34\n\t"
        "s_lshr_b32 s74, s73, 3\n\t"
        "v_bfe_i32 v38, v38, 0, 24\n\t"
        "v_lshrrev_b32 v39, 24, v39\n\t"
        "v_mul_u32_u24 v39, s74, v39\n\t"
        "v_sub_u32 v38, v38, v39\n\t"
        "s_nop 1\n\t"
        "v_max_i32_dpp v38, v38, v38 row_ror:1 row_mask:0xf bank_mask:0xf\n\t"
        "s_nop 1\n\t"
        "v_max_i32_dpp v38, v38, v38 row_ror:2 row_mask:0xf bank_mask:0xf\n\t"
        "s_nop 1\n\t"
        "v_max_i32_dpp v38, v38, v38 row_ror:4 row_mask:0xf bank_mask:0xf\n\t"
        "s_nop 1\n\t"
        "v_max_i32_dpp v38, v38, v38 row_ror:8 row_mask:0xf bank_mask:0xf\n\t"
        "s_nop 0\n\t"
        "v_readfirstlane_b32 s62, v38\n\t"
        "s_nop 0\n\t"
        "s_add_u32 s72, s72, s62\n\t"
        "s_sub_i32 s62, s62, s74\n\t"
        "s_min_i32 s62, s62, 16\n\t"
        "s_max_i32 s62, s62, -16\n\t"
        "s_add_u32 s73, s73, s62\n\t"
        "s_add_u32 s74, s74, s72\n\t"
        "s_add_u32 s74, s74, 20\n\t"
        "s_add_u32 s67, s64, 131072\n\t"
        "s_xor_b32 s64, s64, s67\n\t"
        "s_lshr_b32 s64, s64, 8\n\t"
        "s_and_b32 s64, s64, 0x2000\n\t"
        "s_xor_b32 s64, s64, s67\n\t"
        "s_waitcnt vmcnt(13)\n\t"
        "s_waitcnt vmcnt(12)\n\t"
        "v_pk_add_f32 v[56:57], v[56:57], v[48:49]\n\t"
        "v_pk_add_f32 v[58:59], v[58:59], v[50:51]\n\t"
        "v_pk_add_f32 v[60:61], v[60:61], v[52:53]\n\t"
        "v_pk_add_f32 v[62:63], v[62:63], v[54:55]\n\t"
        "s_waitcnt vmcnt(10)\n\t"
        "v_smfmac_f32_16x16x64_bf16 v[56:59], v[40:43], v[80:87], v44\n\t"
        "s_waitcnt vmcnt(8)\n\t"
        "v_smfmac_f32_16x16x64_bf16 v[60:63], v[40:43], v[88:95], v44\n\t"
        "s_waitcnt vmcnt(6)\n\t"
        "v_smfmac_f32_16x16x64_bf16 v[56:59], v[40:43], v[96:103], v44\n\t"
        "s_waitcnt vmcnt(4)\n\t"
        "v_smfmac_f32_16x16x64_bf16 v[60:63], v[40:43], v[104:111], v44\n\t"
        "s_waitcnt vmcnt(2)\n\t"
        "v_smfmac_f32_16x16x64_bf16 v[56:59], v[40:43], v[112:119], v44\n\t"
        "s_waitcnt vmcnt(0)\n\t"
        "v_smfmac_f32_16x16x64_bf16 v[60:63], v[40:43], v[120:127], v44\n\t"
        "s_nop 5\n\t"
        "v_pk_add_f32 v[64:65], v[56:57], v[58:59]\n\t"
        "s_nop 0\n\t"
        "v_pk_add_f32 v[66:67], v[60:61], v[62:63]\n\t"
        "v_pk_add_f32 v[64:65], v[64:65], v[66:67]\n\t"
        "v_add_f32 v64, v64, v65\n\t"
        "s_nop 1\n\t"
        "v_add_f32_dpp v64, v64, v64 quad_perm:[1,0,3,2] row_mask:0xf bank_mask:0xf\n\t"
        "buffer_store_dwordx4 v[56:59], v32, s[40:43], s65 offen offset:0 sc1\n\t"
        "s_nop 0\n\t"
        "v_add_f32_dpp v64, v64, v64 quad_perm:[2,3,0,1] row_mask:0xf bank_mask:0xf\n\t"
        "buffer_store_dwordx4 v[60:63], v32, s[40:43], s65 offen offset:1024 sc1\n\t"
        "s_nop 0\n\t"
        "v_add_f32_dpp v64, v64, v64 row_half_mirror row_mask:0xf bank_mask:0xf\n\t"
        "s_add_u32 s67, s65, 131072\n\t"
        "s_nop 0\n\t"
        "v_add_f32_dpp v64, v64, v64 row_mirror row_mask:0xf bank_mask:0xf\n\t"
        "s_xor_b32 s65, s65, s67\n\t"
        "v_mov_b32 v65, v64\n\t"
        "s_lshr_b32 s65, s65, 8\n\t"
        "s_nop 0\n\t"
        "v_permlane16_swap_b32 v64, v65\n\t"
        "s_and_b32 s65, s65, 0x2000\n\t"
        "v_add_f32 v64, v64, v65\n\t"
        "s_xor_b32 s65, s65, s67\n\t"
        "v_mov_b32 v65, v64\n\t"
        "s_nop 1\n\t"
        "v_permlane32_swap_b32 v64, v65\n\t"
        "v_add_f32 v64, v64, v65\n\t"
        "v_mul_f32 v64, 0xbb000000, v64\n\t"
        "v_mov_b32 v65, v64\n\t"
        "v_pk_add_f32 v[56:57], v[56:57], v[64:65]\n\t"
        "v_pk_add_f32 v[58:59], v[58:59], v[64:65]\n\t"
        "v_pk_add_f32 v[60:61], v[60:61], v[64:65]\n\t"
        "v_pk_add_f32 v[62:63], v[62:63], v[64:65]\n\t"
        "v_pk_mul_f32 v[64:65], v[56:57], v[56:57]\n\t"
        "v_pk_fma_f32 v[64:65], v[58:59], v[58:59], v[64:65]\n\t"
        "v_pk_fma_f32 v[64:65], v[60:61], v[60:61], v[64:65]\n\t"
        "v_pk_fma_f32 v[64:65], v[62:63], v[62:63], v[64:65]\n\t"
        "v_add_f32 v64, v64, v65\n\t"
        "s_nop 1\n\t"
        "v_add_f32_dpp v64, v64, v64 quad_perm:[1,0,3,2] row_mask:0xf bank_mask:0xf\n\t"
        "v_mov_b32 v66, 0x3727c5ac\n\t"
        "s_nop 0\n\t"
        "v_add_f32_dpp v64, v64, v64 quad_perm:[2,3,0,1] row_mask:0xf bank_mask:0xf\n\t"
        "s_nop 1\n\t"
        "v_add_f32_dpp v64, v64, v64 row_half_mirror row_mask:0xf bank_mask:0xf\n\t"
        "s_nop 1\n\t"
        "v_add_f32_dpp v64, v64, v64 row_mirror row_mask:0xf bank_mask:0xf\n\t"
        "v_mov_b32 v65, v64\n\t"
        "s_nop 1\n\t"
        "v_permlane16_swap_b32 v64, v65\n\t"
        "v_add_f32 v64, v64, v65\n\t"
        "v_mov_b32 v65, v64\n\t"
        "s_nop 1\n\t"
        "v_permlane32_swap_b32 v64, v65\n\t"
        "v_add_f32 v64, v64, v65\n\t"
        "v_fmac_f32 v66, 0x3b000000, v64\n\t"
        "v_rsq_f32 v64, v66\n\t"
        "s_nop 0\n\t"
        "v_mov_b32 v65, v64\n\t"
        "v_pk_mul_f32 v[56:57], v[56:57], v[64:65]\n\t"
        "v_pk_mul_f32 v[58:59], v[58:59], v[64:65]\n\t"
        "v_pk_mul_f32 v[60:61], v[60:61], v[64:65]\n\t"
        "v_pk_mul_f32 v[62:63], v[62:63], v[64:65]\n\t"
        "v_cvt_pk_bf16_f32 v72, v56, v57\n\t"
        "v_cvt_pk_bf16_f32 v73, v58, v59\n\t"
        "v_cvt_pk_bf16_f32 v74, v60, v61\n\t"
        "v_cvt_pk_bf16_f32 v75, v62, v63\n\t"
        "buffer_store_dwordx4 v[72:75], v35, s[40:43], s54 offen offset:0 sc1\n\t"
        "s_add_u32 s67, s54, 65536\n\t"
        "s_xor_b32 s54, s54, s67\n\t"
        "s_lshr_b32 s54, s54, 8\n\t"
        "s_and_b32 s54, s54, 0x2000\n\t"
        "s_xor_b32 s54, s54, s67\n\t"
        "s_xor_b32 s55, s55, 32768\n\t"
        "s_waitcnt vmcnt(0)\n\t"
        "s_memrealtime s[88:89]\n\t"
        "s_barrier\n\t"
        "s_waitcnt lgkmcnt(0)\n\t"
        "s_and_b32 s88, s88, 0xffffff\n\t"
        "s_or_b32 s88, s88, s51\n\t"
        "v_mov_b32 v37, s88\n\t"
        "s_mov_b32 exec_hi, 0\n\t"
        "buffer_store_dword v37, v36, s[40:43], s55 offen offset:0 sc1\n\t"
        "s_mov_b64 exec, -1\n\t"
        "s_add_u32 s51, s51, 16777216\n\t"
        "s_cmp_lt_u32 s51, 1090519040\n\t"
        "s_cbranch_scc1 Ltask_%=\n\t"
        "s_endpgm\n\t"
            RR_ARGS(2));
            __builtin_unreachable();
        } else {
            const unsigned pout = row * 1024, pfo = w == 0 ? O_FE + cu * 4 : OOB;
            asm volatile(
        "s_mov_b64 s[40:41], %[pw]\n\t"
        "s_mov_b32 s42, 184549376\n\t"
        "s_mov_b32 s43, 0x20000\n\t"
        "s_mov_b32 s48, %[w]\n\t"
        "s_mov_b32 s52, %[fi]\n\t"
        "s_mov_b32 s53, %[in]\n\t"
        "s_mov_b32 s54, %[out]\n\t"
        "s_mov_b32 s55, %[fo]\n\t"
        "s_mov_b32 s56, %[db]\n\t"
        "s_mov_b32 s75, %[fs]\n\t"
        "s_memrealtime s[72:73]\n\t"
        "s_mov_b32 s49, 131072\n\t"
        "s_mov_b32 s51, 16777216\n\t"
        "s_waitcnt lgkmcnt(0)\n\t"
        "s_mov_b32 s74, s72\n\t"
        "s_mov_b32 s73, 1480\n\t"
        "v_mbcnt_lo_u32_b32 v38, -1, 0\n\t"
        "v_mbcnt_hi_u32_b32 v38, -1, v38\n\t"
        "v_lshlrev_b32 v32, 4, v38\n\t"
        "v_mul_u32_u24 v36, 128, v38\n\t"
        "v_mov_b32 v33, %[fa]\n\t"
        "v_mov_b32 v35, %[sv]\n\t"
        "s_mov_b64 s[44:45], %[px]\n\t"
        "s_mov_b32 s46, 2097152\n\t"
        "s_mov_b32 s47, 0x20000\n\t"
        "s_mov_b32 s64, %[res]\n\t"
        "s_mov_b32 s65, %[xo]\n\t"
        "v_mov_b32 v40, %[s0]\n\t"
        "v_mov_b32 v41, %[s1]\n\t"
        "v_mov_b32 v42, %[s0]\n\t"
        "v_mov_b32 v43, %[s1]\n\t"
        "v_mov_b32 v44, %[idx]\n\t"
        "v_mov_b32 v45, %[lo]\n\t"
        "v_mov_b32 v46, %[lol]\n\t"
        "v_lshrrev_b32 v47, 1, v32\n\t"
        "v_mov_b32 v48, %[b0]\n\t"
        "v_mov_b32 v49, %[b1]\n\t"
        "v_mov_b32 v50, %[b2]\n\t"
        "v_mov_b32 v51, %[b3]\n\t"
        "v_mov_b32 v52, %[b4]\n\t"
        "v_mov_b32 v53, %[b5]\n\t"
        "v_mov_b32 v54, %[b6]\n\t"
        "v_mov_b32 v55, %[b7]\n\t"
        "v_mov_b32 v34, -1\n\t"
        "s_cmp_lg_u32 s48, 0\n\t"
        "s_cbranch_scc1 Ltask_%=\n\t"
        "v_mov_b32 v34, 0\n\t"
        "Ltask_%=:\n\t"
        "s_mov_b32 s50, 0\n\t"
        "v_cmp_gt_u32 vcc, s51, v34\n\t"
        "s_cbranch_vccz Lok_%=\n\t"
        "s_cmp_eq_u32 s51, s75\n\t"
        "s_cbranch_scc1 Lp_%=\n\t"
        "s_branch Lw1_%=\n\t"
        "Lw0_%=:\n\t"
        "s_sleep 1\n\t"
        "Lw1_%=:\n\t"
        "s_memrealtime s[62:63]\n\t"
        "s_waitcnt lgkmcnt(0)\n\t"
        "s_sub_u32 s63, s74, s62\n\t"
        "s_cmp_lt_u32 s63, 600\n\t"
        "s_cbranch_scc1 Lw0_%=\n\t"
        "Lp_%=:\n\t"
        "buffer_load_dword v34, v33, s[40:43], s52 offen sc1\n\t"
        "s_waitcnt vmcnt(0)\n\t"
        "v_cmp_gt_u32 vcc, s51, v34\n\t"
        "s_cbranch_vccz Lok_%=\n\t"
        "s_add_u32 s50, s50, 1\n\t"
        "s_cmp_eq_u32 s51, s75\n\t"
        "s_cbranch_scc1 Lns_%=\n\t"
        "s_sleep 3\n\t"
        "s_cmp_lt_u32 s50, 3\n\t"
        "s_cbranch_scc1 Lp_%=\n\t"
        "s_sleep 12\n\t"
        "Lns_%=:\n\t"
        "s_cmp_lt_u32 s50, s49\n\t"
        "s_cbranch_scc1 Lp_%=\n\t"
        "s_mov_b32 s49, 0\n\t"
        "Lok_%=:\n\t"
        "s_barrier\n\t"
        "buffer_load_dwordx4 v[56:59], v32, s[40:43], s64 offen offset:0 sc1\n\t"
        "buffer_load_dwordx4 v[60:63], v32, s[40:43], s64 offen offset:1024 sc1\n\t"
        "s_add_u32 s57, s53, 4096\n\t"
        "s_add_u32 s58, s53, 16384\n\t"
        "buffer_load_dwordx4 v[80:83], v45, s[40:43], s53 offen offset:0 sc1\n\t"
        "buffer_load_dwordx4 v[84:87], v45, s[40:43], s53 offen offset:2048 sc1\n\t"
        "buffer_load_dwordx4 v[88:91], v45, s[40:43], s53 offen offset:512 sc1\n\t"
        "buffer_load_dwordx4 v[92:95], v45, s[40:43], s53 offen offset:2560 sc1\n\t"
        "buffer_load_dwordx4 v[96:99], v45, s[40:43], s57 offen offset:0 sc1\n\t"
        "buffer_load_dwordx4 v[100:103], v45, s[40:43], s57 offen offset:2048 sc1\n\t"
        "buffer_load_dwordx4 v[104:107], v45, s[40:43], s57 offen offset:512 sc1\n\t"
        "buffer_load_dwordx4 v[108:111], v45, s[40:43], s57 offen offset:2560 sc1\n\t"
        "buffer_load_dwordx4 v[112:115], v45, s[40:43], s58 offen offset:0 sc1\n\t"
        "buffer_load_dwordx4 v[116:119], v46, s[40:43], s58 offen offset:2048 sc1\n\t"
        "buffer_load_dwordx4 v[120:123], v45, s[40:43], s58 offen offset:512 sc1\n\t"
        "buffer_load_dwordx4 v[124:127], v46, s[40:43], s58 offen offset:2560 sc1\n\t"
        "s_add_u32 s67, s53, 1048576\n\t"
        "s_xor_b32 s53, s53, s67\n\t"
        "s_lshr_b32 s53, s53, 8\n\t"
        "s_and_b32 s53, s53, 0x2000\n\t"
        "s_xor_b32 s53, s53, s67\n\t"
        "v_subrev_u32 v38, s72, v34\n\t"
        "v_subrev_u32 v39, s51, v34\n\t"
        "s_lshr_b32 s74, s73, 3\n\t"
        "v_bfe_i32 v38, v38, 0, 24\n\t"
        "v_lshrrev_b32 v39, 24, v39\n\t"
        "v_mul_u32_u24 v39, s74, v39\n\t"
        "v_sub_u32 v38, v38, v39\n\t"
        "s_nop 1\n\t"
        "v_max_i32_dpp v38, v38, v38 row_ror:1 row_mask:0xf bank_mask:0xf\n\t"
        "s_nop 1\n\t"
        "v_max_i32_dpp v38, v38, v38 row_ror:2 row_mask:0xf bank_mask:0xf\n\t"
        "s_nop 1\n\t"
        "v_max_i32_dpp v38, v38, v38 row_ror:4 row_mask:0xf bank_mask:0xf\n\t"
        "s_nop 1\n\t"
        "v_max_i32_dpp v38, v38, v38 row_ror:8 row_mask:0xf bank_mask:0xf\n\t"
        "s_nop 0\n\t"
        "v_readfirstlane_b32 s62, v38\n\t"
        "s_nop 0\n\t"
        "s_add_u32 s72, s72, s62\n\t"
        "s_sub_i32 s62, s62, s74\n\t"
        "s_min_i32 s62, s62, 16\n\t"
        "s_max_i32 s62, s62, -16\n\t"
        "s_add_u32 s73, s73, s62\n\t"
        "s_add_u32 s74, s74, s72\n\t"
        "s_add_u32 s74, s74, 20\n\t"
        "s_add_u32 s67, s64, 131072\n\t"
        "s_xor_b32 s64, s64, s67\n\t"
        "s_lshr_b32 s64, s64, 8\n\t"
        "s_and_b32 s64, s64, 0x2000\n\t"
        "s_xor_b32 s64, s64, s67\n\t"
        "s_waitcnt vmcnt(13)\n\t"
        "s_waitcnt vmcnt(12)\n\t"
        "v_pk_add_f32 v[56:57], v[56:57], v[48:49]\n\t"
        "v_pk_add_f32 v[58:59], v[58:59], v[50:51]\n\t"
        "v_pk_add_f32 v[60:61], v[60:61], v[52:53]\n\t"
        "v_pk_add_f32 v[62:63], v[62:63], v[54:55]\n\t"
        "s_waitcnt vmcnt(10)\n\t"
        "v_smfmac_f32_16x16x64_bf16 v[56:59], v[40:43], v[80:87], v44\n\t"
        "s_waitcnt vmcnt(8)\n\t"
        "v_smfmac_f32_16x16x64_bf16 v[60:63], v[40:43], v[88:95], v44\n\t"
        "s_waitcnt vmcnt(6)\n\t"
        "v_smfmac_f32_16x16x64_bf16 v[56:59], v[40:43], v[96:103], v44\n\t"
        "s_waitcnt vmcnt(4)\n\t"
        "v_smfmac_f32_16x16x64_bf16 v[60:63], v[40:43], v[104:111], v44\n\t"
        "s_waitcnt vmcnt(2)\n\t"
        "v_smfmac_f32_16x16x64_bf16 v[56:59], v[40:43], v[112:119], v44\n\t"
        "s_waitcnt vmcnt(0)\n\t"
        "v_smfmac_f32_16x16x64_bf16 v[60:63], v[40:43], v[120:127], v44\n\t"
        "s_nop 5\n\t"
        "v_cvt_pk_bf16_f32 v72, v56, v57\n\t"
        "v_cvt_pk_bf16_f32 v73, v58, v59\n\t"
        "v_cvt_pk_bf16_f32 v74, v60, v61\n\t"
        "v_cvt_pk_bf16_f32 v75, v62, v63\n\t"
        "buffer_store_dwordx4 v[72:75], v35, s[44:47], s54 offen sc1\n\t"
        "s_add_u32 s54, s54, 32768\n\t"
        "s_cmp_gt_u32 s51, 50331648\n\t"
        "s_cselect_b32 s55, 0x7ff00000, s55\n\t"
        "s_waitcnt vmcnt(0)\n\t"
        "s_memrealtime s[88:89]\n\t"
        "s_barrier\n\t"
        "s_waitcnt lgkmcnt(0)\n\t"
        "s_and_b32 s88, s88, 0xffffff\n\t"
        "s_or_b32 s88, s88, s51\n\t"
        "v_mov_b32 v37, s88\n\t"
        "s_mov_b64 exec, 0x1\n\t"
        "buffer_store_dword v37, v36, s[40:43], s55 offen offset:0 sc1\n\t"
        "s_mov_b64 exec, -1\n\t"
        "s_add_u32 s51, s51, 16777216\n\t"
        "s_cmp_lt_u32 s51, 1090519040\n\t"
        "s_cbranch_scc1 Ltask_%=\n\t"
        "s_endpgm\n\t"
            RR_ARGS(2));
            __builtin_unreachable();
        }
    }
}

__global__ void __launch_bounds__(256, 1) mega(const u16* __restrict__ x, const u16* __restrict__ mem0, const u16* __restrict__ Wq, const u16* __restrict__ Wk, const u16* __restrict__ Wv,
                                               const u16* __restrict__ Wo, const u16* __restrict__ w1, const u16* __restrict__ b1, const u16* __restrict__ w2,
                                               const u16* __restrict__ b2, unsigned char* __restrict__ ws, u16* __restrict__ out) {
    __shared__ __attribute__((aligned(16))) unsigned char lds_[163840];
    asm volatile("" ::"v"((LDSP unsigned char*)lds_) : "memory");
    unsigned cp = (blockIdx.x >> 5) == 4 ? ((blockIdx.x & 8) * 32 + threadIdx.x) * 128 : 0;
    if (CPF) {
        unsigned long long pc;
        asm volatile(".Lmk_b:\n\ts_getpc_b64 %1\n\tv_min_u32 %0, .Lmk_e-.Lmk_b-8, %0\n\tglobal_load_dword %0, %0, %1" : "+v"(cp), "=&s"(pc));
    }
    asm volatile("" ::"s"(x), "s"(Wv), "s"(ws));
    const int b = blockIdx.x, lane = threadIdx.x & 63, w = __builtin_amdgcn_readfirstlane(threadIdx.x >> 6);
    const int l = (b >> 2) & 1, xq = b & 3, i = b >> 3, r = (i - 24) * 4 + xq;
    ws = (unsigned char*)(((uintptr_t)ws + 0x1fffff) & ~(uintptr_t)0x1fffff);
    const rsrc_t rs = __builtin_amdgcn_make_buffer_rsrc(ws, 0, WS_NEED, 0x20000);
    const int id = i < 16 ? (8 * xq + (i >> 1)) | l << 5 | (i & 1) << 6 | w << 7 : b * 4 + w;
    if (r >= 27) asm volatile("s_waitcnt vmcnt(0)" ::"v"(cp));
    if (r == 31 && l) { master_role(rs, (LDSP unsigned*)lds_, w, lane); return; }
    unsigned gc;
    const int hp = (1 - l) * 16 + r - 11;
    if (i < 16 && !l) {
        const int row = w * 8 + (lane >> 3), g = lane >> 4;
        v4u v[8];
        unsigned g0, t;
        asm volatile(
        "buffer_load_dword %[g0], off, %[rs], %[gd] sc1\n\t"
        "global_load_dwordx4 %0, %[xo], %[px] offset:0\n\t"
        "global_load_dwordx4 %1, %[xo], %[px] offset:128\n\t"
        "global_load_dwordx4 %2, %[xo], %[px] offset:256\n\t"
        "global_load_dwordx4 %3, %[xo], %[px] offset:384\n\t"
        "global_load_dwordx4 %4, %[xo], %[px] offset:512\n\t"
        "global_load_dwordx4 %5, %[xo], %[px] offset:640\n\t"
        "global_load_dwordx4 %6, %[xo], %[px] offset:768\n\t"
        "global_load_dwordx4 %7, %[xo], %[px] offset:896\n\t"
        "s_waitcnt vmcnt(0)\n\t"
            : "=&v"(v[0]), "=&v"(v[1]), "=&v"(v[2]), "=&v"(v[3]), "=&v"(v[4]), "=&v"(v[5]), "=&v"(v[6]), "=&v"(v[7]), [g0] "=&v"(g0), [t] "=&s"(t)
            : [xo] "v"(((i & 1) * 32 + row) * 1024 + (lane & 7) * 16), [px] "s"(x), [rs] "s"(rs), [gd] "s"(O_HR + id * 4), [rv] "s"(__builtin_amdgcn_make_buffer_rsrc((void*)Wv, 0, 4 << 20, 0x20000)),
              [ga] "v"(g * 8192 + (((g * 2 | (w & 1) * 8) ^ lane) & 15) * 16), [wb] "s"((id & 31) * 256 + w * 65536), [ld] "s"(32768 + w * 2048)
            : "memory", "scc");
        gc = (__builtin_amdgcn_readfirstlane(g0) + 2) & ~1u;
        st4c(rs, lane ? OOB : 0, gc, O_HC + id * 4);
        ln8(v);
#pragma unroll
        for (int k = 0; k < 8; ++k)
            lds_wr16((LDSP unsigned char*)lds_ + k * 4096 + (row & 15) * 256 + ((((row >> 4) * 8 + (lane & 7)) ^ (row & 15)) * 16), v[k]);
    } else {
    const unsigned g0 = ld4c(rs, 0, O_HR + id * 4);
    if (r >= 27 && r < 31) { ln_role(rs, (LDSP unsigned*)lds_, x, l * 4 + r - 27, w, lane, id, g0); return; }
    if (l ? i < 16 : i >= 16) {
        const int wv = ((i < 31 ? i : 30) & 15) * 4 + w;
        constexpr int LG = PFG == 64 ? 5 : 4, NU = 32 << LG;
        unsigned tmp = 0;
        const unsigned o1 = PFG == 64 ? (lane >> 5) * 8192 + (lane & 31) * 64 : (lane >> 4) * 8192 + (lane & 15) * 128;
        const unsigned o3 = PFG == 64 ? ((lane >> 2) & 7) * 1024 + (lane >> 5) * 16384 + (lane & 3) * 64 : ((lane >> 1) & 7) * 1024 + ((lane >> 4) & 1) * 16384 + (lane & 1) * 128 + (lane >> 5) * 131072;
#pragma unroll 1
        for (int u = (l ? PFM << LG : PFS) + wv; u < NU; u += 60 + l * 4) {
            const int bk = u >> LG;
            const unsigned long long tb = bk < 16 ? (bk < 8 ? 0x1a14131912111810ull : 0x1d0a021c171b1615ull) : (bk < 24 ? 0xd051f0c041e0b03ull : 0x90108000f070e06ull);
            const int cd = (int)(tb >> ((bk & 7) * 8)) & 31, mat = cd >> 3, k2 = cd & 7, j = u & ((1 << LG) - 1), jw = j & 3;
            const u16* p = mat == 3 ? Wo + (xq * 8 + (j >> 2) * (6 - LG)) * 65536 + jw * 128 + ((k2 >> 1) ^ jw) * 16384 + (k2 & 1) * 4096
                                    : (mat == 2 ? Wv : (mat == 0 ? Wq : Wk)) + (k2 * 64 + (j << (6 - LG))) * 4096 + xq * 1024;
            asm volatile("global_load_dword %0, %1, %2" PFNT : "+v"(tmp) : "v"(mat == 3 ? o3 : o1), "s"(p));
        }
        if (TLP && w < 2) asm volatile("buffer_load_dword %0, %1, %2, 0 offen sc1" : "+v"(tmp) : "v"((threadIdx.x << 21) + 0x1fff80), "s"(rs));
        asm volatile("s_waitcnt vmcnt(0)" : "+v"(tmp));
    }
    if (r == 31) return;
    if (r >= 11 && l) arm(rs, mem0, hp * 4 + w, lane);
    gc = (__builtin_amdgcn_readfirstlane(g0) + 2) & ~1u;
    st4c(rs, lane ? OOB : 0, gc, O_HC + id * 4);
    }
    asm volatile("" ::"v"(cp), "s"(gc));
    if (i < 16) head_role(ws, Wq, Wk, Wv, Wo, id, gc);
    else if (i < 20) wo_role(ws, Wo, l, xq * 2 + ((i >> 1) & 1), i & 1, w, lane, id, gc);
    else if (i < 24) mr_role(ws, l, (xq * 4 + i - 20) * 4 + w, lane, id, gc);
    else if (r < 11) mlp_role(ws, w1, w2, b1, l, r, w, lane, id, gc);
    else {
        const unsigned lp = gate(rs, (LDSP unsigned*)lds_, id, gc, hp, w, lane) ^ l << 13;
        rr_role(ws, x, b2, out, r < 19 ? 1 : 2, l, lp, (r - 11) & 7, w, lane);
    }
}
}

extern "C" void kernel_launch(void* const* d_in, const int* in_sizes, int n_in, void* d_out, int out_size, void* d_ws, size_t ws_size, hipStream_t stream) {
    using namespace mk;
    if (MPN) (void)hipMemsetAsync((char*)d_ws + O_WOP, 0xff, 32u << 20, stream);
    mega<<<256, 256, 0, stream>>>((const u16*)d_in[0], (const u16*)d_in[1], (const u16*)d_in[2], (const u16*)d_in[3], (const u16*)d_in[4], (const u16*)d_in[5], (const u16*)d_in[8],
                                  (const u16*)d_in[9], (const u16*)d_in[10], (const u16*)d_in[11], (unsigned char*)d_ws, (u16*)d_out);
}
```
